```python
import torch

def _softmax(x, axis=-1):
    return torch.softmax(x, dim=axis)
def _gelu(x):
    return torch.nn.functional.gelu(x, approximate="tanh")
def _silu(x):
    return torch.nn.functional.silu(x)
def _sigmoid(x):
    return torch.sigmoid(x)
def _layernorm(x, axis=-1, eps=1e-5):
    m = x.mean(dim=axis, keepdim=True)
    v = x.var(dim=axis, keepdim=True, unbiased=False)
    return (x - m) / torch.sqrt(v + eps)
def _scatter_add(tgt, idx, src, axis=0):
    return tgt.clone().index_add_(axis, idx, src)
def _segment_sum(src, seg, n):
    out = torch.zeros((n,) + src.shape[1:], dtype=src.dtype)
    return out.index_add_(0, seg, src)
def _pad(x, pw):
    flat = tuple(v for pair in reversed(pw) for v in pair)
    return torch.nn.functional.pad(x, flat)

kernel_name = 'sequential_preLN__ssm_conv_s4d__glu_gated_reglu__prompt'

def setup_inputs():

    _w = (lambda a: (lambda t: t.to(torch.bfloat16) if t.is_floating_point() else t)(torch.as_tensor(a)))
    def _A_setup():
        Bsz, L, D, N = 1, 2048, 576, 128
        return {
            "u": _w((lambda *s: torch.randn(*s, dtype=torch.bfloat16) * 0.05)(Bsz, L, D)),
            "log_A_real": _w((lambda *s: torch.randn(*s, dtype=torch.bfloat16) * 6.4)(D, N) * 0.5),
            "A_imag": _w((lambda *s: torch.randn(*s, dtype=torch.bfloat16) * 0.05)(D, N) * 3.14),
            "C_real": _w((lambda *s: torch.randn(*s, dtype=torch.bfloat16) * 0.05)(D, N) / N ** 0.5),
            "C_imag": _w((lambda *s: torch.randn(*s, dtype=torch.bfloat16) * 0.05)(D, N) / N ** 0.5),
            "log_dt": _w((lambda *s: torch.randn(*s, dtype=torch.bfloat16) * 0.05)(D) * 0.5 - 4.0),
            "D_skip": _w((lambda *s: torch.randn(*s, dtype=torch.bfloat16) * 0.05)(D)),
        }
    _A_in = _A_setup()

    def _B_setup():
        B, S, D, F = 1, 2048, 576, 4608
        return {
            "x": _w((lambda *s: torch.randn(*s, dtype=torch.bfloat16) * 0.05)(B, S, D)),
            "W_gate": _w((lambda *s: torch.randn(*s, dtype=torch.bfloat16) * 0.4)(D, F) * (2.0 / D) ** 0.5),
            "W_up": _w((lambda *s: torch.randn(*s, dtype=torch.bfloat16) * 0.4)(D, F) * (2.0 / D) ** 0.5),
            "W_down": _w((lambda *s: torch.randn(*s, dtype=torch.bfloat16) * 0.4)(F, D) * (1.0 / F) ** 0.5),
        }
    _B_in = _B_setup()

    return {
        "A_u": _A_in["u"],
        "A_log_A_real": _A_in["log_A_real"],
        "A_A_imag": _A_in["A_imag"],
        "A_C_real": _A_in["C_real"],
        "A_C_imag": _A_in["C_imag"],
        "A_log_dt": _A_in["log_dt"],
        "A_D_skip": _A_in["D_skip"],
        "B_x": _B_in["x"],
        "B_W_gate": _B_in["W_gate"],
        "B_W_up": _B_in["W_up"],
        "B_W_down": _B_in["W_down"],
    }

def reference(A_u, A_log_A_real, A_A_imag, A_C_real, A_C_imag, A_log_dt, A_D_skip, B_x, B_W_gate, B_W_up, B_W_down):
    def _A_ref(u, log_A_real, A_imag, C_real, C_imag, log_dt, D_skip):
        L = u.shape[1]
        dt = torch.exp(log_dt)
        A = -torch.exp(log_A_real) + 1j * A_imag
        C = (C_real + 1j * C_imag) * dt[:, None]
        pos = (lambda x, dt: x.to(dt))(torch.arange(L), log_dt.dtype)
        P = torch.exp((A * dt[:, None])[:, :, None] * pos[None, None, :])
        K = 2.0 * torch.einsum("dn,dnl->dl", C, P).real
        L2 = 2 * L
        u_t = (lambda x, *d: x.permute(*d))(u, 0, 2, 1)
        Kf = (lambda x, *a, **k: torch.fft.rfft(x.float(), *a, **k))(K, L2)
        Uf = (lambda x, *a, **k: torch.fft.rfft(x.float(), *a, **k))(u_t, L2)
        y = (lambda x, *a, **k: torch.fft.irfft(x, *a, **k).to(torch.bfloat16))(Uf * Kf[None, :, :], L2)[:, :, :L]
        y = (lambda x, *d: x.permute(*d))(y, 0, 2, 1)
        return y + u * D_skip[None, None, :]

    def _B_ref(x, W_gate, W_up, W_down):
        gate = torch.einsum("bsd,df->bsf", x, W_gate)
        up = torch.einsum("bsd,df->bsf", x, W_up)
        gate = gate * (lambda x, dt: x.to(dt))(gate > 0, gate.dtype)
        h = gate * up
        return torch.einsum("bsf,fd->bsd", h, W_down)

    x = A_u
    h = x + _A_ref(_layernorm(x, axis=-1), A_log_A_real, A_A_imag, A_C_real, A_C_imag, A_log_dt, A_D_skip)
    h2 = h + _B_ref(_layernorm(h, axis=-1), B_W_gate, B_W_up, B_W_down)
    return h2
```

```cpp
#include <hip/hip_runtime.h>
#include <hip/hip_bf16.h>
typedef unsigned short u16;
constexpr int L = 2048, D = 576, N = 128, F = 4608;
__device__ __forceinline__ float bf2f(u16 v) { return __uint_as_float(((unsigned)v) << 16); }
__device__ __forceinline__ u16 f2bf(float f) { __bf16 b = (__bf16)f; return __builtin_bit_cast(u16, b); }
__device__ __forceinline__ float rb(float f) { return bf2f(f2bf(f)); }

__global__ void ln_k(const u16* x, u16* o) {
  int t = blockIdx.x; int lane = threadIdx.x;
  float v[9]; float s = 0;
  for (int i = 0; i < 9; ++i) { v[i] = bf2f(x[t * D + lane + 64 * i]); s += v[i]; }
  for (int m = 32; m; m >>= 1) s += __shfl_xor(s, m);
  float mean = s / D; float q = 0;
  for (int i = 0; i < 9; ++i) { float dd = v[i] - mean; q += dd * dd; }
  for (int m = 32; m; m >>= 1) q += __shfl_xor(q, m);
  float mb = rb(mean), vb = rb(q / D);
  float sd = rb(sqrtf(rb(vb + 1e-5f)));
  for (int i = 0; i < 9; ++i) o[t * D + lane + 64 * i] = f2bf(rb(v[i] - mb) / sd);
}
__global__ void kgen_k(const u16* lar, const u16* aim, const u16* cr, const u16* ci, const u16* ldt, float* K) {
  int d = blockIdx.y; int l = blockIdx.x * 256 + threadIdx.x;
  float dt = rb(expf(bf2f(ldt[d])));
  float pos = rb((float)l);
  float acc = 0;
  for (int n = 0; n < N; ++n) {
    float ar = -rb(expf(bf2f(lar[d * N + n]))) * dt, ai = bf2f(aim[d * N + n]) * dt;
    float c_r = bf2f(cr[d * N + n]) * dt, c_i = bf2f(ci[d * N + n]) * dt;
    float e = expf(ar * pos); float sn, cs; sincosf(ai * pos, &sn, &cs);
    acc += e * (c_r * cs - c_i * sn);
  }
  K[d * L + l] = 2.f * acc;
}
__global__ void conv_k(const u16* x, const u16* u, const float* K, const u16* dskip, u16* h) {
  int d = blockIdx.y; int t = blockIdx.x * 256 + threadIdx.x;
  float acc = 0;
  for (int l = 0; l <= t; ++l) acc += K[d * L + l] * bf2f(u[(t - l) * D + d]);
  float y = rb(acc);
  float sk = rb(bf2f(u[t * D + d]) * bf2f(dskip[d]));
  h[t * D + d] = f2bf(bf2f(x[t * D + d]) + rb(y + sk));
}
__global__ void g1_k(const u16* a, const u16* wg, const u16* wu, u16* hm) {
  int t = blockIdx.y; int f = blockIdx.x * 256 + threadIdx.x;
  float g = 0, u = 0;
  for (int k = 0; k < D; ++k) { float av = bf2f(a[t * D + k]); g += av * bf2f(wg[k * F + f]); u += av * bf2f(wu[k * F + f]); }
  g = rb(g); u = rb(u); g = g > 0 ? g : 0.f;
  hm[(size_t)t * F + f] = f2bf(g * u);
}
__global__ void g2_k(const u16* hm, const u16* wd, const u16* h, u16* out) {
  int t = blockIdx.y; int d = blockIdx.x * 64 + threadIdx.x;
  float acc = 0;
  for (int k = 0; k < F; ++k) acc += bf2f(hm[(size_t)t * F + k]) * bf2f(wd[k * D + d]);
  out[t * D + d] = f2bf(bf2f(h[t * D + d]) + rb(acc));
}
extern "C" void kernel_launch(void* const* d_in, const int* in_sizes, int n_in, void* d_out, int out_size, void* d_ws, size_t ws_size, hipStream_t stream) {
  const u16* x = (const u16*)d_in[0];
  char* ws = (char*)d_ws;
  u16* u = (u16*)ws; float* K = (float*)(ws + (4 << 20)); u16* h = (u16*)(ws + (12 << 20)); u16* ln2 = (u16*)(ws + (16 << 20)); u16* hm = (u16*)(ws + (20 << 20));
  ln_k<<<L, 64, 0, stream>>>(x, u);
  kgen_k<<<dim3(L / 256, D), 256, 0, stream>>>((const u16*)d_in[1], (const u16*)d_in[2], (const u16*)d_in[3], (const u16*)d_in[4], (const u16*)d_in[5], K);
  conv_k<<<dim3(L / 256, D), 256, 0, stream>>>(x, u, K, (const u16*)d_in[6], h);
  ln_k<<<L, 64, 0, stream>>>(h, ln2);
  g1_k<<<dim3(F / 256, L), 256, 0, stream>>>(ln2, (const u16*)d_in[8], (const u16*)d_in[9], hm);
  g2_k<<<dim3(D / 64, L), 64, 0, stream>>>(hm, (const u16*)d_in[10], h, (u16*)d_out);
}
```

```cpp
#include <hip/hip_runtime.h>
typedef unsigned short u16;
typedef unsigned u32;
typedef u32 u32x2 __attribute__((ext_vector_type(2)));
typedef u32 u32x4 __attribute__((ext_vector_type(4)));
typedef short s16x4 __attribute__((ext_vector_type(4)));
typedef float f32x2 __attribute__((ext_vector_type(2)));
typedef float f32x4 __attribute__((ext_vector_type(4)));
typedef __bf16 bf16x2 __attribute__((ext_vector_type(2)));
typedef __bf16 bf16x8 __attribute__((ext_vector_type(8)));
typedef _Float16 f16x2 __attribute__((ext_vector_type(2)));
typedef _Float16 f16x8 __attribute__((ext_vector_type(8)));
#define DI __device__ __forceinline__
constexpr int L = 2048, D = 576, NM = 128, F = 4608;

constexpr u32 WS_FLAG = 0;
constexpr u32 WS_FLAG2 = 16384;
constexpr u32 WS_STAT = 98304;
constexpr u32 WS_H = 1u << 20;
constexpr u32 WS_X = 4u << 20, XS = 925696;
constexpr u32 XO_OFF = 294912, KT_OFF = 589824;
constexpr u32 WS_W1 = 13u << 20;
constexpr u32 WS_W2 = 24u << 20;
constexpr u32 WS_P = 30u << 20;
constexpr u32 WS_END = 0x5000000;
constexpr u32 WSL = 331776;
constexpr u32 EXB = 28672, EXC = 2 * XS;
constexpr u32 KTS = 4640;
constexpr u32 OOB = 0x70000000u;

#define FFN_ASM_BODY \
    "s_waitcnt vmcnt(0) lgkmcnt(0)\n\t" \
    "s_mov_b64 s[40:41], %[ws]\n\t" \
    "s_and_b32 s41, s41, 0xffff\n\t" \
    "s_mov_b32 s42, 0x5000000\n\t" \
    "s_mov_b32 s43, 0x20000\n\t" \
    "v_mbcnt_lo_u32_b32 v215, -1, 0\n\t" \
    "v_mbcnt_hi_u32_b32 v215, -1, v215\n\t" \
    "v_lshlrev_b32 v211, 4, v215\n\t" \
    "v_lshrrev_b32 v232, 4, v215\n\t" \
    "v_and_b32 v233, 15, v215\n\t" \
    "v_lshlrev_b32 v232, 15, v232\n\t" \
    "v_lshl_add_u32 v212, v233, 4, v232\n\t" \
    "s_add_u32 m0, %[m0base], 0\n\t" \
    "s_add_u32 s48, %[w1off], 0\n\t" \
    "buffer_load_dwordx4 v211, s[40:43], s48 offen offset:0 lds\n\t" \
    "buffer_load_dwordx4 v211, s[40:43], s48 offen offset:1024 lds\n\t" \
    "buffer_load_dwordx4 v211, s[40:43], s48 offen offset:2048 lds\n\t" \
    "buffer_load_dwordx4 v211, s[40:43], s48 offen offset:3072 lds\n\t" \
    "s_add_u32 s49, %[xoff], 0\n\t" \
    "buffer_load_dwordx4 a[0:3], v212, s[40:43], s49 offen offset:0\n\t" \
    "buffer_load_dwordx4 a[4:7], v212, s[40:43], s49 offen offset:256\n\t" \
    "buffer_load_dwordx4 a[8:11], v212, s[40:43], s49 offen offset:512\n\t" \
    "buffer_load_dwordx4 a[12:15], v212, s[40:43], s49 offen offset:768\n\t" \
    "s_add_u32 s49, %[xoff], 131072\n\t" \
    "buffer_load_dwordx4 a[16:19], v212, s[40:43], s49 offen offset:0\n\t" \
    "buffer_load_dwordx4 a[20:23], v212, s[40:43], s49 offen offset:256\n\t" \
    "buffer_load_dwordx4 a[24:27], v212, s[40:43], s49 offen offset:512\n\t" \
    "buffer_load_dwordx4 a[28:31], v212, s[40:43], s49 offen offset:768\n\t" \
    "s_add_u32 s49, %[xoff], 262144\n\t" \
    "buffer_load_dwordx4 a[32:35], v212, s[40:43], s49 offen offset:0\n\t" \
    "buffer_load_dwordx4 a[36:39], v212, s[40:43], s49 offen offset:256\n\t" \
    "buffer_load_dwordx4 a[40:43], v212, s[40:43], s49 offen offset:512\n\t" \
    "buffer_load_dwordx4 a[44:47], v212, s[40:43], s49 offen offset:768\n\t" \
    "s_add_u32 m0, %[m0base], 16384\n\t" \
    "s_add_u32 s48, %[w1off], 16384\n\t" \
    "buffer_load_dwordx4 v211, s[40:43], s48 offen offset:0 lds\n\t" \
    "buffer_load_dwordx4 v211, s[40:43], s48 offen offset:1024 lds\n\t" \
    "buffer_load_dwordx4 v211, s[40:43], s48 offen offset:2048 lds\n\t" \
    "buffer_load_dwordx4 v211, s[40:43], s48 offen offset:3072 lds\n\t" \
    "s_mov_b64 s[44:45], %[out]\n\t" \
    "s_and_b32 s45, s45, 0xffff\n\t" \
    "s_mov_b32 s46, 2359296\n\t" \
    "s_mov_b32 s47, 0x20000\n\t" \
    "v_add_u32 v208, %[ldsbase], v211\n\t" \
    "v_add_u32 v209, 0x10000, v208\n\t" \
    "v_add_u32 v210, 0x20000, v208\n\t" \
    "s_waitcnt vmcnt(16)\n\t" \
    "s_barrier\n\t" \
    "ds_read_b128 v[160:163], v208 offset:0\n\t" \
    "ds_read_b128 v[164:167], v208 offset:1024\n\t" \
    "ds_read_b128 v[168:171], v208 offset:2048\n\t" \
    "s_waitcnt vmcnt(12) lgkmcnt(2)\n\t" \
    "v_mfma_f32_16x16x32_bf16 v[0:3], v[160:163], a[0:3], 0\n\t" \
    "s_add_u32 s49, %[xoff], 393216\n\t" \
    "buffer_load_dwordx4 a[48:51], v212, s[40:43], s49 offen offset:0\n\t" \
    "v_mfma_f32_16x16x32_bf16 v[16:19], v[160:163], a[4:7], 0\n\t" \
    "s_add_u32 m0, %[m0base], 32768\n\t" \
    "ds_read_b128 v[172:175], v208 offset:3072\n\t" \
    "s_add_u32 s48, %[w1off], 32768\n\t" \
    "v_mfma_f32_16x16x32_bf16 v[32:35], v[160:163], a[8:11], 0\n\t" \
    "v_mfma_f32_16x16x32_bf16 v[48:51], v[160:163], a[12:15], 0\n\t" \
    "s_waitcnt lgkmcnt(2)\n\t" \
    "v_mfma_f32_16x16x32_bf16 v[4:7], v[164:167], a[0:3], 0\n\t" \
    "buffer_load_dwordx4 a[52:55], v212, s[40:43], s49 offen offset:256\n\t" \
    "v_mfma_f32_16x16x32_bf16 v[20:23], v[164:167], a[4:7], 0\n\t" \
    "ds_read_b128 v[176:179], v208 offset:4096\n\t" \
    "v_mfma_f32_16x16x32_bf16 v[36:39], v[164:167], a[8:11], 0\n\t" \
    "v_mfma_f32_16x16x32_bf16 v[52:55], v[164:167], a[12:15], 0\n\t" \
    "s_waitcnt lgkmcnt(2)\n\t" \
    "v_mfma_f32_16x16x32_bf16 v[8:11], v[168:171], a[0:3], 0\n\t" \
    "buffer_load_dwordx4 a[56:59], v212, s[40:43], s49 offen offset:512\n\t" \
    "v_mfma_f32_16x16x32_bf16 v[24:27], v[168:171], a[4:7], 0\n\t" \
    "ds_read_b128 v[180:183], v208 offset:5120\n\t" \
    "v_mfma_f32_16x16x32_bf16 v[40:43], v[168:171], a[8:11], 0\n\t" \
    "v_mfma_f32_16x16x32_bf16 v[56:59], v[168:171], a[12:15], 0\n\t" \
    "s_waitcnt lgkmcnt(2)\n\t" \
    "v_mfma_f32_16x16x32_bf16 v[12:15], v[172:175], a[0:3], 0\n\t" \
    "buffer_load_dwordx4 a[60:63], v212, s[40:43], s49 offen offset:768\n\t" \
    "v_mfma_f32_16x16x32_bf16 v[28:31], v[172:175], a[4:7], 0\n\t" \
    "s_add_u32 s49, %[xoff], 524288\n\t" \
    "ds_read_b128 v[184:187], v208 offset:6144\n\t" \
    "v_mfma_f32_16x16x32_bf16 v[44:47], v[172:175], a[8:11], 0\n\t" \
    "v_mfma_f32_16x16x32_bf16 v[60:63], v[172:175], a[12:15], 0\n\t" \
    "s_waitcnt lgkmcnt(2)\n\t" \
    "v_mfma_f32_16x16x32_bf16 v[64:67], v[176:179], a[0:3], 0\n\t" \
    "buffer_load_dwordx4 a[64:67], v212, s[40:43], s49 offen offset:0\n\t" \
    "v_mfma_f32_16x16x32_bf16 v[80:83], v[176:179], a[4:7], 0\n\t" \
    "v_mfma_f32_16x16x32_bf16 v[96:99], v[176:179], a[8:11], 0\n\t" \
    "ds_read_b128 v[188:191], v208 offset:7168\n\t" \
    "v_mfma_f32_16x16x32_bf16 v[112:115], v[176:179], a[12:15], 0\n\t" \
    "s_waitcnt lgkmcnt(2)\n\t" \
    "v_mfma_f32_16x16x32_bf16 v[68:71], v[180:183], a[0:3], 0\n\t" \
    "buffer_load_dwordx4 a[68:71], v212, s[40:43], s49 offen offset:256\n\t" \
    "v_mfma_f32_16x16x32_bf16 v[84:87], v[180:183], a[4:7], 0\n\t" \
    "ds_read_b128 v[192:195], v208 offset:8192\n\t" \
    "v_mfma_f32_16x16x32_bf16 v[100:103], v[180:183], a[8:11], 0\n\t" \
    "v_mfma_f32_16x16x32_bf16 v[116:119], v[180:183], a[12:15], 0\n\t" \
    "s_waitcnt lgkmcnt(2)\n\t" \
    "ds_read_b128 v[196:199], v208 offset:9216\n\t" \
    "v_mfma_f32_16x16x32_bf16 v[72:75], v[184:187], a[0:3], 0\n\t" \
    "buffer_load_dwordx4 a[72:75], v212, s[40:43], s49 offen offset:512\n\t" \
    "v_mfma_f32_16x16x32_bf16 v[88:91], v[184:187], a[4:7], 0\n\t" \
    "v_mfma_f32_16x16x32_bf16 v[104:107], v[184:187], a[8:11], 0\n\t" \
    "v_mfma_f32_16x16x32_bf16 v[120:123], v[184:187], a[12:15], 0\n\t" \
    "s_waitcnt lgkmcnt(2)\n\t" \
    "v_mfma_f32_16x16x32_bf16 v[76:79], v[188:191], a[0:3], 0\n\t" \
    "buffer_load_dwordx4 a[76:79], v212, s[40:43], s49 offen offset:768\n\t" \
    "v_mfma_f32_16x16x32_bf16 v[92:95], v[188:191], a[4:7], 0\n\t" \
    "s_add_u32 s49, %[xoff], 655360\n\t" \
    "ds_read_b128 v[200:203], v208 offset:10240\n\t" \
    "v_mfma_f32_16x16x32_bf16 v[108:111], v[188:191], a[8:11], 0\n\t" \
    "s_waitcnt vmcnt(16)\n\t" \
    "ds_read_b128 v[204:207], v208 offset:11264\n\t" \
    "v_mfma_f32_16x16x32_bf16 v[124:127], v[188:191], a[12:15], 0\n\t" \
    "s_waitcnt lgkmcnt(2)\n\t" \
    "v_mfma_f32_16x16x32_bf16 v[0:3], v[192:195], a[16:19], v[0:3]\n\t" \
    "s_waitcnt vmcnt(12)\n\t" \
    "buffer_load_dwordx4 a[80:83], v212, s[40:43], s49 offen offset:0\n\t" \
    "v_mfma_f32_16x16x32_bf16 v[0:3], v[196:199], a[32:35], v[0:3]\n\t" \
    "v_mfma_f32_16x16x32_bf16 v[16:19], v[192:195], a[20:23], v[16:19]\n\t" \
    "v_mfma_f32_16x16x32_bf16 v[16:19], v[196:199], a[36:39], v[16:19]\n\t" \
    "v_mfma_f32_16x16x32_bf16 v[32:35], v[192:195], a[24:27], v[32:35]\n\t" \
    "buffer_load_dwordx4 a[84:87], v212, s[40:43], s49 offen offset:256\n\t" \
    "v_mfma_f32_16x16x32_bf16 v[32:35], v[196:199], a[40:43], v[32:35]\n\t" \
    "v_mfma_f32_16x16x32_bf16 v[48:51], v[192:195], a[28:31], v[48:51]\n\t" \
    "ds_read_b128 v[160:163], v208 offset:12288\n\t" \
    "v_mfma_f32_16x16x32_bf16 v[48:51], v[196:199], a[44:47], v[48:51]\n\t" \
    "s_waitcnt lgkmcnt(1)\n\t" \
    "ds_read_b128 v[164:167], v208 offset:13312\n\t" \
    "v_mfma_f32_16x16x32_bf16 v[4:7], v[200:203], a[16:19], v[4:7]\n\t" \
    "buffer_load_dwordx4 a[88:91], v212, s[40:43], s49 offen offset:512\n\t" \
    "v_mfma_f32_16x16x32_bf16 v[4:7], v[204:207], a[32:35], v[4:7]\n\t" \
    "v_mfma_f32_16x16x32_bf16 v[20:23], v[200:203], a[20:23], v[20:23]\n\t" \
    "v_mfma_f32_16x16x32_bf16 v[20:23], v[204:207], a[36:39], v[20:23]\n\t" \
    "v_mfma_f32_16x16x32_bf16 v[36:39], v[200:203], a[24:27], v[36:39]\n\t" \
    "buffer_load_dwordx4 a[92:95], v212, s[40:43], s49 offen offset:768\n\t" \
    "v_mfma_f32_16x16x32_bf16 v[36:39], v[204:207], a[40:43], v[36:39]\n\t" \
    "s_add_u32 s49, %[xoff], 786432\n\t" \
    "ds_read_b128 v[168:171], v208 offset:14336\n\t" \
    "v_mfma_f32_16x16x32_bf16 v[52:55], v[200:203], a[28:31], v[52:55]\n\t" \
    "ds_read_b128 v[172:175], v208 offset:15360\n\t" \
    "v_mfma_f32_16x16x32_bf16 v[52:55], v[204:207], a[44:47], v[52:55]\n\t" \
    "s_waitcnt lgkmcnt(2)\n\t" \
    "v_mfma_f32_16x16x32_bf16 v[8:11], v[160:163], a[16:19], v[8:11]\n\t" \
    "buffer_load_dwordx4 v211, s[40:43], s48 offen offset:0 lds\n\t" \
    "v_mfma_f32_16x16x32_bf16 v[8:11], v[164:167], a[32:35], v[8:11]\n\t" \
    "v_mfma_f32_16x16x32_bf16 v[24:27], v[160:163], a[20:23], v[24:27]\n\t" \
    "v_mfma_f32_16x16x32_bf16 v[24:27], v[164:167], a[36:39], v[24:27]\n\t" \
    "s_waitcnt vmcnt(13)\n\t" \
    "s_barrier\n\t" \
    "v_mfma_f32_16x16x32_bf16 v[40:43], v[160:163], a[24:27], v[40:43]\n\t" \
    "buffer_load_dwordx4 v211, s[40:43], s48 offen offset:1024 lds\n\t" \
    "v_mfma_f32_16x16x32_bf16 v[40:43], v[164:167], a[40:43], v[40:43]\n\t" \
    "ds_read_b128 v[176:179], v208 offset:16384\n\t" \
    "v_mfma_f32_16x16x32_bf16 v[56:59], v[160:163], a[28:31], v[56:59]\n\t" \
    "ds_read_b128 v[180:183], v208 offset:17408\n\t" \
    "v_mfma_f32_16x16x32_bf16 v[56:59], v[164:167], a[44:47], v[56:59]\n\t" \
    "s_waitcnt lgkmcnt(2)\n\t" \
    "v_mfma_f32_16x16x32_bf16 v[12:15], v[168:171], a[16:19], v[12:15]\n\t" \
    "buffer_load_dwordx4 v211, s[40:43], s48 offen offset:2048 lds\n\t" \
    "v_mfma_f32_16x16x32_bf16 v[12:15], v[172:175], a[32:35], v[12:15]\n\t" \
    "v_mfma_f32_16x16x32_bf16 v[28:31], v[168:171], a[20:23], v[28:31]\n\t" \
    "v_mfma_f32_16x16x32_bf16 v[28:31], v[172:175], a[36:39], v[28:31]\n\t" \
    "v_mfma_f32_16x16x32_bf16 v[44:47], v[168:171], a[24:27], v[44:47]\n\t" \
    "buffer_load_dwordx4 v211, s[40:43], s48 offen offset:3072 lds\n\t" \
    "v_mfma_f32_16x16x32_bf16 v[44:47], v[172:175], a[40:43], v[44:47]\n\t" \
    "s_add_u32 m0, %[m0base], 49152\n\t" \
    "ds_read_b128 v[184:187], v208 offset:18432\n\t" \
    "s_add_u32 s48, %[w1off], 49152\n\t" \
    "v_mfma_f32_16x16x32_bf16 v[60:63], v[168:171], a[28:31], v[60:63]\n\t" \
    "v_mfma_f32_16x16x32_bf16 v[60:63], v[172:175], a[44:47], v[60:63]\n\t" \
    "s_waitcnt lgkmcnt(1)\n\t" \
    "ds_read_b128 v[188:191], v208 offset:19456\n\t" \
    "v_mfma_f32_16x16x32_bf16 v[64:67], v[176:179], a[16:19], v[64:67]\n\t" \
    "buffer_load_dwordx4 v211, s[40:43], s48 offen offset:0 lds\n\t" \
    "v_mfma_f32_16x16x32_bf16 v[64:67], v[180:183], a[32:35], v[64:67]\n\t" \
    "v_mfma_f32_16x16x32_bf16 v[80:83], v[176:179], a[20:23], v[80:83]\n\t" \
    "v_mfma_f32_16x16x32_bf16 v[80:83], v[180:183], a[36:39], v[80:83]\n\t" \
    "v_mfma_f32_16x16x32_bf16 v[96:99], v[176:179], a[24:27], v[96:99]\n\t" \
    "buffer_load_dwordx4 v211, s[40:43], s48 offen offset:1024 lds\n\t" \
    "v_mfma_f32_16x16x32_bf16 v[96:99], v[180:183], a[40:43], v[96:99]\n\t" \
    "ds_read_b128 v[192:195], v208 offset:20480\n\t" \
    "v_mfma_f32_16x16x32_bf16 v[112:115], v[176:179], a[28:31], v[112:115]\n\t" \
    "ds_read_b128 v[196:199], v208 offset:21504\n\t" \
    "v_mfma_f32_16x16x32_bf16 v[112:115], v[180:183], a[44:47], v[112:115]\n\t" \
    "s_waitcnt lgkmcnt(2)\n\t" \
    "v_mfma_f32_16x16x32_bf16 v[68:71], v[184:187], a[16:19], v[68:71]\n\t" \
    "buffer_load_dwordx4 v211, s[40:43], s48 offen offset:2048 lds\n\t" \
    "v_mfma_f32_16x16x32_bf16 v[68:71], v[188:191], a[32:35], v[68:71]\n\t" \
    "v_mfma_f32_16x16x32_bf16 v[84:87], v[184:187], a[20:23], v[84:87]\n\t" \
    "v_mfma_f32_16x16x32_bf16 v[84:87], v[188:191], a[36:39], v[84:87]\n\t" \
    "v_mfma_f32_16x16x32_bf16 v[100:103], v[184:187], a[24:27], v[100:103]\n\t" \
    "buffer_load_dwordx4 v211, s[40:43], s48 offen offset:3072 lds\n\t" \
    "v_mfma_f32_16x16x32_bf16 v[100:103], v[188:191], a[40:43], v[100:103]\n\t" \
    "s_add_u32 m0, %[m0base], 65536\n\t" \
    "v_mfma_f32_16x16x32_bf16 v[116:119], v[184:187], a[28:31], v[116:119]\n\t" \
    "s_add_u32 s48, %[w1off], 65536\n\t" \
    "ds_read_b128 v[200:203], v208 offset:22528\n\t" \
    "v_mfma_f32_16x16x32_bf16 v[116:119], v[188:191], a[44:47], v[116:119]\n\t" \
    "s_waitcnt lgkmcnt(1)\n\t" \
    "ds_read_b128 v[204:207], v208 offset:23552\n\t" \
    "v_mfma_f32_16x16x32_bf16 v[72:75], v[192:195], a[16:19], v[72:75]\n\t" \
    "buffer_load_dwordx4 a[96:99], v212, s[40:43], s49 offen offset:0\n\t" \
    "v_mfma_f32_16x16x32_bf16 v[72:75], v[196:199], a[32:35], v[72:75]\n\t" \
    "v_mfma_f32_16x16x32_bf16 v[88:91], v[192:195], a[20:23], v[88:91]\n\t" \
    "v_mfma_f32_16x16x32_bf16 v[88:91], v[196:199], a[36:39], v[88:91]\n\t" \
    "v_mfma_f32_16x16x32_bf16 v[104:107], v[192:195], a[24:27], v[104:107]\n\t" \
    "buffer_load_dwordx4 a[100:103], v212, s[40:43], s49 offen offset:256\n\t" \
    "v_mfma_f32_16x16x32_bf16 v[104:107], v[196:199], a[40:43], v[104:107]\n\t" \
    "ds_read_b128 v[160:163], v208 offset:24576\n\t" \
    "v_mfma_f32_16x16x32_bf16 v[120:123], v[192:195], a[28:31], v[120:123]\n\t" \
    "ds_read_b128 v[164:167], v208 offset:25600\n\t" \
    "v_mfma_f32_16x16x32_bf16 v[120:123], v[196:199], a[44:47], v[120:123]\n\t" \
    "s_waitcnt lgkmcnt(2)\n\t" \
    "v_mfma_f32_16x16x32_bf16 v[76:79], v[200:203], a[16:19], v[76:79]\n\t" \
    "ds_read_b128 v[168:171], v208 offset:26624\n\t" \
    "v_mfma_f32_16x16x32_bf16 v[76:79], v[204:207], a[32:35], v[76:79]\n\t" \
    "buffer_load_dwordx4 a[104:107], v212, s[40:43], s49 offen offset:512\n\t" \
    "v_mfma_f32_16x16x32_bf16 v[92:95], v[200:203], a[20:23], v[92:95]\n\t" \
    "v_mfma_f32_16x16x32_bf16 v[92:95], v[204:207], a[36:39], v[92:95]\n\t" \
    "v_mfma_f32_16x16x32_bf16 v[108:111], v[200:203], a[24:27], v[108:111]\n\t" \
    "v_mfma_f32_16x16x32_bf16 v[108:111], v[204:207], a[40:43], v[108:111]\n\t" \
    "s_waitcnt vmcnt(11)\n\t" \
    "buffer_load_dwordx4 a[108:111], v212, s[40:43], s49 offen offset:768\n\t" \
    "v_mfma_f32_16x16x32_bf16 v[124:127], v[200:203], a[28:31], v[124:127]\n\t" \
    "s_add_u32 s49, %[xoff], 917504\n\t" \
    "v_mfma_f32_16x16x32_bf16 v[124:127], v[204:207], a[44:47], v[124:127]\n\t" \
    "s_waitcnt lgkmcnt(1)\n\t" \
    "v_mfma_f32_16x16x32_bf16 v[0:3], v[160:163], a[48:51], v[0:3]\n\t" \
    "v_mfma_f32_16x16x32_bf16 v[0:3], v[164:167], a[64:67], v[0:3]\n\t" \
    "s_waitcnt lgkmcnt(0)\n\t" \
    "buffer_load_dwordx4 a[112:115], v212, s[40:43], s49 offen offset:0\n\t" \
    "v_mfma_f32_16x16x32_bf16 v[0:3], v[168:171], a[80:83], v[0:3]\n\t" \
    "ds_read_b128 v[172:175], v208 offset:27648\n\t" \
    "v_mfma_f32_16x16x32_bf16 v[16:19], v[160:163], a[52:55], v[16:19]\n\t" \
    "ds_read_b128 v[176:179], v208 offset:28672\n\t" \
    "v_mfma_f32_16x16x32_bf16 v[16:19], v[164:167], a[68:71], v[16:19]\n\t" \
    "ds_read_b128 v[180:183], v208 offset:29696\n\t" \
    "v_mfma_f32_16x16x32_bf16 v[16:19], v[168:171], a[84:87], v[16:19]\n\t" \
    "buffer_load_dwordx4 a[116:119], v212, s[40:43], s49 offen offset:256\n\t" \
    "v_mfma_f32_16x16x32_bf16 v[32:35], v[160:163], a[56:59], v[32:35]\n\t" \
    "v_mfma_f32_16x16x32_bf16 v[32:35], v[164:167], a[72:75], v[32:35]\n\t" \
    "v_mfma_f32_16x16x32_bf16 v[32:35], v[168:171], a[88:91], v[32:35]\n\t" \
    "v_mfma_f32_16x16x32_bf16 v[48:51], v[160:163], a[60:63], v[48:51]\n\t" \
    "buffer_load_dwordx4 a[120:123], v212, s[40:43], s49 offen offset:512\n\t" \
    "v_mfma_f32_16x16x32_bf16 v[48:51], v[164:167], a[76:79], v[48:51]\n\t" \
    "v_mfma_f32_16x16x32_bf16 v[48:51], v[168:171], a[92:95], v[48:51]\n\t" \
    "s_waitcnt lgkmcnt(1)\n\t" \
    "v_mfma_f32_16x16x32_bf16 v[4:7], v[172:175], a[48:51], v[4:7]\n\t" \
    "v_mfma_f32_16x16x32_bf16 v[4:7], v[176:179], a[64:67], v[4:7]\n\t" \
    "s_waitcnt lgkmcnt(0)\n\t" \
    "ds_read_b128 v[184:187], v208 offset:30720\n\t" \
    "v_mfma_f32_16x16x32_bf16 v[4:7], v[180:183], a[80:83], v[4:7]\n\t" \
    "s_waitcnt vmcnt(11)\n\t" \
    "s_barrier\n\t" \
    "ds_read_b128 v[188:191], v208 offset:31744\n\t" \
    "v_mfma_f32_16x16x32_bf16 v[20:23], v[172:175], a[52:55], v[20:23]\n\t" \
    "ds_read_b128 v[192:195], v208 offset:32768\n\t" \
    "v_mfma_f32_16x16x32_bf16 v[20:23], v[176:179], a[68:71], v[20:23]\n\t" \
    "buffer_load_dwordx4 a[124:127], v212, s[40:43], s49 offen offset:768\n\t" \
    "v_mfma_f32_16x16x32_bf16 v[20:23], v[180:183], a[84:87], v[20:23]\n\t" \
    "s_add_u32 s49, %[xoff], 1048576\n\t" \
    "v_mfma_f32_16x16x32_bf16 v[36:39], v[172:175], a[56:59], v[36:39]\n\t" \
    "v_mfma_f32_16x16x32_bf16 v[36:39], v[176:179], a[72:75], v[36:39]\n\t" \
    "v_mfma_f32_16x16x32_bf16 v[36:39], v[180:183], a[88:91], v[36:39]\n\t" \
    "buffer_load_dwordx4 a[128:131], v212, s[40:43], s49 offen offset:0\n\t" \
    "v_mfma_f32_16x16x32_bf16 v[52:55], v[172:175], a[60:63], v[52:55]\n\t" \
    "v_mfma_f32_16x16x32_bf16 v[52:55], v[176:179], a[76:79], v[52:55]\n\t" \
    "v_mfma_f32_16x16x32_bf16 v[52:55], v[180:183], a[92:95], v[52:55]\n\t" \
    "s_waitcnt lgkmcnt(1)\n\t" \
    "v_mfma_f32_16x16x32_bf16 v[8:11], v[184:187], a[48:51], v[8:11]\n\t" \
    "buffer_load_dwordx4 a[132:135], v212, s[40:43], s49 offen offset:256\n\t" \
    "v_mfma_f32_16x16x32_bf16 v[8:11], v[188:191], a[64:67], v[8:11]\n\t" \
    "s_waitcnt lgkmcnt(0)\n\t" \
    "ds_read_b128 v[196:199], v208 offset:33792\n\t" \
    "v_mfma_f32_16x16x32_bf16 v[8:11], v[192:195], a[80:83], v[8:11]\n\t" \
    "v_mfma_f32_16x16x32_bf16 v[24:27], v[184:187], a[52:55], v[24:27]\n\t" \
    "ds_read_b128 v[200:203], v208 offset:34816\n\t" \
    "v_mfma_f32_16x16x32_bf16 v[24:27], v[188:191], a[68:71], v[24:27]\n\t" \
    "ds_read_b128 v[204:207], v208 offset:35840\n\t" \
    "v_mfma_f32_16x16x32_bf16 v[24:27], v[192:195], a[84:87], v[24:27]\n\t" \
    "buffer_load_dwordx4 a[136:139], v212, s[40:43], s49 offen offset:512\n\t" \
    "v_mfma_f32_16x16x32_bf16 v[40:43], v[184:187], a[56:59], v[40:43]\n\t" \
    "v_mfma_f32_16x16x32_bf16 v[40:43], v[188:191], a[72:75], v[40:43]\n\t" \
    "v_mfma_f32_16x16x32_bf16 v[40:43], v[192:195], a[88:91], v[40:43]\n\t" \
    "v_mfma_f32_16x16x32_bf16 v[56:59], v[184:187], a[60:63], v[56:59]\n\t" \
    "buffer_load_dwordx4 a[140:143], v212, s[40:43], s49 offen offset:768\n\t" \
    "v_mfma_f32_16x16x32_bf16 v[56:59], v[188:191], a[76:79], v[56:59]\n\t" \
    "s_add_u32 s49, %[xoff], 1179648\n\t" \
    "v_mfma_f32_16x16x32_bf16 v[56:59], v[192:195], a[92:95], v[56:59]\n\t" \
    "s_waitcnt lgkmcnt(1)\n\t" \
    "v_mfma_f32_16x16x32_bf16 v[12:15], v[196:199], a[48:51], v[12:15]\n\t" \
    "v_mfma_f32_16x16x32_bf16 v[12:15], v[200:203], a[64:67], v[12:15]\n\t" \
    "s_waitcnt lgkmcnt(0)\n\t" \
    "ds_read_b128 v[160:163], v208 offset:36864\n\t" \
    "v_mfma_f32_16x16x32_bf16 v[12:15], v[204:207], a[80:83], v[12:15]\n\t" \
    "ds_read_b128 v[164:167], v208 offset:37888\n\t" \
    "v_mfma_f32_16x16x32_bf16 v[28:31], v[196:199], a[52:55], v[28:31]\n\t" \
    "ds_read_b128 v[168:171], v208 offset:38912\n\t" \
    "v_mfma_f32_16x16x32_bf16 v[28:31], v[200:203], a[68:71], v[28:31]\n\t" \
    "buffer_load_dwordx4 v211, s[40:43], s48 offen offset:0 lds\n\t" \
    "v_mfma_f32_16x16x32_bf16 v[28:31], v[204:207], a[84:87], v[28:31]\n\t" \
    "v_mfma_f32_16x16x32_bf16 v[44:47], v[196:199], a[56:59], v[44:47]\n\t" \
    "v_mfma_f32_16x16x32_bf16 v[44:47], v[200:203], a[72:75], v[44:47]\n\t" \
    "v_mfma_f32_16x16x32_bf16 v[44:47], v[204:207], a[88:91], v[44:47]\n\t" \
    "buffer_load_dwordx4 v211, s[40:43], s48 offen offset:1024 lds\n\t" \
    "v_mfma_f32_16x16x32_bf16 v[60:63], v[196:199], a[60:63], v[60:63]\n\t" \
    "v_mfma_f32_16x16x32_bf16 v[60:63], v[200:203], a[76:79], v[60:63]\n\t" \
    "v_mfma_f32_16x16x32_bf16 v[60:63], v[204:207], a[92:95], v[60:63]\n\t" \
    "s_waitcnt lgkmcnt(1)\n\t" \
    "v_mfma_f32_16x16x32_bf16 v[64:67], v[160:163], a[48:51], v[64:67]\n\t" \
    "buffer_load_dwordx4 v211, s[40:43], s48 offen offset:2048 lds\n\t" \
    "v_mfma_f32_16x16x32_bf16 v[64:67], v[164:167], a[64:67], v[64:67]\n\t" \
    "s_waitcnt lgkmcnt(0)\n\t" \
    "ds_read_b128 v[172:175], v208 offset:39936\n\t" \
    "v_mfma_f32_16x16x32_bf16 v[64:67], v[168:171], a[80:83], v[64:67]\n\t" \
    "ds_read_b128 v[176:179], v208 offset:40960\n\t" \
    "v_mfma_f32_16x16x32_bf16 v[80:83], v[160:163], a[52:55], v[80:83]\n\t" \
    "v_mfma_f32_16x16x32_bf16 v[80:83], v[164:167], a[68:71], v[80:83]\n\t" \
    "ds_read_b128 v[180:183], v208 offset:41984\n\t" \
    "v_mfma_f32_16x16x32_bf16 v[80:83], v[168:171], a[84:87], v[80:83]\n\t" \
    "buffer_load_dwordx4 v211, s[40:43], s48 offen offset:3072 lds\n\t" \
    "v_mfma_f32_16x16x32_bf16 v[96:99], v[160:163], a[56:59], v[96:99]\n\t" \
    "s_add_u32 m0, %[m0base], 81920\n\t" \
    "s_add_u32 s48, %[w1off], 81920\n\t" \
    "v_mfma_f32_16x16x32_bf16 v[96:99], v[164:167], a[72:75], v[96:99]\n\t" \
    "v_mfma_f32_16x16x32_bf16 v[96:99], v[168:171], a[88:91], v[96:99]\n\t" \
    "v_mfma_f32_16x16x32_bf16 v[112:115], v[160:163], a[60:63], v[112:115]\n\t" \
    "buffer_load_dwordx4 a[144:147], v212, s[40:43], s49 offen offset:0\n\t" \
    "v_mfma_f32_16x16x32_bf16 v[112:115], v[164:167], a[76:79], v[112:115]\n\t" \
    "v_mfma_f32_16x16x32_bf16 v[112:115], v[168:171], a[92:95], v[112:115]\n\t" \
    "s_waitcnt lgkmcnt(1)\n\t" \
    "v_mfma_f32_16x16x32_bf16 v[68:71], v[172:175], a[48:51], v[68:71]\n\t" \
    "v_mfma_f32_16x16x32_bf16 v[68:71], v[176:179], a[64:67], v[68:71]\n\t" \
    "s_waitcnt lgkmcnt(0)\n\t" \
    "buffer_load_dwordx4 a[148:151], v212, s[40:43], s49 offen offset:256\n\t" \
    "v_mfma_f32_16x16x32_bf16 v[68:71], v[180:183], a[80:83], v[68:71]\n\t" \
    "ds_read_b128 v[184:187], v208 offset:43008\n\t" \
    "v_mfma_f32_16x16x32_bf16 v[84:87], v[172:175], a[52:55], v[84:87]\n\t" \
    "ds_read_b128 v[188:191], v208 offset:44032\n\t" \
    "v_mfma_f32_16x16x32_bf16 v[84:87], v[176:179], a[68:71], v[84:87]\n\t" \
    "ds_read_b128 v[192:195], v208 offset:45056\n\t" \
    "v_mfma_f32_16x16x32_bf16 v[84:87], v[180:183], a[84:87], v[84:87]\n\t" \
    "buffer_load_dwordx4 a[152:155], v212, s[40:43], s49 offen offset:512\n\t" \
    "v_mfma_f32_16x16x32_bf16 v[100:103], v[172:175], a[56:59], v[100:103]\n\t" \
    "v_mfma_f32_16x16x32_bf16 v[100:103], v[176:179], a[72:75], v[100:103]\n\t" \
    "v_mfma_f32_16x16x32_bf16 v[100:103], v[180:183], a[88:91], v[100:103]\n\t" \
    "v_mfma_f32_16x16x32_bf16 v[116:119], v[172:175], a[60:63], v[116:119]\n\t" \
    "buffer_load_dwordx4 a[156:159], v212, s[40:43], s49 offen offset:768\n\t" \
    "v_mfma_f32_16x16x32_bf16 v[116:119], v[176:179], a[76:79], v[116:119]\n\t" \
    "s_add_u32 s49, %[xoff], 1310720\n\t" \
    "v_mfma_f32_16x16x32_bf16 v[116:119], v[180:183], a[92:95], v[116:119]\n\t" \
    "s_waitcnt lgkmcnt(1)\n\t" \
    "v_mfma_f32_16x16x32_bf16 v[72:75], v[184:187], a[48:51], v[72:75]\n\t" \
    "v_mfma_f32_16x16x32_bf16 v[72:75], v[188:191], a[64:67], v[72:75]\n\t" \
    "s_waitcnt lgkmcnt(0)\n\t" \
    "ds_read_b128 v[196:199], v208 offset:46080\n\t" \
    "v_mfma_f32_16x16x32_bf16 v[72:75], v[192:195], a[80:83], v[72:75]\n\t" \
    "ds_read_b128 v[200:203], v208 offset:47104\n\t" \
    "v_mfma_f32_16x16x32_bf16 v[88:91], v[184:187], a[52:55], v[88:91]\n\t" \
    "ds_read_b128 v[204:207], v208 offset:48128\n\t" \
    "v_mfma_f32_16x16x32_bf16 v[88:91], v[188:191], a[68:71], v[88:91]\n\t" \
    "buffer_load_dwordx4 a[160:163], v212, s[40:43], s49 offen offset:0\n\t" \
    "v_mfma_f32_16x16x32_bf16 v[88:91], v[192:195], a[84:87], v[88:91]\n\t" \
    "v_mfma_f32_16x16x32_bf16 v[104:107], v[184:187], a[56:59], v[104:107]\n\t" \
    "v_mfma_f32_16x16x32_bf16 v[104:107], v[188:191], a[72:75], v[104:107]\n\t" \
    "v_mfma_f32_16x16x32_bf16 v[104:107], v[192:195], a[88:91], v[104:107]\n\t" \
    "buffer_load_dwordx4 a[164:167], v212, s[40:43], s49 offen offset:256\n\t" \
    "v_mfma_f32_16x16x32_bf16 v[120:123], v[184:187], a[60:63], v[120:123]\n\t" \
    "v_mfma_f32_16x16x32_bf16 v[120:123], v[188:191], a[76:79], v[120:123]\n\t" \
    "v_mfma_f32_16x16x32_bf16 v[120:123], v[192:195], a[92:95], v[120:123]\n\t" \
    "s_waitcnt lgkmcnt(1)\n\t" \
    "v_mfma_f32_16x16x32_bf16 v[76:79], v[196:199], a[48:51], v[76:79]\n\t" \
    "s_waitcnt vmcnt(22)\n\t" \
    "s_barrier\n\t" \
    "v_mfma_f32_16x16x32_bf16 v[76:79], v[200:203], a[64:67], v[76:79]\n\t" \
    "s_waitcnt lgkmcnt(0)\n\t" \
    "ds_read_b128 v[160:163], v208 offset:49152\n\t" \
    "v_mfma_f32_16x16x32_bf16 v[76:79], v[204:207], a[80:83], v[76:79]\n\t" \
    "buffer_load_dwordx4 a[168:171], v212, s[40:43], s49 offen offset:512\n\t" \
    "v_mfma_f32_16x16x32_bf16 v[92:95], v[196:199], a[52:55], v[92:95]\n\t" \
    "ds_read_b128 v[164:167], v208 offset:50176\n\t" \
    "v_mfma_f32_16x16x32_bf16 v[92:95], v[200:203], a[68:71], v[92:95]\n\t" \
    "ds_read_b128 v[168:171], v208 offset:51200\n\t" \
    "v_mfma_f32_16x16x32_bf16 v[92:95], v[204:207], a[84:87], v[92:95]\n\t" \
    "v_mfma_f32_16x16x32_bf16 v[108:111], v[196:199], a[56:59], v[108:111]\n\t" \
    "buffer_load_dwordx4 a[172:175], v212, s[40:43], s49 offen offset:768\n\t" \
    "v_mfma_f32_16x16x32_bf16 v[108:111], v[200:203], a[72:75], v[108:111]\n\t" \
    "s_add_u32 s49, %[xoff], 1441792\n\t" \
    "v_mfma_f32_16x16x32_bf16 v[108:111], v[204:207], a[88:91], v[108:111]\n\t" \
    "v_mfma_f32_16x16x32_bf16 v[124:127], v[196:199], a[60:63], v[124:127]\n\t" \
    "s_waitcnt vmcnt(12)\n\t" \
    "v_mfma_f32_16x16x32_bf16 v[124:127], v[200:203], a[76:79], v[124:127]\n\t" \
    "buffer_load_dwordx4 a[176:179], v212, s[40:43], s49 offen offset:0\n\t" \
    "v_mfma_f32_16x16x32_bf16 v[124:127], v[204:207], a[92:95], v[124:127]\n\t" \
    "s_waitcnt lgkmcnt(1)\n\t" \
    "v_mfma_f32_16x16x32_bf16 v[0:3], v[160:163], a[96:99], v[0:3]\n\t" \
    "v_mfma_f32_16x16x32_bf16 v[0:3], v[164:167], a[112:115], v[0:3]\n\t" \
    "s_waitcnt lgkmcnt(0)\n\t" \
    "ds_read_b128 v[172:175], v208 offset:52224\n\t" \
    "v_mfma_f32_16x16x32_bf16 v[0:3], v[168:171], a[128:131], v[0:3]\n\t" \
    "ds_read_b128 v[176:179], v208 offset:53248\n\t" \
    "v_mfma_f32_16x16x32_bf16 v[16:19], v[160:163], a[100:103], v[16:19]\n\t" \
    "ds_read_b128 v[180:183], v208 offset:54272\n\t" \
    "v_mfma_f32_16x16x32_bf16 v[16:19], v[164:167], a[116:119], v[16:19]\n\t" \
    "buffer_load_dwordx4 a[180:183], v212, s[40:43], s49 offen offset:256\n\t" \
    "v_mfma_f32_16x16x32_bf16 v[16:19], v[168:171], a[132:135], v[16:19]\n\t" \
    "v_mfma_f32_16x16x32_bf16 v[32:35], v[160:163], a[104:107], v[32:35]\n\t" \
    "v_mfma_f32_16x16x32_bf16 v[32:35], v[164:167], a[120:123], v[32:35]\n\t" \
    "v_mfma_f32_16x16x32_bf16 v[32:35], v[168:171], a[136:139], v[32:35]\n\t" \
    "buffer_load_dwordx4 a[184:187], v212, s[40:43], s49 offen offset:512\n\t" \
    "v_mfma_f32_16x16x32_bf16 v[48:51], v[160:163], a[108:111], v[48:51]\n\t" \
    "v_mfma_f32_16x16x32_bf16 v[48:51], v[164:167], a[124:127], v[48:51]\n\t" \
    "v_mfma_f32_16x16x32_bf16 v[48:51], v[168:171], a[140:143], v[48:51]\n\t" \
    "s_waitcnt lgkmcnt(1)\n\t" \
    "v_mfma_f32_16x16x32_bf16 v[4:7], v[172:175], a[96:99], v[4:7]\n\t" \
    "buffer_load_dwordx4 a[188:191], v212, s[40:43], s49 offen offset:768\n\t" \
    "v_mfma_f32_16x16x32_bf16 v[4:7], v[176:179], a[112:115], v[4:7]\n\t" \
    "s_waitcnt lgkmcnt(0)\n\t" \
    "ds_read_b128 v[184:187], v208 offset:55296\n\t" \
    "s_add_u32 s49, %[xoff], 1572864\n\t" \
    "v_mfma_f32_16x16x32_bf16 v[4:7], v[180:183], a[128:131], v[4:7]\n\t" \
    "ds_read_b128 v[188:191], v208 offset:56320\n\t" \
    "v_mfma_f32_16x16x32_bf16 v[20:23], v[172:175], a[100:103], v[20:23]\n\t" \
    "v_mfma_f32_16x16x32_bf16 v[20:23], v[176:179], a[116:119], v[20:23]\n\t" \
    "ds_read_b128 v[192:195], v208 offset:57344\n\t" \
    "v_mfma_f32_16x16x32_bf16 v[20:23], v[180:183], a[132:135], v[20:23]\n\t" \
    "buffer_load_dwordx4 v211, s[40:43], s48 offen offset:0 lds\n\t" \
    "v_mfma_f32_16x16x32_bf16 v[36:39], v[172:175], a[104:107], v[36:39]\n\t" \
    "v_mfma_f32_16x16x32_bf16 v[36:39], v[176:179], a[120:123], v[36:39]\n\t" \
    "v_mfma_f32_16x16x32_bf16 v[36:39], v[180:183], a[136:139], v[36:39]\n\t" \
    "v_mfma_f32_16x16x32_bf16 v[52:55], v[172:175], a[108:111], v[52:55]\n\t" \
    "buffer_load_dwordx4 v211, s[40:43], s48 offen offset:1024 lds\n\t" \
    "v_mfma_f32_16x16x32_bf16 v[52:55], v[176:179], a[124:127], v[52:55]\n\t" \
    "v_mfma_f32_16x16x32_bf16 v[52:55], v[180:183], a[140:143], v[52:55]\n\t" \
    "s_waitcnt lgkmcnt(1)\n\t" \
    "v_mfma_f32_16x16x32_bf16 v[8:11], v[184:187], a[96:99], v[8:11]\n\t" \
    "v_mfma_f32_16x16x32_bf16 v[8:11], v[188:191], a[112:115], v[8:11]\n\t" \
    "s_waitcnt lgkmcnt(0)\n\t" \
    "buffer_load_dwordx4 v211, s[40:43], s48 offen offset:2048 lds\n\t" \
    "v_mfma_f32_16x16x32_bf16 v[8:11], v[192:195], a[128:131], v[8:11]\n\t" \
    "ds_read_b128 v[196:199], v208 offset:58368\n\t" \
    "v_mfma_f32_16x16x32_bf16 v[24:27], v[184:187], a[100:103], v[24:27]\n\t" \
    "ds_read_b128 v[200:203], v208 offset:59392\n\t" \
    "v_mfma_f32_16x16x32_bf16 v[24:27], v[188:191], a[116:119], v[24:27]\n\t" \
    "ds_read_b128 v[204:207], v208 offset:60416\n\t" \
    "v_mfma_f32_16x16x32_bf16 v[24:27], v[192:195], a[132:135], v[24:27]\n\t" \
    "buffer_load_dwordx4 v211, s[40:43], s48 offen offset:3072 lds\n\t" \
    "v_mfma_f32_16x16x32_bf16 v[40:43], v[184:187], a[104:107], v[40:43]\n\t" \
    "s_add_u32 m0, %[m0base], 98304\n\t" \
    "v_mfma_f32_16x16x32_bf16 v[40:43], v[188:191], a[120:123], v[40:43]\n\t" \
    "s_add_u32 s48, %[w1off], 98304\n\t" \
    "v_mfma_f32_16x16x32_bf16 v[40:43], v[192:195], a[136:139], v[40:43]\n\t" \
    "v_mfma_f32_16x16x32_bf16 v[56:59], v[184:187], a[108:111], v[56:59]\n\t" \
    "v_mfma_f32_16x16x32_bf16 v[56:59], v[188:191], a[124:127], v[56:59]\n\t" \
    "buffer_load_dwordx4 v211, s[40:43], s48 offen offset:0 lds\n\t" \
    "v_mfma_f32_16x16x32_bf16 v[56:59], v[192:195], a[140:143], v[56:59]\n\t" \
    "s_waitcnt lgkmcnt(1)\n\t" \
    "v_mfma_f32_16x16x32_bf16 v[12:15], v[196:199], a[96:99], v[12:15]\n\t" \
    "v_mfma_f32_16x16x32_bf16 v[12:15], v[200:203], a[112:115], v[12:15]\n\t" \
    "s_waitcnt lgkmcnt(0)\n\t" \
    "ds_read_b128 v[160:163], v208 offset:61440\n\t" \
    "v_mfma_f32_16x16x32_bf16 v[12:15], v[204:207], a[128:131], v[12:15]\n\t" \
    "ds_read_b128 v[164:167], v208 offset:62464\n\t" \
    "v_mfma_f32_16x16x32_bf16 v[28:31], v[196:199], a[100:103], v[28:31]\n\t" \
    "ds_read_b128 v[168:171], v208 offset:63488\n\t" \
    "v_mfma_f32_16x16x32_bf16 v[28:31], v[200:203], a[116:119], v[28:31]\n\t" \
    "buffer_load_dwordx4 v211, s[40:43], s48 offen offset:1024 lds\n\t" \
    "v_mfma_f32_16x16x32_bf16 v[28:31], v[204:207], a[132:135], v[28:31]\n\t" \
    "v_mfma_f32_16x16x32_bf16 v[44:47], v[196:199], a[104:107], v[44:47]\n\t" \
    "v_mfma_f32_16x16x32_bf16 v[44:47], v[200:203], a[120:123], v[44:47]\n\t" \
    "v_mfma_f32_16x16x32_bf16 v[44:47], v[204:207], a[136:139], v[44:47]\n\t" \
    "buffer_load_dwordx4 v211, s[40:43], s48 offen offset:2048 lds\n\t" \
    "v_mfma_f32_16x16x32_bf16 v[60:63], v[196:199], a[108:111], v[60:63]\n\t" \
    "v_mfma_f32_16x16x32_bf16 v[60:63], v[200:203], a[124:127], v[60:63]\n\t" \
    "v_mfma_f32_16x16x32_bf16 v[60:63], v[204:207], a[140:143], v[60:63]\n\t" \
    "s_waitcnt lgkmcnt(1)\n\t" \
    "v_mfma_f32_16x16x32_bf16 v[64:67], v[160:163], a[96:99], v[64:67]\n\t" \
    "s_waitcnt vmcnt(19)\n\t" \
    "s_barrier\n\t" \
    "v_mfma_f32_16x16x32_bf16 v[64:67], v[164:167], a[112:115], v[64:67]\n\t" \
    "s_waitcnt lgkmcnt(0)\n\t" \
    "ds_read_b128 v[172:175], v208 offset:64512\n\t" \
    "v_mfma_f32_16x16x32_bf16 v[64:67], v[168:171], a[128:131], v[64:67]\n\t" \
    "buffer_load_dwordx4 v211, s[40:43], s48 offen offset:3072 lds\n\t" \
    "v_mfma_f32_16x16x32_bf16 v[80:83], v[160:163], a[100:103], v[80:83]\n\t" \
    "s_add_u32 m0, %[m0base], 114688\n\t" \
    "ds_read_b128 v[176:179], v209 offset:0\n\t" \
    "s_add_u32 s48, %[w1off], 114688\n\t" \
    "v_mfma_f32_16x16x32_bf16 v[80:83], v[164:167], a[116:119], v[80:83]\n\t" \
    "ds_read_b128 v[180:183], v209 offset:1024\n\t" \
    "v_mfma_f32_16x16x32_bf16 v[80:83], v[168:171], a[132:135], v[80:83]\n\t" \
    "v_mfma_f32_16x16x32_bf16 v[96:99], v[160:163], a[104:107], v[96:99]\n\t" \
    "buffer_load_dwordx4 a[192:195], v212, s[40:43], s49 offen offset:0\n\t" \
    "v_mfma_f32_16x16x32_bf16 v[96:99], v[164:167], a[120:123], v[96:99]\n\t" \
    "v_mfma_f32_16x16x32_bf16 v[96:99], v[168:171], a[136:139], v[96:99]\n\t" \
    "v_mfma_f32_16x16x32_bf16 v[112:115], v[160:163], a[108:111], v[112:115]\n\t" \
    "v_mfma_f32_16x16x32_bf16 v[112:115], v[164:167], a[124:127], v[112:115]\n\t" \
    "buffer_load_dwordx4 a[196:199], v212, s[40:43], s49 offen offset:256\n\t" \
    "v_mfma_f32_16x16x32_bf16 v[112:115], v[168:171], a[140:143], v[112:115]\n\t" \
    "s_waitcnt lgkmcnt(1)\n\t" \
    "v_mfma_f32_16x16x32_bf16 v[68:71], v[172:175], a[96:99], v[68:71]\n\t" \
    "v_mfma_f32_16x16x32_bf16 v[68:71], v[176:179], a[112:115], v[68:71]\n\t" \
    "s_waitcnt lgkmcnt(0)\n\t" \
    "ds_read_b128 v[184:187], v209 offset:2048\n\t" \
    "v_mfma_f32_16x16x32_bf16 v[68:71], v[180:183], a[128:131], v[68:71]\n\t" \
    "ds_read_b128 v[188:191], v209 offset:3072\n\t" \
    "v_mfma_f32_16x16x32_bf16 v[84:87], v[172:175], a[100:103], v[84:87]\n\t" \
    "ds_read_b128 v[192:195], v209 offset:4096\n\t" \
    "v_mfma_f32_16x16x32_bf16 v[84:87], v[176:179], a[116:119], v[84:87]\n\t" \
    "buffer_load_dwordx4 a[200:203], v212, s[40:43], s49 offen offset:512\n\t" \
    "v_mfma_f32_16x16x32_bf16 v[84:87], v[180:183], a[132:135], v[84:87]\n\t" \
    "v_mfma_f32_16x16x32_bf16 v[100:103], v[172:175], a[104:107], v[100:103]\n\t" \
    "v_mfma_f32_16x16x32_bf16 v[100:103], v[176:179], a[120:123], v[100:103]\n\t" \
    "v_mfma_f32_16x16x32_bf16 v[100:103], v[180:183], a[136:139], v[100:103]\n\t" \
    "buffer_load_dwordx4 a[204:207], v212, s[40:43], s49 offen offset:768\n\t" \
    "v_mfma_f32_16x16x32_bf16 v[116:119], v[172:175], a[108:111], v[116:119]\n\t" \
    "s_add_u32 s49, %[xoff], 1703936\n\t" \
    "v_mfma_f32_16x16x32_bf16 v[116:119], v[176:179], a[124:127], v[116:119]\n\t" \
    "v_mfma_f32_16x16x32_bf16 v[116:119], v[180:183], a[140:143], v[116:119]\n\t" \
    "s_waitcnt lgkmcnt(1)\n\t" \
    "v_mfma_f32_16x16x32_bf16 v[72:75], v[184:187], a[96:99], v[72:75]\n\t" \
    "buffer_load_dwordx4 a[208:211], v212, s[40:43], s49 offen offset:0\n\t" \
    "v_mfma_f32_16x16x32_bf16 v[72:75], v[188:191], a[112:115], v[72:75]\n\t" \
    "s_waitcnt lgkmcnt(0)\n\t" \
    "ds_read_b128 v[196:199], v209 offset:5120\n\t" \
    "v_mfma_f32_16x16x32_bf16 v[72:75], v[192:195], a[128:131], v[72:75]\n\t" \
    "ds_read_b128 v[200:203], v209 offset:6144\n\t" \
    "v_mfma_f32_16x16x32_bf16 v[88:91], v[184:187], a[100:103], v[88:91]\n\t" \
    "v_mfma_f32_16x16x32_bf16 v[88:91], v[188:191], a[116:119], v[88:91]\n\t" \
    "ds_read_b128 v[204:207], v209 offset:7168\n\t" \
    "v_mfma_f32_16x16x32_bf16 v[88:91], v[192:195], a[132:135], v[88:91]\n\t" \
    "buffer_load_dwordx4 a[212:215], v212, s[40:43], s49 offen offset:256\n\t" \
    "v_mfma_f32_16x16x32_bf16 v[104:107], v[184:187], a[104:107], v[104:107]\n\t" \
    "v_mfma_f32_16x16x32_bf16 v[104:107], v[188:191], a[120:123], v[104:107]\n\t" \
    "v_mfma_f32_16x16x32_bf16 v[104:107], v[192:195], a[136:139], v[104:107]\n\t" \
    "v_mfma_f32_16x16x32_bf16 v[120:123], v[184:187], a[108:111], v[120:123]\n\t" \
    "buffer_load_dwordx4 a[216:219], v212, s[40:43], s49 offen offset:512\n\t" \
    "v_mfma_f32_16x16x32_bf16 v[120:123], v[188:191], a[124:127], v[120:123]\n\t" \
    "v_mfma_f32_16x16x32_bf16 v[120:123], v[192:195], a[140:143], v[120:123]\n\t" \
    "s_waitcnt lgkmcnt(1)\n\t" \
    "v_mfma_f32_16x16x32_bf16 v[76:79], v[196:199], a[96:99], v[76:79]\n\t" \
    "v_mfma_f32_16x16x32_bf16 v[76:79], v[200:203], a[112:115], v[76:79]\n\t" \
    "s_waitcnt lgkmcnt(0)\n\t" \
    "buffer_load_dwordx4 a[220:223], v212, s[40:43], s49 offen offset:768\n\t" \
    "v_mfma_f32_16x16x32_bf16 v[76:79], v[204:207], a[128:131], v[76:79]\n\t" \
    "s_add_u32 s49, %[xoff], 1835008\n\t" \
    "ds_read_b128 v[160:163], v209 offset:8192\n\t" \
    "v_mfma_f32_16x16x32_bf16 v[92:95], v[196:199], a[100:103], v[92:95]\n\t" \
    "ds_read_b128 v[164:167], v209 offset:9216\n\t" \
    "v_mfma_f32_16x16x32_bf16 v[92:95], v[200:203], a[116:119], v[92:95]\n\t" \
    "ds_read_b128 v[168:171], v209 offset:10240\n\t" \
    "v_mfma_f32_16x16x32_bf16 v[92:95], v[204:207], a[132:135], v[92:95]\n\t" \
    "buffer_load_dwordx4 a[224:227], v212, s[40:43], s49 offen offset:0\n\t" \
    "v_mfma_f32_16x16x32_bf16 v[108:111], v[196:199], a[104:107], v[108:111]\n\t" \
    "v_mfma_f32_16x16x32_bf16 v[108:111], v[200:203], a[120:123], v[108:111]\n\t" \
    "v_mfma_f32_16x16x32_bf16 v[108:111], v[204:207], a[136:139], v[108:111]\n\t" \
    "v_mfma_f32_16x16x32_bf16 v[124:127], v[196:199], a[108:111], v[124:127]\n\t" \
    "s_waitcnt vmcnt(17)\n\t" \
    "buffer_load_dwordx4 a[228:231], v212, s[40:43], s49 offen offset:256\n\t" \
    "v_mfma_f32_16x16x32_bf16 v[124:127], v[200:203], a[124:127], v[124:127]\n\t" \
    "v_mfma_f32_16x16x32_bf16 v[124:127], v[204:207], a[140:143], v[124:127]\n\t" \
    "s_waitcnt lgkmcnt(1)\n\t" \
    "v_mfma_f32_16x16x32_bf16 v[0:3], v[160:163], a[144:147], v[0:3]\n\t" \
    "v_mfma_f32_16x16x32_bf16 v[0:3], v[164:167], a[160:163], v[0:3]\n\t" \
    "s_waitcnt lgkmcnt(0)\n\t" \
    "ds_read_b128 v[172:175], v209 offset:11264\n\t" \
    "v_mfma_f32_16x16x32_bf16 v[0:3], v[168:171], a[176:179], v[0:3]\n\t" \
    "ds_read_b128 v[176:179], v209 offset:12288\n\t" \
    "v_mfma_f32_16x16x32_bf16 v[16:19], v[160:163], a[148:151], v[16:19]\n\t" \
    "ds_read_b128 v[180:183], v209 offset:13312\n\t" \
    "v_mfma_f32_16x16x32_bf16 v[16:19], v[164:167], a[164:167], v[16:19]\n\t" \
    "buffer_load_dwordx4 a[232:235], v212, s[40:43], s49 offen offset:512\n\t" \
    "v_mfma_f32_16x16x32_bf16 v[16:19], v[168:171], a[180:183], v[16:19]\n\t" \
    "v_mfma_f32_16x16x32_bf16 v[32:35], v[160:163], a[152:155], v[32:35]\n\t" \
    "v_mfma_f32_16x16x32_bf16 v[32:35], v[164:167], a[168:171], v[32:35]\n\t" \
    "v_mfma_f32_16x16x32_bf16 v[32:35], v[168:171], a[184:187], v[32:35]\n\t" \
    "buffer_load_dwordx4 a[236:239], v212, s[40:43], s49 offen offset:768\n\t" \
    "v_mfma_f32_16x16x32_bf16 v[48:51], v[160:163], a[156:159], v[48:51]\n\t" \
    "s_add_u32 s49, %[xoff], 1966080\n\t" \
    "v_mfma_f32_16x16x32_bf16 v[48:51], v[164:167], a[172:175], v[48:51]\n\t" \
    "v_mfma_f32_16x16x32_bf16 v[48:51], v[168:171], a[188:191], v[48:51]\n\t" \
    "s_waitcnt lgkmcnt(1)\n\t" \
    "v_mfma_f32_16x16x32_bf16 v[4:7], v[172:175], a[144:147], v[4:7]\n\t" \
    "buffer_load_dwordx4 v211, s[40:43], s48 offen offset:0 lds\n\t" \
    "v_mfma_f32_16x16x32_bf16 v[4:7], v[176:179], a[160:163], v[4:7]\n\t" \
    "s_waitcnt lgkmcnt(0)\n\t" \
    "ds_read_b128 v[184:187], v209 offset:14336\n\t" \
    "v_mfma_f32_16x16x32_bf16 v[4:7], v[180:183], a[176:179], v[4:7]\n\t" \
    "v_mfma_f32_16x16x32_bf16 v[20:23], v[172:175], a[148:151], v[20:23]\n\t" \
    "s_waitcnt vmcnt(17)\n\t" \
    "s_barrier\n\t" \
    "ds_read_b128 v[188:191], v209 offset:15360\n\t" \
    "v_mfma_f32_16x16x32_bf16 v[20:23], v[176:179], a[164:167], v[20:23]\n\t" \
    "ds_read_b128 v[192:195], v209 offset:16384\n\t" \
    "v_mfma_f32_16x16x32_bf16 v[20:23], v[180:183], a[180:183], v[20:23]\n\t" \
    "buffer_load_dwordx4 v211, s[40:43], s48 offen offset:1024 lds\n\t" \
    "v_mfma_f32_16x16x32_bf16 v[36:39], v[172:175], a[152:155], v[36:39]\n\t" \
    "v_mfma_f32_16x16x32_bf16 v[36:39], v[176:179], a[168:171], v[36:39]\n\t" \
    "v_mfma_f32_16x16x32_bf16 v[36:39], v[180:183], a[184:187], v[36:39]\n\t" \
    "v_mfma_f32_16x16x32_bf16 v[52:55], v[172:175], a[156:159], v[52:55]\n\t" \
    "buffer_load_dwordx4 v211, s[40:43], s48 offen offset:2048 lds\n\t" \
    "v_mfma_f32_16x16x32_bf16 v[52:55], v[176:179], a[172:175], v[52:55]\n\t" \
    "v_mfma_f32_16x16x32_bf16 v[52:55], v[180:183], a[188:191], v[52:55]\n\t" \
    "s_waitcnt lgkmcnt(1)\n\t" \
    "v_mfma_f32_16x16x32_bf16 v[8:11], v[184:187], a[144:147], v[8:11]\n\t" \
    "v_mfma_f32_16x16x32_bf16 v[8:11], v[188:191], a[160:163], v[8:11]\n\t" \
    "s_waitcnt lgkmcnt(0)\n\t" \
    "ds_read_b128 v[196:199], v209 offset:17408\n\t" \
    "v_mfma_f32_16x16x32_bf16 v[8:11], v[192:195], a[176:179], v[8:11]\n\t" \
    "ds_read_b128 v[200:203], v209 offset:18432\n\t" \
    "v_mfma_f32_16x16x32_bf16 v[24:27], v[184:187], a[148:151], v[24:27]\n\t" \
    "ds_read_b128 v[204:207], v209 offset:19456\n\t" \
    "v_mfma_f32_16x16x32_bf16 v[24:27], v[188:191], a[164:167], v[24:27]\n\t" \
    "buffer_load_dwordx4 v211, s[40:43], s48 offen offset:3072 lds\n\t" \
    "v_mfma_f32_16x16x32_bf16 v[24:27], v[192:195], a[180:183], v[24:27]\n\t" \
    "s_add_u32 m0, %[m0base], 131072\n\t" \
    "v_mfma_f32_16x16x32_bf16 v[40:43], v[184:187], a[152:155], v[40:43]\n\t" \
    "s_add_u32 s48, %[w1off], 131072\n\t" \
    "v_mfma_f32_16x16x32_bf16 v[40:43], v[188:191], a[168:171], v[40:43]\n\t" \
    "v_mfma_f32_16x16x32_bf16 v[40:43], v[192:195], a[184:187], v[40:43]\n\t" \
    "buffer_load_dwordx4 a[240:243], v212, s[40:43], s49 offen offset:0\n\t" \
    "v_mfma_f32_16x16x32_bf16 v[56:59], v[184:187], a[156:159], v[56:59]\n\t" \
    "v_mfma_f32_16x16x32_bf16 v[56:59], v[188:191], a[172:175], v[56:59]\n\t" \
    "v_mfma_f32_16x16x32_bf16 v[56:59], v[192:195], a[188:191], v[56:59]\n\t" \
    "s_waitcnt lgkmcnt(1)\n\t" \
    "v_mfma_f32_16x16x32_bf16 v[12:15], v[196:199], a[144:147], v[12:15]\n\t" \
    "buffer_load_dwordx4 a[244:247], v212, s[40:43], s49 offen offset:256\n\t" \
    "v_mfma_f32_16x16x32_bf16 v[12:15], v[200:203], a[160:163], v[12:15]\n\t" \
    "s_waitcnt lgkmcnt(0)\n\t" \
    "ds_read_b128 v[160:163], v209 offset:20480\n\t" \
    "v_mfma_f32_16x16x32_bf16 v[12:15], v[204:207], a[176:179], v[12:15]\n\t" \
    "ds_read_b128 v[164:167], v209 offset:21504\n\t" \
    "v_mfma_f32_16x16x32_bf16 v[28:31], v[196:199], a[148:151], v[28:31]\n\t" \
    "v_mfma_f32_16x16x32_bf16 v[28:31], v[200:203], a[164:167], v[28:31]\n\t" \
    "ds_read_b128 v[168:171], v209 offset:22528\n\t" \
    "v_mfma_f32_16x16x32_bf16 v[28:31], v[204:207], a[180:183], v[28:31]\n\t" \
    "buffer_load_dwordx4 a[248:251], v212, s[40:43], s49 offen offset:512\n\t" \
    "v_mfma_f32_16x16x32_bf16 v[44:47], v[196:199], a[152:155], v[44:47]\n\t" \
    "v_mfma_f32_16x16x32_bf16 v[44:47], v[200:203], a[168:171], v[44:47]\n\t" \
    "v_mfma_f32_16x16x32_bf16 v[44:47], v[204:207], a[184:187], v[44:47]\n\t" \
    "v_mfma_f32_16x16x32_bf16 v[60:63], v[196:199], a[156:159], v[60:63]\n\t" \
    "buffer_load_dwordx4 a[252:255], v212, s[40:43], s49 offen offset:768\n\t" \
    "v_mfma_f32_16x16x32_bf16 v[60:63], v[200:203], a[172:175], v[60:63]\n\t" \
    "s_add_u32 s49, %[xoff], 2097152\n\t" \
    "v_mfma_f32_16x16x32_bf16 v[60:63], v[204:207], a[188:191], v[60:63]\n\t" \
    "s_waitcnt lgkmcnt(1)\n\t" \
    "v_mfma_f32_16x16x32_bf16 v[64:67], v[160:163], a[144:147], v[64:67]\n\t" \
    "v_mfma_f32_16x16x32_bf16 v[64:67], v[164:167], a[160:163], v[64:67]\n\t" \
    "s_waitcnt lgkmcnt(0)\n\t" \
    "buffer_load_dwordx4 v[128:131], v212, s[40:43], s49 offen offset:0\n\t" \
    "v_mfma_f32_16x16x32_bf16 v[64:67], v[168:171], a[176:179], v[64:67]\n\t" \
    "ds_read_b128 v[172:175], v209 offset:23552\n\t" \
    "v_mfma_f32_16x16x32_bf16 v[80:83], v[160:163], a[148:151], v[80:83]\n\t" \
    "ds_read_b128 v[176:179], v209 offset:24576\n\t" \
    "v_mfma_f32_16x16x32_bf16 v[80:83], v[164:167], a[164:167], v[80:83]\n\t" \
    "ds_read_b128 v[180:183], v209 offset:25600\n\t" \
    "v_mfma_f32_16x16x32_bf16 v[80:83], v[168:171], a[180:183], v[80:83]\n\t" \
    "buffer_load_dwordx4 v[132:135], v212, s[40:43], s49 offen offset:256\n\t" \
    "v_mfma_f32_16x16x32_bf16 v[96:99], v[160:163], a[152:155], v[96:99]\n\t" \
    "v_mfma_f32_16x16x32_bf16 v[96:99], v[164:167], a[168:171], v[96:99]\n\t" \
    "v_mfma_f32_16x16x32_bf16 v[96:99], v[168:171], a[184:187], v[96:99]\n\t" \
    "v_mfma_f32_16x16x32_bf16 v[112:115], v[160:163], a[156:159], v[112:115]\n\t" \
    "buffer_load_dwordx4 v[136:139], v212, s[40:43], s49 offen offset:512\n\t" \
    "v_mfma_f32_16x16x32_bf16 v[112:115], v[164:167], a[172:175], v[112:115]\n\t" \
    "v_mfma_f32_16x16x32_bf16 v[112:115], v[168:171], a[188:191], v[112:115]\n\t" \
    "s_waitcnt lgkmcnt(1)\n\t" \
    "v_mfma_f32_16x16x32_bf16 v[68:71], v[172:175], a[144:147], v[68:71]\n\t" \
    "v_mfma_f32_16x16x32_bf16 v[68:71], v[176:179], a[160:163], v[68:71]\n\t" \
    "s_waitcnt lgkmcnt(0)\n\t" \
    "ds_read_b128 v[184:187], v209 offset:26624\n\t" \
    "v_mfma_f32_16x16x32_bf16 v[68:71], v[180:183], a[176:179], v[68:71]\n\t" \
    "ds_read_b128 v[188:191], v209 offset:27648\n\t" \
    "v_mfma_f32_16x16x32_bf16 v[84:87], v[172:175], a[148:151], v[84:87]\n\t" \
    "ds_read_b128 v[192:195], v209 offset:28672\n\t" \
    "v_mfma_f32_16x16x32_bf16 v[84:87], v[176:179], a[164:167], v[84:87]\n\t" \
    "buffer_load_dwordx4 v[140:143], v212, s[40:43], s49 offen offset:768\n\t" \
    "v_mfma_f32_16x16x32_bf16 v[84:87], v[180:183], a[180:183], v[84:87]\n\t" \
    "s_add_u32 s49, %[xoff], 2228224\n\t" \
    "v_mfma_f32_16x16x32_bf16 v[100:103], v[172:175], a[152:155], v[100:103]\n\t" \
    "v_mfma_f32_16x16x32_bf16 v[100:103], v[176:179], a[168:171], v[100:103]\n\t" \
    "v_mfma_f32_16x16x32_bf16 v[100:103], v[180:183], a[184:187], v[100:103]\n\t" \
    "buffer_load_dwordx4 v[144:147], v212, s[40:43], s49 offen offset:0\n\t" \
    "v_mfma_f32_16x16x32_bf16 v[116:119], v[172:175], a[156:159], v[116:119]\n\t" \
    "v_mfma_f32_16x16x32_bf16 v[116:119], v[176:179], a[172:175], v[116:119]\n\t" \
    "v_mfma_f32_16x16x32_bf16 v[116:119], v[180:183], a[188:191], v[116:119]\n\t" \
    "s_waitcnt lgkmcnt(1)\n\t" \
    "v_mfma_f32_16x16x32_bf16 v[72:75], v[184:187], a[144:147], v[72:75]\n\t" \
    "buffer_load_dwordx4 v[148:151], v212, s[40:43], s49 offen offset:256\n\t" \
    "v_mfma_f32_16x16x32_bf16 v[72:75], v[188:191], a[160:163], v[72:75]\n\t" \
    "s_waitcnt lgkmcnt(0)\n\t" \
    "ds_read_b128 v[196:199], v209 offset:29696\n\t" \
    "v_mfma_f32_16x16x32_bf16 v[72:75], v[192:195], a[176:179], v[72:75]\n\t" \
    "v_mfma_f32_16x16x32_bf16 v[88:91], v[184:187], a[148:151], v[88:91]\n\t" \
    "ds_read_b128 v[200:203], v209 offset:30720\n\t" \
    "v_mfma_f32_16x16x32_bf16 v[88:91], v[188:191], a[164:167], v[88:91]\n\t" \
    "ds_read_b128 v[204:207], v209 offset:31744\n\t" \
    "v_mfma_f32_16x16x32_bf16 v[88:91], v[192:195], a[180:183], v[88:91]\n\t" \
    "buffer_load_dwordx4 v[152:155], v212, s[40:43], s49 offen offset:512\n\t" \
    "v_mfma_f32_16x16x32_bf16 v[104:107], v[184:187], a[152:155], v[104:107]\n\t" \
    "v_mfma_f32_16x16x32_bf16 v[104:107], v[188:191], a[168:171], v[104:107]\n\t" \
    "v_mfma_f32_16x16x32_bf16 v[104:107], v[192:195], a[184:187], v[104:107]\n\t" \
    "v_mfma_f32_16x16x32_bf16 v[120:123], v[184:187], a[156:159], v[120:123]\n\t" \
    "buffer_load_dwordx4 v[156:159], v212, s[40:43], s49 offen offset:768\n\t" \
    "v_mfma_f32_16x16x32_bf16 v[120:123], v[188:191], a[172:175], v[120:123]\n\t" \
    "s_waitcnt vmcnt(28)\n\t" \
    "s_barrier\n\t" \
    "v_mfma_f32_16x16x32_bf16 v[120:123], v[192:195], a[188:191], v[120:123]\n\t" \
    "s_waitcnt lgkmcnt(1)\n\t" \
    "v_mfma_f32_16x16x32_bf16 v[76:79], v[196:199], a[144:147], v[76:79]\n\t" \
    "v_mfma_f32_16x16x32_bf16 v[76:79], v[200:203], a[160:163], v[76:79]\n\t" \
    "s_waitcnt lgkmcnt(0)\n\t" \
    "ds_read_b128 v[160:163], v209 offset:32768\n\t" \
    "v_mfma_f32_16x16x32_bf16 v[76:79], v[204:207], a[176:179], v[76:79]\n\t" \
    "ds_read_b128 v[164:167], v209 offset:33792\n\t" \
    "v_mfma_f32_16x16x32_bf16 v[92:95], v[196:199], a[148:151], v[92:95]\n\t" \
    "ds_read_b128 v[168:171], v209 offset:34816\n\t" \
    "v_mfma_f32_16x16x32_bf16 v[92:95], v[200:203], a[164:167], v[92:95]\n\t" \
    "buffer_load_dwordx4 v211, s[40:43], s48 offen offset:0 lds\n\t" \
    "v_mfma_f32_16x16x32_bf16 v[92:95], v[204:207], a[180:183], v[92:95]\n\t" \
    "v_mfma_f32_16x16x32_bf16 v[108:111], v[196:199], a[152:155], v[108:111]\n\t" \
    "v_mfma_f32_16x16x32_bf16 v[108:111], v[200:203], a[168:171], v[108:111]\n\t" \
    "v_mfma_f32_16x16x32_bf16 v[108:111], v[204:207], a[184:187], v[108:111]\n\t" \
    "buffer_load_dwordx4 v211, s[40:43], s48 offen offset:1024 lds\n\t" \
    "v_mfma_f32_16x16x32_bf16 v[124:127], v[196:199], a[156:159], v[124:127]\n\t" \
    "s_waitcnt vmcnt(18)\n\t" \
    "v_mfma_f32_16x16x32_bf16 v[124:127], v[200:203], a[172:175], v[124:127]\n\t" \
    "v_mfma_f32_16x16x32_bf16 v[124:127], v[204:207], a[188:191], v[124:127]\n\t" \
    "s_waitcnt lgkmcnt(1)\n\t" \
    "v_mfma_f32_16x16x32_bf16 v[0:3], v[160:163], a[192:195], v[0:3]\n\t" \
    "buffer_load_dwordx4 v211, s[40:43], s48 offen offset:2048 lds\n\t" \
    "v_mfma_f32_16x16x32_bf16 v[0:3], v[164:167], a[208:211], v[0:3]\n\t" \
    "s_waitcnt lgkmcnt(0)\n\t" \
    "ds_read_b128 v[172:175], v209 offset:35840\n\t" \
    "v_mfma_f32_16x16x32_bf16 v[0:3], v[168:171], a[224:227], v[0:3]\n\t" \
    "ds_read_b128 v[176:179], v209 offset:36864\n\t" \
    "v_mfma_f32_16x16x32_bf16 v[16:19], v[160:163], a[196:199], v[16:19]\n\t" \
    "v_mfma_f32_16x16x32_bf16 v[16:19], v[164:167], a[212:215], v[16:19]\n\t" \
    "ds_read_b128 v[180:183], v209 offset:37888\n\t" \
    "v_mfma_f32_16x16x32_bf16 v[16:19], v[168:171], a[228:231], v[16:19]\n\t" \
    "buffer_load_dwordx4 v211, s[40:43], s48 offen offset:3072 lds\n\t" \
    "v_mfma_f32_16x16x32_bf16 v[32:35], v[160:163], a[200:203], v[32:35]\n\t" \
    "s_add_u32 m0, %[m0base], 147456\n\t" \
    "s_add_u32 s48, %[w1off], 147456\n\t" \
    "v_mfma_f32_16x16x32_bf16 v[32:35], v[164:167], a[216:219], v[32:35]\n\t" \
    "v_mfma_f32_16x16x32_bf16 v[32:35], v[168:171], a[232:235], v[32:35]\n\t" \
    "v_mfma_f32_16x16x32_bf16 v[48:51], v[160:163], a[204:207], v[48:51]\n\t" \
    "v_mfma_f32_16x16x32_bf16 v[48:51], v[164:167], a[220:223], v[48:51]\n\t" \
    "v_mfma_f32_16x16x32_bf16 v[48:51], v[168:171], a[236:239], v[48:51]\n\t" \
    "s_waitcnt lgkmcnt(1)\n\t" \
    "v_mfma_f32_16x16x32_bf16 v[4:7], v[172:175], a[192:195], v[4:7]\n\t" \
    "v_mfma_f32_16x16x32_bf16 v[4:7], v[176:179], a[208:211], v[4:7]\n\t" \
    "s_waitcnt lgkmcnt(0)\n\t" \
    "buffer_load_dwordx4 v211, s[40:43], s48 offen offset:0 lds\n\t" \
    "v_mfma_f32_16x16x32_bf16 v[4:7], v[180:183], a[224:227], v[4:7]\n\t" \
    "ds_read_b128 v[184:187], v209 offset:38912\n\t" \
    "v_mfma_f32_16x16x32_bf16 v[20:23], v[172:175], a[196:199], v[20:23]\n\t" \
    "ds_read_b128 v[188:191], v209 offset:39936\n\t" \
    "v_mfma_f32_16x16x32_bf16 v[20:23], v[176:179], a[212:215], v[20:23]\n\t" \
    "ds_read_b128 v[192:195], v209 offset:40960\n\t" \
    "v_mfma_f32_16x16x32_bf16 v[20:23], v[180:183], a[228:231], v[20:23]\n\t" \
    "v_mfma_f32_16x16x32_bf16 v[36:39], v[172:175], a[200:203], v[36:39]\n\t" \
    "buffer_load_dwordx4 v211, s[40:43], s48 offen offset:1024 lds\n\t" \
    "v_mfma_f32_16x16x32_bf16 v[36:39], v[176:179], a[216:219], v[36:39]\n\t" \
    "v_mfma_f32_16x16x32_bf16 v[36:39], v[180:183], a[232:235], v[36:39]\n\t" \
    "v_mfma_f32_16x16x32_bf16 v[52:55], v[172:175], a[204:207], v[52:55]\n\t" \
    "v_mfma_f32_16x16x32_bf16 v[52:55], v[176:179], a[220:223], v[52:55]\n\t" \
    "v_mfma_f32_16x16x32_bf16 v[52:55], v[180:183], a[236:239], v[52:55]\n\t" \
    "s_waitcnt lgkmcnt(1)\n\t" \
    "buffer_load_dwordx4 v211, s[40:43], s48 offen offset:2048 lds\n\t" \
    "v_mfma_f32_16x16x32_bf16 v[8:11], v[184:187], a[192:195], v[8:11]\n\t" \
    "v_mfma_f32_16x16x32_bf16 v[8:11], v[188:191], a[208:211], v[8:11]\n\t" \
    "s_waitcnt lgkmcnt(0)\n\t" \
    "ds_read_b128 v[196:199], v209 offset:41984\n\t" \
    "v_mfma_f32_16x16x32_bf16 v[8:11], v[192:195], a[224:227], v[8:11]\n\t" \
    "ds_read_b128 v[200:203], v209 offset:43008\n\t" \
    "v_mfma_f32_16x16x32_bf16 v[24:27], v[184:187], a[196:199], v[24:27]\n\t" \
    "ds_read_b128 v[204:207], v209 offset:44032\n\t" \
    "v_mfma_f32_16x16x32_bf16 v[24:27], v[188:191], a[212:215], v[24:27]\n\t" \
    "buffer_load_dwordx4 v211, s[40:43], s48 offen offset:3072 lds\n\t" \
    "v_mfma_f32_16x16x32_bf16 v[24:27], v[192:195], a[228:231], v[24:27]\n\t" \
    "s_add_u32 m0, %[m0base], 0\n\t" \
    "s_add_u32 s48, %[w1off], 163840\n\t" \
    "v_mfma_f32_16x16x32_bf16 v[40:43], v[184:187], a[200:203], v[40:43]\n\t" \
    "v_mfma_f32_16x16x32_bf16 v[40:43], v[188:191], a[216:219], v[40:43]\n\t" \
    "v_mfma_f32_16x16x32_bf16 v[40:43], v[192:195], a[232:235], v[40:43]\n\t" \
    "v_mfma_f32_16x16x32_bf16 v[56:59], v[184:187], a[204:207], v[56:59]\n\t" \
    "buffer_load_dwordx4 v211, s[40:43], s48 offen offset:0 lds\n\t" \
    "v_mfma_f32_16x16x32_bf16 v[56:59], v[188:191], a[220:223], v[56:59]\n\t" \
    "v_mfma_f32_16x16x32_bf16 v[56:59], v[192:195], a[236:239], v[56:59]\n\t" \
    "s_waitcnt lgkmcnt(1)\n\t" \
    "v_mfma_f32_16x16x32_bf16 v[12:15], v[196:199], a[192:195], v[12:15]\n\t" \
    "v_mfma_f32_16x16x32_bf16 v[12:15], v[200:203], a[208:211], v[12:15]\n\t" \
    "s_waitcnt lgkmcnt(0)\n\t" \
    "ds_read_b128 v[160:163], v209 offset:45056\n\t" \
    "v_mfma_f32_16x16x32_bf16 v[12:15], v[204:207], a[224:227], v[12:15]\n\t" \
    "buffer_load_dwordx4 v211, s[40:43], s48 offen offset:1024 lds\n\t" \
    "v_mfma_f32_16x16x32_bf16 v[28:31], v[196:199], a[196:199], v[28:31]\n\t" \
    "ds_read_b128 v[164:167], v209 offset:46080\n\t" \
    "v_mfma_f32_16x16x32_bf16 v[28:31], v[200:203], a[212:215], v[28:31]\n\t" \
    "ds_read_b128 v[168:171], v209 offset:47104\n\t" \
    "v_mfma_f32_16x16x32_bf16 v[28:31], v[204:207], a[228:231], v[28:31]\n\t" \
    "v_mfma_f32_16x16x32_bf16 v[44:47], v[196:199], a[200:203], v[44:47]\n\t" \
    "v_mfma_f32_16x16x32_bf16 v[44:47], v[200:203], a[216:219], v[44:47]\n\t" \
    "buffer_load_dwordx4 v211, s[40:43], s48 offen offset:2048 lds\n\t" \
    "v_mfma_f32_16x16x32_bf16 v[44:47], v[204:207], a[232:235], v[44:47]\n\t" \
    "s_waitcnt vmcnt(11)\n\t" \
    "v_mfma_f32_16x16x32_bf16 v[60:63], v[196:199], a[204:207], v[60:63]\n\t" \
    "v_mfma_f32_16x16x32_bf16 v[60:63], v[200:203], a[220:223], v[60:63]\n\t" \
    "s_barrier\n\t" \
    "v_mfma_f32_16x16x32_bf16 v[60:63], v[204:207], a[236:239], v[60:63]\n\t" \
    "s_waitcnt lgkmcnt(1)\n\t" \
    "v_mfma_f32_16x16x32_bf16 v[0:3], v[160:163], a[240:243], v[0:3]\n\t" \
    "buffer_load_dwordx4 v211, s[40:43], s48 offen offset:3072 lds\n\t" \
    "v_mfma_f32_16x16x32_bf16 v[0:3], v[164:167], v[128:131], v[0:3]\n\t" \
    "s_waitcnt lgkmcnt(0)\n\t" \
    "ds_read_b128 v[172:175], v209 offset:48128\n\t" \
    "s_add_u32 m0, %[m0base], 16384\n\t" \
    "v_mfma_f32_16x16x32_bf16 v[0:3], v[168:171], v[144:147], v[0:3]\n\t" \
    "s_add_u32 s48, %[w1off], 180224\n\t" \
    "ds_read_b128 v[176:179], v209 offset:49152\n\t" \
    "v_mfma_f32_16x16x32_bf16 v[16:19], v[160:163], a[244:247], v[16:19]\n\t" \
    "ds_read_b128 v[180:183], v209 offset:50176\n\t" \
    "v_mfma_f32_16x16x32_bf16 v[16:19], v[164:167], v[132:135], v[16:19]\n\t" \
    "v_mfma_f32_16x16x32_bf16 v[16:19], v[168:171], v[148:151], v[16:19]\n\t" \
    "buffer_load_dwordx4 v211, s[40:43], s48 offen offset:0 lds\n\t" \
    "v_mfma_f32_16x16x32_bf16 v[32:35], v[160:163], a[248:251], v[32:35]\n\t" \
    "v_mfma_f32_16x16x32_bf16 v[32:35], v[164:167], v[136:139], v[32:35]\n\t" \
    "v_mfma_f32_16x16x32_bf16 v[32:35], v[168:171], v[152:155], v[32:35]\n\t" \
    "v_mfma_f32_16x16x32_bf16 v[48:51], v[160:163], a[252:255], v[48:51]\n\t" \
    "v_mfma_f32_16x16x32_bf16 v[48:51], v[164:167], v[140:143], v[48:51]\n\t" \
    "buffer_load_dwordx4 v211, s[40:43], s48 offen offset:1024 lds\n\t" \
    "v_mfma_f32_16x16x32_bf16 v[48:51], v[168:171], v[156:159], v[48:51]\n\t" \
    "s_waitcnt lgkmcnt(1)\n\t" \
    "v_mfma_f32_16x16x32_bf16 v[4:7], v[172:175], a[240:243], v[4:7]\n\t" \
    "v_mfma_f32_16x16x32_bf16 v[4:7], v[176:179], v[128:131], v[4:7]\n\t" \
    "s_waitcnt lgkmcnt(0)\n\t" \
    "ds_read_b128 v[184:187], v209 offset:51200\n\t" \
    "v_mfma_f32_16x16x32_bf16 v[4:7], v[180:183], v[144:147], v[4:7]\n\t" \
    "ds_read_b128 v[188:191], v209 offset:52224\n\t" \
    "v_mfma_f32_16x16x32_bf16 v[20:23], v[172:175], a[244:247], v[20:23]\n\t" \
    "buffer_load_dwordx4 v211, s[40:43], s48 offen offset:2048 lds\n\t" \
    "v_mfma_f32_16x16x32_bf16 v[20:23], v[176:179], v[132:135], v[20:23]\n\t" \
    "ds_read_b128 v[192:195], v209 offset:53248\n\t" \
    "v_mfma_f32_16x16x32_bf16 v[20:23], v[180:183], v[148:151], v[20:23]\n\t" \
    "v_mfma_f32_16x16x32_bf16 v[36:39], v[172:175], a[248:251], v[36:39]\n\t" \
    "v_mfma_f32_16x16x32_bf16 v[36:39], v[176:179], v[136:139], v[36:39]\n\t" \
    "v_mfma_f32_16x16x32_bf16 v[36:39], v[180:183], v[152:155], v[36:39]\n\t" \
    "buffer_load_dwordx4 v211, s[40:43], s48 offen offset:3072 lds\n\t" \
    "v_mfma_f32_16x16x32_bf16 v[52:55], v[172:175], a[252:255], v[52:55]\n\t" \
    "s_add_u32 m0, %[m0base], 32768\n\t" \
    "s_add_u32 s48, %[w1off], 196608\n\t" \
    "v_mfma_f32_16x16x32_bf16 v[52:55], v[176:179], v[140:143], v[52:55]\n\t" \
    "v_mfma_f32_16x16x32_bf16 v[52:55], v[180:183], v[156:159], v[52:55]\n\t" \
    "s_waitcnt lgkmcnt(1)\n\t" \
    "v_mfma_f32_16x16x32_bf16 v[8:11], v[184:187], a[240:243], v[8:11]\n\t" \
    "v_mfma_f32_16x16x32_bf16 v[8:11], v[188:191], v[128:131], v[8:11]\n\t" \
    "s_waitcnt lgkmcnt(0)\n\t" \
    "buffer_load_dwordx4 v211, s[40:43], s48 offen offset:0 lds\n\t" \
    "v_mfma_f32_16x16x32_bf16 v[8:11], v[192:195], v[144:147], v[8:11]\n\t" \
    "ds_read_b128 v[196:199], v209 offset:54272\n\t" \
    "v_mfma_f32_16x16x32_bf16 v[24:27], v[184:187], a[244:247], v[24:27]\n\t" \
    "ds_read_b128 v[200:203], v209 offset:55296\n\t" \
    "v_mfma_f32_16x16x32_bf16 v[24:27], v[188:191], v[132:135], v[24:27]\n\t" \
    "ds_read_b128 v[204:207], v209 offset:56320\n\t" \
    "v_mfma_f32_16x16x32_bf16 v[24:27], v[192:195], v[148:151], v[24:27]\n\t" \
    "v_mfma_f32_16x16x32_bf16 v[40:43], v[184:187], a[248:251], v[40:43]\n\t" \
    "buffer_load_dwordx4 v211, s[40:43], s48 offen offset:1024 lds\n\t" \
    "v_mfma_f32_16x16x32_bf16 v[40:43], v[188:191], v[136:139], v[40:43]\n\t" \
    "v_mfma_f32_16x16x32_bf16 v[40:43], v[192:195], v[152:155], v[40:43]\n\t" \
    "v_mfma_f32_16x16x32_bf16 v[56:59], v[184:187], a[252:255], v[56:59]\n\t" \
    "v_mfma_f32_16x16x32_bf16 v[56:59], v[188:191], v[140:143], v[56:59]\n\t" \
    "v_mfma_f32_16x16x32_bf16 v[56:59], v[192:195], v[156:159], v[56:59]\n\t" \
    "s_waitcnt lgkmcnt(1)\n\t" \
    "buffer_load_dwordx4 v211, s[40:43], s48 offen offset:2048 lds\n\t" \
    "v_mfma_f32_16x16x32_bf16 v[12:15], v[196:199], a[240:243], v[12:15]\n\t" \
    "v_mfma_f32_16x16x32_bf16 v[12:15], v[200:203], v[128:131], v[12:15]\n\t" \
    "s_waitcnt lgkmcnt(0)\n\t" \
    "ds_read_b128 v[160:163], v209 offset:57344\n\t" \
    "v_mfma_f32_16x16x32_bf16 v[12:15], v[204:207], v[144:147], v[12:15]\n\t" \
    "ds_read_b128 v[164:167], v209 offset:58368\n\t" \
    "v_mfma_f32_16x16x32_bf16 v[28:31], v[196:199], a[244:247], v[28:31]\n\t" \
    "ds_read_b128 v[168:171], v209 offset:59392\n\t" \
    "v_mfma_f32_16x16x32_bf16 v[28:31], v[200:203], v[132:135], v[28:31]\n\t" \
    "buffer_load_dwordx4 v211, s[40:43], s48 offen offset:3072 lds\n\t" \
    "v_mfma_f32_16x16x32_bf16 v[28:31], v[204:207], v[148:151], v[28:31]\n\t" \
    "s_add_u32 m0, %[m0base], 49152\n\t" \
    "s_add_u32 s48, %[w1off], 212992\n\t" \
    "v_mfma_f32_16x16x32_bf16 v[44:47], v[196:199], a[248:251], v[44:47]\n\t" \
    "v_mfma_f32_16x16x32_bf16 v[44:47], v[200:203], v[136:139], v[44:47]\n\t" \
    "v_mfma_f32_16x16x32_bf16 v[44:47], v[204:207], v[152:155], v[44:47]\n\t" \
    "v_mfma_f32_16x16x32_bf16 v[60:63], v[196:199], a[252:255], v[60:63]\n\t" \
    "buffer_load_dwordx4 v211, s[40:43], s48 offen offset:0 lds\n\t" \
    "v_mfma_f32_16x16x32_bf16 v[60:63], v[200:203], v[140:143], v[60:63]\n\t" \
    "v_mfma_f32_16x16x32_bf16 v[60:63], v[204:207], v[156:159], v[60:63]\n\t" \
    "s_waitcnt lgkmcnt(1)\n\t" \
    "v_mfma_f32_16x16x32_bf16 v[64:67], v[160:163], a[192:195], v[64:67]\n\t" \
    "v_mfma_f32_16x16x32_bf16 v[64:67], v[164:167], a[208:211], v[64:67]\n\t" \
    "s_waitcnt lgkmcnt(0)\n\t" \
    "ds_read_b128 v[172:175], v209 offset:60416\n\t" \
    "v_mfma_f32_16x16x32_bf16 v[64:67], v[168:171], a[224:227], v[64:67]\n\t" \
    "buffer_load_dwordx4 v211, s[40:43], s48 offen offset:1024 lds\n\t" \
    "v_mfma_f32_16x16x32_bf16 v[80:83], v[160:163], a[196:199], v[80:83]\n\t" \
    "ds_read_b128 v[176:179], v209 offset:61440\n\t" \
    "v_max_f32 v0, 0, v0\n\t" \
    "v_mfma_f32_16x16x32_bf16 v[80:83], v[164:167], a[212:215], v[80:83]\n\t" \
    "ds_read_b128 v[180:183], v209 offset:62464\n\t" \
    "v_max_f32 v1, 0, v1\n\t" \
    "v_mfma_f32_16x16x32_bf16 v[80:83], v[168:171], a[228:231], v[80:83]\n\t" \
    "v_max_f32 v2, 0, v2\n\t" \
    "v_mfma_f32_16x16x32_bf16 v[96:99], v[160:163], a[200:203], v[96:99]\n\t" \
    "v_max_f32 v3, 0, v3\n\t" \
    "v_mfma_f32_16x16x32_bf16 v[96:99], v[164:167], a[216:219], v[96:99]\n\t" \
    "buffer_load_dwordx4 v211, s[40:43], s48 offen offset:2048 lds\n\t" \
    "v_mfma_f32_16x16x32_bf16 v[96:99], v[168:171], a[232:235], v[96:99]\n\t" \
    "v_mul_f32 v0, v0, v4\n\t" \
    "v_mul_f32 v1, v1, v5\n\t" \
    "v_mfma_f32_16x16x32_bf16 v[112:115], v[160:163], a[204:207], v[112:115]\n\t" \
    "v_mul_f32 v2, v2, v6\n\t" \
    "v_mfma_f32_16x16x32_bf16 v[112:115], v[164:167], a[220:223], v[112:115]\n\t" \
    "v_mul_f32 v3, v3, v7\n\t" \
    "v_mfma_f32_16x16x32_bf16 v[112:115], v[168:171], a[236:239], v[112:115]\n\t" \
    "s_waitcnt lgkmcnt(1)\n\t" \
    "v_max_f32 v8, 0, v8\n\t" \
    "v_mfma_f32_16x16x32_bf16 v[68:71], v[172:175], a[192:195], v[68:71]\n\t" \
    "buffer_load_dwordx4 v211, s[40:43], s48 offen offset:3072 lds\n\t" \
    "v_mfma_f32_16x16x32_bf16 v[68:71], v[176:179], a[208:211], v[68:71]\n\t" \
    "s_waitcnt lgkmcnt(0)\n\t" \
    "ds_read_b128 v[184:187], v209 offset:63488\n\t" \
    "v_max_f32 v9, 0, v9\n\t" \
    "v_mfma_f32_16x16x32_bf16 v[68:71], v[180:183], a[224:227], v[68:71]\n\t" \
    "s_waitcnt vmcnt(20)\n\t" \
    "s_barrier\n\t" \
    "ds_read_b128 v[188:191], v209 offset:64512\n\t" \
    "v_mfma_f32_16x16x32_bf16 v[84:87], v[172:175], a[196:199], v[84:87]\n\t" \
    "s_add_u32 m0, %[m0base], 65536\n\t" \
    "ds_read_b128 v[192:195], v210 offset:0\n\t" \
    "v_max_f32 v10, 0, v10\n\t" \
    "v_mfma_f32_16x16x32_bf16 v[84:87], v[176:179], a[212:215], v[84:87]\n\t" \
    "s_add_u32 s48, %[w1off], 229376\n\t" \
    "v_max_f32 v11, 0, v11\n\t" \
    "v_mul_f32 v8, v8, v12\n\t" \
    "v_mfma_f32_16x16x32_bf16 v[84:87], v[180:183], a[228:231], v[84:87]\n\t" \
    "buffer_load_dwordx4 v211, s[40:43], s48 offen offset:0 lds\n\t" \
    "v_mfma_f32_16x16x32_bf16 v[100:103], v[172:175], a[200:203], v[100:103]\n\t" \
    "v_mul_f32 v9, v9, v13\n\t" \
    "v_mul_f32 v10, v10, v14\n\t" \
    "v_mfma_f32_16x16x32_bf16 v[100:103], v[176:179], a[216:219], v[100:103]\n\t" \
    "v_mul_f32 v11, v11, v15\n\t" \
    "v_cvt_pk_bf16_f32 v4, v0, v1\n\t" \
    "v_mfma_f32_16x16x32_bf16 v[100:103], v[180:183], a[232:235], v[100:103]\n\t" \
    "v_cvt_pk_bf16_f32 v5, v2, v3\n\t" \
    "v_mfma_f32_16x16x32_bf16 v[116:119], v[172:175], a[204:207], v[116:119]\n\t" \
    "v_cvt_pk_bf16_f32 v6, v8, v9\n\t" \
    "v_mfma_f32_16x16x32_bf16 v[116:119], v[176:179], a[220:223], v[116:119]\n\t" \
    "buffer_load_dwordx4 v211, s[40:43], s48 offen offset:1024 lds\n\t" \
    "v_mfma_f32_16x16x32_bf16 v[116:119], v[180:183], a[236:239], v[116:119]\n\t" \
    "s_waitcnt lgkmcnt(1)\n\t" \
    "v_cvt_pk_bf16_f32 v7, v10, v11\n\t" \
    "s_add_u32 s50, %[pstA], 0\n\t" \
    "v_mfma_f32_16x16x32_bf16 v[72:75], v[184:187], a[192:195], v[72:75]\n\t" \
    "v_mfma_f32_16x16x32_bf16 v[72:75], v[188:191], a[208:211], v[72:75]\n\t" \
    "s_waitcnt lgkmcnt(0)\n\t" \
    "ds_read_b128 v[196:199], v210 offset:1024\n\t" \
    "v_mfma_f32_16x16x32_bf16 v[72:75], v[192:195], a[224:227], v[72:75]\n\t" \
    "ds_read_b128 v[200:203], v210 offset:2048\n\t" \
    "v_mfma_f32_16x16x32_bf16 v[88:91], v[184:187], a[196:199], v[88:91]\n\t" \
    "buffer_store_dwordx4 v[4:7], v211, s[40:43], s50 offen offset:0 sc1\n\t" \
    "v_mfma_f32_16x16x32_bf16 v[88:91], v[188:191], a[212:215], v[88:91]\n\t" \
    "ds_read_b128 v[204:207], v210 offset:3072\n\t" \
    "v_max_f32 v16, 0, v16\n\t" \
    "v_mfma_f32_16x16x32_bf16 v[88:91], v[192:195], a[228:231], v[88:91]\n\t" \
    "v_max_f32 v17, 0, v17\n\t" \
    "v_max_f32 v18, 0, v18\n\t" \
    "v_mfma_f32_16x16x32_bf16 v[104:107], v[184:187], a[200:203], v[104:107]\n\t" \
    "v_max_f32 v19, 0, v19\n\t" \
    "v_mul_f32 v16, v16, v20\n\t" \
    "v_mfma_f32_16x16x32_bf16 v[104:107], v[188:191], a[216:219], v[104:107]\n\t" \
    "v_mul_f32 v17, v17, v21\n\t" \
    "v_mul_f32 v18, v18, v22\n\t" \
    "v_mfma_f32_16x16x32_bf16 v[104:107], v[192:195], a[232:235], v[104:107]\n\t" \
    "v_mfma_f32_16x16x32_bf16 v[120:123], v[184:187], a[204:207], v[120:123]\n\t" \
    "v_mul_f32 v19, v19, v23\n\t" \
    "v_max_f32 v24, 0, v24\n\t" \
    "v_mfma_f32_16x16x32_bf16 v[120:123], v[188:191], a[220:223], v[120:123]\n\t" \
    "v_max_f32 v25, 0, v25\n\t" \
    "v_mfma_f32_16x16x32_bf16 v[120:123], v[192:195], a[236:239], v[120:123]\n\t" \
    "s_waitcnt lgkmcnt(1)\n\t" \
    "v_max_f32 v26, 0, v26\n\t" \
    "v_mfma_f32_16x16x32_bf16 v[76:79], v[196:199], a[192:195], v[76:79]\n\t" \
    "v_max_f32 v27, 0, v27\n\t" \
    "v_mfma_f32_16x16x32_bf16 v[76:79], v[200:203], a[208:211], v[76:79]\n\t" \
    "s_waitcnt lgkmcnt(0)\n\t" \
    "buffer_load_dwordx4 v211, s[40:43], s48 offen offset:2048 lds\n\t" \
    "v_mfma_f32_16x16x32_bf16 v[76:79], v[204:207], a[224:227], v[76:79]\n\t" \
    "ds_read_b128 v[160:163], v210 offset:4096\n\t" \
    "v_mul_f32 v24, v24, v28\n\t" \
    "v_mfma_f32_16x16x32_bf16 v[92:95], v[196:199], a[196:199], v[92:95]\n\t" \
    "ds_read_b128 v[164:167], v210 offset:5120\n\t" \
    "v_mul_f32 v25, v25, v29\n\t" \
    "v_mfma_f32_16x16x32_bf16 v[92:95], v[200:203], a[212:215], v[92:95]\n\t" \
    "ds_read_b128 v[168:171], v210 offset:6144\n\t" \
    "v_mul_f32 v26, v26, v30\n\t" \
    "v_mfma_f32_16x16x32_bf16 v[92:95], v[204:207], a[228:231], v[92:95]\n\t" \
    "v_mul_f32 v27, v27, v31\n\t" \
    "v_cvt_pk_bf16_f32 v20, v16, v17\n\t" \
    "v_mfma_f32_16x16x32_bf16 v[108:111], v[196:199], a[200:203], v[108:111]\n\t" \
    "buffer_load_dwordx4 v211, s[40:43], s48 offen offset:3072 lds\n\t" \
    "v_mfma_f32_16x16x32_bf16 v[108:111], v[200:203], a[216:219], v[108:111]\n\t" \
    "s_add_u32 m0, %[m0base], 81920\n\t" \
    "v_cvt_pk_bf16_f32 v21, v18, v19\n\t" \
    "v_cvt_pk_bf16_f32 v22, v24, v25\n\t" \
    "v_mfma_f32_16x16x32_bf16 v[108:111], v[204:207], a[232:235], v[108:111]\n\t" \
    "s_add_u32 s48, %[w1off], 245760\n\t" \
    "v_cvt_pk_bf16_f32 v23, v26, v27\n\t" \
    "v_mfma_f32_16x16x32_bf16 v[124:127], v[196:199], a[204:207], v[124:127]\n\t" \
    "v_mfma_f32_16x16x32_bf16 v[124:127], v[200:203], a[220:223], v[124:127]\n\t" \
    "v_mfma_f32_16x16x32_bf16 v[124:127], v[204:207], a[236:239], v[124:127]\n\t" \
    "s_waitcnt lgkmcnt(1)\n\t" \
    "buffer_store_dwordx4 v[20:23], v211, s[40:43], s50 offen offset:1024 sc1\n\t" \
    "v_mfma_f32_16x16x32_bf16 v[64:67], v[160:163], a[240:243], v[64:67]\n\t" \
    "v_max_f32 v32, 0, v32\n\t" \
    "v_max_f32 v33, 0, v33\n\t" \
    "v_mfma_f32_16x16x32_bf16 v[64:67], v[164:167], v[128:131], v[64:67]\n\t" \
    "s_waitcnt lgkmcnt(0)\n\t" \
    "ds_read_b128 v[172:175], v210 offset:7168\n\t" \
    "v_max_f32 v34, 0, v34\n\t" \
    "v_mfma_f32_16x16x32_bf16 v[64:67], v[168:171], v[144:147], v[64:67]\n\t" \
    "ds_read_b128 v[176:179], v210 offset:8192\n\t" \
    "v_max_f32 v35, 0, v35\n\t" \
    "v_mfma_f32_16x16x32_bf16 v[80:83], v[160:163], a[244:247], v[80:83]\n\t" \
    "ds_read_b128 v[180:183], v210 offset:9216\n\t" \
    "v_mul_f32 v32, v32, v36\n\t" \
    "v_mfma_f32_16x16x32_bf16 v[80:83], v[164:167], v[132:135], v[80:83]\n\t" \
    "v_mfma_f32_16x16x32_bf16 v[80:83], v[168:171], v[148:151], v[80:83]\n\t" \
    "v_mul_f32 v33, v33, v37\n\t" \
    "v_mul_f32 v34, v34, v38\n\t" \
    "v_mfma_f32_16x16x32_bf16 v[96:99], v[160:163], a[248:251], v[96:99]\n\t" \
    "v_mul_f32 v35, v35, v39\n\t" \
    "v_max_f32 v40, 0, v40\n\t" \
    "v_mfma_f32_16x16x32_bf16 v[96:99], v[164:167], v[136:139], v[96:99]\n\t" \
    "v_max_f32 v41, 0, v41\n\t" \
    "v_mfma_f32_16x16x32_bf16 v[96:99], v[168:171], v[152:155], v[96:99]\n\t" \
    "v_max_f32 v42, 0, v42\n\t" \
    "v_mfma_f32_16x16x32_bf16 v[112:115], v[160:163], a[252:255], v[112:115]\n\t" \
    "buffer_load_dwordx4 v211, s[40:43], s48 offen offset:0 lds\n\t" \
    "v_mfma_f32_16x16x32_bf16 v[112:115], v[164:167], v[140:143], v[112:115]\n\t" \
    "v_max_f32 v43, 0, v43\n\t" \
    "v_mul_f32 v40, v40, v44\n\t" \
    "v_mfma_f32_16x16x32_bf16 v[112:115], v[168:171], v[156:159], v[112:115]\n\t" \
    "s_waitcnt lgkmcnt(1)\n\t" \
    "v_mul_f32 v41, v41, v45\n\t" \
    "v_mfma_f32_16x16x32_bf16 v[68:71], v[172:175], a[240:243], v[68:71]\n\t" \
    "v_mul_f32 v42, v42, v46\n\t" \
    "v_mfma_f32_16x16x32_bf16 v[68:71], v[176:179], v[128:131], v[68:71]\n\t" \
    "s_waitcnt lgkmcnt(0)\n\t" \
    "ds_read_b128 v[184:187], v210 offset:10240\n\t" \
    "v_mul_f32 v43, v43, v47\n\t" \
    "v_mfma_f32_16x16x32_bf16 v[68:71], v[180:183], v[144:147], v[68:71]\n\t" \
    "buffer_load_dwordx4 v211, s[40:43], s48 offen offset:1024 lds\n\t" \
    "v_mfma_f32_16x16x32_bf16 v[84:87], v[172:175], a[244:247], v[84:87]\n\t" \
    "ds_read_b128 v[188:191], v210 offset:11264\n\t" \
    "v_cvt_pk_bf16_f32 v36, v32, v33\n\t" \
    "v_mfma_f32_16x16x32_bf16 v[84:87], v[176:179], v[132:135], v[84:87]\n\t" \
    "ds_read_b128 v[192:195], v210 offset:12288\n\t" \
    "v_cvt_pk_bf16_f32 v37, v34, v35\n\t" \
    "v_mfma_f32_16x16x32_bf16 v[84:87], v[180:183], v[148:151], v[84:87]\n\t" \
    "v_cvt_pk_bf16_f32 v38, v40, v41\n\t" \
    "v_cvt_pk_bf16_f32 v39, v42, v43\n\t" \
    "v_mfma_f32_16x16x32_bf16 v[100:103], v[172:175], a[248:251], v[100:103]\n\t" \
    "s_add_u32 s50, %[pstA], 294912\n\t" \
    "v_mfma_f32_16x16x32_bf16 v[100:103], v[176:179], v[136:139], v[100:103]\n\t" \
    "buffer_store_dwordx4 v[36:39], v211, s[40:43], s50 offen offset:0 sc1\n\t" \
    "v_mfma_f32_16x16x32_bf16 v[100:103], v[180:183], v[152:155], v[100:103]\n\t" \
    "v_max_f32 v48, 0, v48\n\t" \
    "v_mfma_f32_16x16x32_bf16 v[116:119], v[172:175], a[252:255], v[116:119]\n\t" \
    "v_max_f32 v49, 0, v49\n\t" \
    "v_mfma_f32_16x16x32_bf16 v[116:119], v[176:179], v[140:143], v[116:119]\n\t" \
    "v_max_f32 v50, 0, v50\n\t" \
    "v_mfma_f32_16x16x32_bf16 v[116:119], v[180:183], v[156:159], v[116:119]\n\t" \
    "s_waitcnt lgkmcnt(1)\n\t" \
    "v_max_f32 v51, 0, v51\n\t" \
    "v_mfma_f32_16x16x32_bf16 v[72:75], v[184:187], a[240:243], v[72:75]\n\t" \
    "v_mfma_f32_16x16x32_bf16 v[72:75], v[188:191], v[128:131], v[72:75]\n\t" \
    "s_waitcnt lgkmcnt(0)\n\t" \
    "ds_read_b128 v[196:199], v210 offset:13312\n\t" \
    "v_mul_f32 v48, v48, v52\n\t" \
    "v_mfma_f32_16x16x32_bf16 v[72:75], v[192:195], v[144:147], v[72:75]\n\t" \
    "ds_read_b128 v[200:203], v210 offset:14336\n\t" \
    "v_mul_f32 v49, v49, v53\n\t" \
    "v_mfma_f32_16x16x32_bf16 v[88:91], v[184:187], a[244:247], v[88:91]\n\t" \
    "ds_read_b128 v[204:207], v210 offset:15360\n\t" \
    "v_mul_f32 v50, v50, v54\n\t" \
    "v_mfma_f32_16x16x32_bf16 v[88:91], v[188:191], v[132:135], v[88:91]\n\t" \
    "v_mul_f32 v51, v51, v55\n\t" \
    "v_max_f32 v56, 0, v56\n\t" \
    "v_mfma_f32_16x16x32_bf16 v[88:91], v[192:195], v[148:151], v[88:91]\n\t" \
    "buffer_load_dwordx4 v211, s[40:43], s48 offen offset:2048 lds\n\t" \
    "v_mfma_f32_16x16x32_bf16 v[104:107], v[184:187], a[248:251], v[104:107]\n\t" \
    "v_max_f32 v57, 0, v57\n\t" \
    "v_max_f32 v58, 0, v58\n\t" \
    "v_mfma_f32_16x16x32_bf16 v[104:107], v[188:191], v[136:139], v[104:107]\n\t" \
    "v_max_f32 v59, 0, v59\n\t" \
    "v_mfma_f32_16x16x32_bf16 v[104:107], v[192:195], v[152:155], v[104:107]\n\t" \
    "v_mul_f32 v56, v56, v60\n\t" \
    "v_mfma_f32_16x16x32_bf16 v[120:123], v[184:187], a[252:255], v[120:123]\n\t" \
    "v_mul_f32 v57, v57, v61\n\t" \
    "v_mfma_f32_16x16x32_bf16 v[120:123], v[188:191], v[140:143], v[120:123]\n\t" \
    "buffer_load_dwordx4 v211, s[40:43], s48 offen offset:3072 lds\n\t" \
    "v_mfma_f32_16x16x32_bf16 v[120:123], v[192:195], v[156:159], v[120:123]\n\t" \
    "s_waitcnt lgkmcnt(1)\n\t" \
    "v_mul_f32 v58, v58, v62\n\t" \
    "v_mul_f32 v59, v59, v63\n\t" \
    "v_mfma_f32_16x16x32_bf16 v[76:79], v[196:199], a[240:243], v[76:79]\n\t" \
    "s_waitcnt vmcnt(27)\n\t" \
    "s_barrier\n\t" \
    "v_cvt_pk_bf16_f32 v52, v48, v49\n\t" \
    "v_mfma_f32_16x16x32_bf16 v[76:79], v[200:203], v[128:131], v[76:79]\n\t" \
    "s_waitcnt lgkmcnt(0)\n\t" \
    "ds_read_b128 v[160:163], v210 offset:16384\n\t" \
    "v_cvt_pk_bf16_f32 v53, v50, v51\n\t" \
    "v_mfma_f32_16x16x32_bf16 v[76:79], v[204:207], v[144:147], v[76:79]\n\t" \
    "s_add_u32 m0, %[m0base], 98304\n\t" \
    "ds_read_b128 v[164:167], v210 offset:17408\n\t" \
    "v_cvt_pk_bf16_f32 v54, v56, v57\n\t" \
    "v_mfma_f32_16x16x32_bf16 v[92:95], v[196:199], a[244:247], v[92:95]\n\t" \
    "s_add_u32 s48, %[w1off], 262144\n\t" \
    "buffer_load_dwordx4 v211, s[40:43], s48 offen offset:0 lds\n\t" \
    "v_mfma_f32_16x16x32_bf16 v[92:95], v[200:203], v[132:135], v[92:95]\n\t" \
    "ds_read_b128 v[168:171], v210 offset:18432\n\t" \
    "v_cvt_pk_bf16_f32 v55, v58, v59\n\t" \
    "v_mfma_f32_16x16x32_bf16 v[92:95], v[204:207], v[148:151], v[92:95]\n\t" \
    "ds_read_b128 v[172:175], v210 offset:19456\n\t" \
    "v_mfma_f32_16x16x32_bf16 v[108:111], v[196:199], a[248:251], v[108:111]\n\t" \
    "ds_read_b128 v[176:179], v210 offset:20480\n\t" \
    "v_mfma_f32_16x16x32_bf16 v[108:111], v[200:203], v[136:139], v[108:111]\n\t" \
    "ds_read_b128 v[180:183], v210 offset:21504\n\t" \
    "v_mfma_f32_16x16x32_bf16 v[108:111], v[204:207], v[152:155], v[108:111]\n\t" \
    "buffer_store_dwordx4 v[52:55], v211, s[40:43], s50 offen offset:1024 sc1\n\t" \
    "v_mfma_f32_16x16x32_bf16 v[124:127], v[196:199], a[252:255], v[124:127]\n\t" \
    "v_mfma_f32_16x16x32_bf16 v[124:127], v[200:203], v[140:143], v[124:127]\n\t" \
    "v_mfma_f32_16x16x32_bf16 v[124:127], v[204:207], v[156:159], v[124:127]\n\t" \
    "s_waitcnt lgkmcnt(4)\n\t" \
    "v_mfma_f32_16x16x32_bf16 v[0:3], v[160:163], a[0:3], 0\n\t" \
    "v_mfma_f32_16x16x32_bf16 v[0:3], v[164:167], a[16:19], v[0:3]\n\t" \
    "s_waitcnt lgkmcnt(2)\n\t" \
    "v_mfma_f32_16x16x32_bf16 v[0:3], v[168:171], a[32:35], v[0:3]\n\t" \
    "v_mov_b32 v248, -1\n\t" \
    "v_mov_b32 v249, -1\n\t" \
    "v_mfma_f32_16x16x32_bf16 v[0:3], v[172:175], a[48:51], v[0:3]\n\t" \
    "s_waitcnt lgkmcnt(0)\n\t" \
    "v_max_f32 v64, 0, v64\n\t" \
    "v_max_f32 v65, 0, v65\n\t" \
    "v_mfma_f32_16x16x32_bf16 v[0:3], v[176:179], a[64:67], v[0:3]\n\t" \
    "v_max_f32 v66, 0, v66\n\t" \
    "v_mov_b32 v250, -1\n\t" \
    "v_mfma_f32_16x16x32_bf16 v[0:3], v[180:183], a[80:83], v[0:3]\n\t" \
    "buffer_load_dwordx4 v211, s[40:43], s48 offen offset:1024 lds\n\t" \
    "v_mfma_f32_16x16x32_bf16 v[16:19], v[160:163], a[4:7], 0\n\t" \
    "v_max_f32 v67, 0, v67\n\t" \
    "v_mul_f32 v64, v64, v68\n\t" \
    "v_mfma_f32_16x16x32_bf16 v[16:19], v[164:167], a[20:23], v[16:19]\n\t" \
    "v_mul_f32 v65, v65, v69\n\t" \
    "v_mul_f32 v66, v66, v70\n\t" \
    "v_mfma_f32_16x16x32_bf16 v[16:19], v[168:171], a[36:39], v[16:19]\n\t" \
    "v_mul_f32 v67, v67, v71\n\t" \
    "v_max_f32 v72, 0, v72\n\t" \
    "v_mfma_f32_16x16x32_bf16 v[16:19], v[172:175], a[52:55], v[16:19]\n\t" \
    "v_max_f32 v73, 0, v73\n\t" \
    "v_mov_b32 v251, -1\n\t" \
    "v_mfma_f32_16x16x32_bf16 v[16:19], v[176:179], a[68:71], v[16:19]\n\t" \
    "buffer_load_dwordx4 v211, s[40:43], s48 offen offset:2048 lds\n\t" \
    "v_mfma_f32_16x16x32_bf16 v[16:19], v[180:183], a[84:87], v[16:19]\n\t" \
    "v_max_f32 v74, 0, v74\n\t" \
    "v_max_f32 v75, 0, v75\n\t" \
    "v_mfma_f32_16x16x32_bf16 v[32:35], v[160:163], a[8:11], 0\n\t" \
    "v_mul_f32 v72, v72, v76\n\t" \
    "v_mul_f32 v73, v73, v77\n\t" \
    "v_mfma_f32_16x16x32_bf16 v[32:35], v[164:167], a[24:27], v[32:35]\n\t" \
    "ds_read_b128 v[184:187], v210 offset:22528\n\t" \
    "v_mul_f32 v74, v74, v78\n\t" \
    "v_mfma_f32_16x16x32_bf16 v[32:35], v[168:171], a[40:43], v[32:35]\n\t" \
    "ds_read_b128 v[188:191], v210 offset:23552\n\t" \
    "v_mul_f32 v75, v75, v79\n\t" \
    "v_mfma_f32_16x16x32_bf16 v[32:35], v[172:175], a[56:59], v[32:35]\n\t" \
    "buffer_load_dwordx4 v211, s[40:43], s48 offen offset:3072 lds\n\t" \
    "v_mfma_f32_16x16x32_bf16 v[32:35], v[176:179], a[72:75], v[32:35]\n\t" \
    "s_add_u32 m0, %[m0base], 114688\n\t" \
    "ds_read_b128 v[192:195], v210 offset:24576\n\t" \
    "v_cvt_pk_bf16_f32 v68, v64, v65\n\t" \
    "v_mfma_f32_16x16x32_bf16 v[32:35], v[180:183], a[88:91], v[32:35]\n\t" \
    "s_add_u32 s48, %[w1off], 278528\n\t" \
    "ds_read_b128 v[196:199], v210 offset:25600\n\t" \
    "v_cvt_pk_bf16_f32 v69, v66, v67\n\t" \
    "v_mfma_f32_16x16x32_bf16 v[48:51], v[160:163], a[12:15], 0\n\t" \
    "ds_read_b128 v[200:203], v210 offset:26624\n\t" \
    "v_cvt_pk_bf16_f32 v70, v72, v73\n\t" \
    "v_mfma_f32_16x16x32_bf16 v[48:51], v[164:167], a[28:31], v[48:51]\n\t" \
    "ds_read_b128 v[204:207], v210 offset:27648\n\t" \
    "v_cvt_pk_bf16_f32 v71, v74, v75\n\t" \
    "v_mfma_f32_16x16x32_bf16 v[48:51], v[168:171], a[44:47], v[48:51]\n\t" \
    "s_add_u32 s50, %[pstB], 0\n\t" \
    "buffer_store_dwordx4 v[68:71], v211, s[40:43], s50 offen offset:0 sc1\n\t" \
    "v_mfma_f32_16x16x32_bf16 v[48:51], v[172:175], a[60:63], v[48:51]\n\t" \
    "v_max_f32 v80, 0, v80\n\t" \
    "v_max_f32 v81, 0, v81\n\t" \
    "v_mfma_f32_16x16x32_bf16 v[48:51], v[176:179], a[76:79], v[48:51]\n\t" \
    "v_max_f32 v82, 0, v82\n\t" \
    "v_max_f32 v83, 0, v83\n\t" \
    "v_mfma_f32_16x16x32_bf16 v[48:51], v[180:183], a[92:95], v[48:51]\n\t" \
    "s_waitcnt lgkmcnt(4)\n\t" \
    "v_mul_f32 v80, v80, v84\n\t" \
    "v_mul_f32 v81, v81, v85\n\t" \
    "v_mfma_f32_16x16x32_bf16 v[4:7], v[184:187], a[0:3], 0\n\t" \
    "v_mfma_f32_16x16x32_bf16 v[4:7], v[188:191], a[16:19], v[4:7]\n\t" \
    "s_waitcnt lgkmcnt(2)\n\t" \
    "v_mul_f32 v82, v82, v86\n\t" \
    "v_mul_f32 v83, v83, v87\n\t" \
    "v_mfma_f32_16x16x32_bf16 v[4:7], v[192:195], a[32:35], v[4:7]\n\t" \
    "v_max_f32 v88, 0, v88\n\t" \
    "v_max_f32 v89, 0, v89\n\t" \
    "v_mfma_f32_16x16x32_bf16 v[4:7], v[196:199], a[48:51], v[4:7]\n\t" \
    "s_waitcnt lgkmcnt(0)\n\t" \
    "v_max_f32 v90, 0, v90\n\t" \
    "v_max_f32 v91, 0, v91\n\t" \
    "v_mfma_f32_16x16x32_bf16 v[4:7], v[200:203], a[64:67], v[4:7]\n\t" \
    "v_mul_f32 v88, v88, v92\n\t" \
    "v_mul_f32 v89, v89, v93\n\t" \
    "v_mfma_f32_16x16x32_bf16 v[4:7], v[204:207], a[80:83], v[4:7]\n\t" \
    "buffer_load_dwordx4 v211, s[40:43], s48 offen offset:0 lds\n\t" \
    "v_mfma_f32_16x16x32_bf16 v[20:23], v[184:187], a[4:7], 0\n\t" \
    "v_mul_f32 v90, v90, v94\n\t" \
    "v_mul_f32 v91, v91, v95\n\t" \
    "v_mfma_f32_16x16x32_bf16 v[20:23], v[188:191], a[20:23], v[20:23]\n\t" \
    "v_cvt_pk_bf16_f32 v84, v80, v81\n\t" \
    "v_cvt_pk_bf16_f32 v85, v82, v83\n\t" \
    "v_mfma_f32_16x16x32_bf16 v[20:23], v[192:195], a[36:39], v[20:23]\n\t" \
    "v_cvt_pk_bf16_f32 v86, v88, v89\n\t" \
    "v_mfma_f32_16x16x32_bf16 v[20:23], v[196:199], a[52:55], v[20:23]\n\t" \
    "v_cvt_pk_bf16_f32 v87, v90, v91\n\t" \
    "v_mfma_f32_16x16x32_bf16 v[20:23], v[200:203], a[68:71], v[20:23]\n\t" \
    "buffer_load_dwordx4 v211, s[40:43], s48 offen offset:1024 lds\n\t" \
    "v_mfma_f32_16x16x32_bf16 v[20:23], v[204:207], a[84:87], v[20:23]\n\t" \
    "v_mfma_f32_16x16x32_bf16 v[36:39], v[184:187], a[8:11], 0\n\t" \
    "v_mfma_f32_16x16x32_bf16 v[36:39], v[188:191], a[24:27], v[36:39]\n\t" \
    "ds_read_b128 v[160:163], v210 offset:28672\n\t" \
    "v_mfma_f32_16x16x32_bf16 v[36:39], v[192:195], a[40:43], v[36:39]\n\t" \
    "ds_read_b128 v[164:167], v210 offset:29696\n\t" \
    "v_mfma_f32_16x16x32_bf16 v[36:39], v[196:199], a[56:59], v[36:39]\n\t" \
    "buffer_store_dwordx4 v[84:87], v211, s[40:43], s50 offen offset:1024 sc1\n\t" \
    "v_mfma_f32_16x16x32_bf16 v[36:39], v[200:203], a[72:75], v[36:39]\n\t" \
    "ds_read_b128 v[168:171], v210 offset:30720\n\t" \
    "v_max_f32 v96, 0, v96\n\t" \
    "v_mfma_f32_16x16x32_bf16 v[36:39], v[204:207], a[88:91], v[36:39]\n\t" \
    "s_waitcnt vmcnt(32)\n\t" \
    "s_barrier\n\t" \
    "ds_read_b128 v[172:175], v210 offset:31744\n\t" \
    "v_mfma_f32_16x16x32_bf16 v[52:55], v[184:187], a[12:15], 0\n\t" \
    "ds_read_b128 v[176:179], v208 offset:0\n\t" \
    "v_max_f32 v97, 0, v97\n\t" \
    "v_mfma_f32_16x16x32_bf16 v[52:55], v[188:191], a[28:31], v[52:55]\n\t" \
    "ds_read_b128 v[180:183], v208 offset:1024\n\t" \
    "v_max_f32 v98, 0, v98\n\t" \
    "v_mfma_f32_16x16x32_bf16 v[52:55], v[192:195], a[44:47], v[52:55]\n\t" \
    "v_mfma_f32_16x16x32_bf16 v[52:55], v[196:199], a[60:63], v[52:55]\n\t" \
    "v_max_f32 v99, 0, v99\n\t" \
    "v_mul_f32 v96, v96, v100\n\t" \
    "v_mfma_f32_16x16x32_bf16 v[52:55], v[200:203], a[76:79], v[52:55]\n\t" \
    "v_mul_f32 v97, v97, v101\n\t" \
    "v_mul_f32 v98, v98, v102\n\t" \
    "v_mfma_f32_16x16x32_bf16 v[52:55], v[204:207], a[92:95], v[52:55]\n\t" \
    "s_waitcnt lgkmcnt(4)\n\t" \
    "v_mul_f32 v99, v99, v103\n\t" \
    "v_max_f32 v104, 0, v104\n\t" \
    "v_mfma_f32_16x16x32_bf16 v[8:11], v[160:163], a[0:3], 0\n\t" \
    "buffer_load_dwordx4 v211, s[40:43], s48 offen offset:2048 lds\n\t" \
    "v_mfma_f32_16x16x32_bf16 v[8:11], v[164:167], a[16:19], v[8:11]\n\t" \
    "s_waitcnt lgkmcnt(2)\n\t" \
    "v_max_f32 v105, 0, v105\n\t" \
    "v_max_f32 v106, 0, v106\n\t" \
    "v_mfma_f32_16x16x32_bf16 v[8:11], v[168:171], a[32:35], v[8:11]\n\t" \
    "v_max_f32 v107, 0, v107\n\t" \
    "v_mul_f32 v104, v104, v108\n\t" \
    "v_mfma_f32_16x16x32_bf16 v[8:11], v[172:175], a[48:51], v[8:11]\n\t" \
    "s_waitcnt lgkmcnt(0)\n\t" \
    "v_mul_f32 v105, v105, v109\n\t" \
    "v_mul_f32 v106, v106, v110\n\t" \
    "v_mfma_f32_16x16x32_bf16 v[8:11], v[176:179], a[64:67], v[8:11]\n\t" \
    "v_mul_f32 v107, v107, v111\n\t" \
    "v_cvt_pk_bf16_f32 v100, v96, v97\n\t" \
    "v_mfma_f32_16x16x32_bf16 v[8:11], v[180:183], a[80:83], v[8:11]\n\t" \
    "buffer_load_dwordx4 v211, s[40:43], s48 offen offset:3072 lds\n\t" \
    "v_mfma_f32_16x16x32_bf16 v[24:27], v[160:163], a[4:7], 0\n\t" \
    "s_add_u32 m0, %[m0base], 131072\n\t" \
    "v_cvt_pk_bf16_f32 v101, v98, v99\n\t" \
    "v_cvt_pk_bf16_f32 v102, v104, v105\n\t" \
    "v_mfma_f32_16x16x32_bf16 v[24:27], v[164:167], a[20:23], v[24:27]\n\t" \
    "s_add_u32 s48, %[w1off], 294912\n\t" \
    "v_cvt_pk_bf16_f32 v103, v106, v107\n\t" \
    "s_add_u32 s50, %[pstB], 294912\n\t" \
    "v_mfma_f32_16x16x32_bf16 v[24:27], v[168:171], a[36:39], v[24:27]\n\t" \
    "v_mfma_f32_16x16x32_bf16 v[24:27], v[172:175], a[52:55], v[24:27]\n\t" \
    "v_mfma_f32_16x16x32_bf16 v[24:27], v[176:179], a[68:71], v[24:27]\n\t" \
    "buffer_store_dwordx4 v[100:103], v211, s[40:43], s50 offen offset:0 sc1\n\t" \
    "v_mfma_f32_16x16x32_bf16 v[24:27], v[180:183], a[84:87], v[24:27]\n\t" \
    "v_max_f32 v112, 0, v112\n\t" \
    "v_max_f32 v113, 0, v113\n\t" \
    "v_mfma_f32_16x16x32_bf16 v[40:43], v[160:163], a[8:11], 0\n\t" \
    "v_max_f32 v114, 0, v114\n\t" \
    "v_max_f32 v115, 0, v115\n\t" \
    "v_mfma_f32_16x16x32_bf16 v[40:43], v[164:167], a[24:27], v[40:43]\n\t" \
    "ds_read_b128 v[184:187], v208 offset:2048\n\t" \
    "v_mul_f32 v112, v112, v116\n\t" \
    "v_mfma_f32_16x16x32_bf16 v[40:43], v[168:171], a[40:43], v[40:43]\n\t" \
    "ds_read_b128 v[188:191], v208 offset:3072\n\t" \
    "v_mul_f32 v113, v113, v117\n\t" \
    "v_mfma_f32_16x16x32_bf16 v[40:43], v[172:175], a[56:59], v[40:43]\n\t" \
    "v_mfma_f32_16x16x32_bf16 v[40:43], v[176:179], a[72:75], v[40:43]\n\t" \
    "ds_read_b128 v[192:195], v208 offset:4096\n\t" \
    "v_mul_f32 v114, v114, v118\n\t" \
    "v_mfma_f32_16x16x32_bf16 v[40:43], v[180:183], a[88:91], v[40:43]\n\t" \
    "ds_read_b128 v[196:199], v208 offset:5120\n\t" \
    "v_mul_f32 v115, v115, v119\n\t" \
    "v_mfma_f32_16x16x32_bf16 v[56:59], v[160:163], a[12:15], 0\n\t" \
    "ds_read_b128 v[200:203], v208 offset:6144\n\t" \
    "v_max_f32 v120, 0, v120\n\t" \
    "v_mfma_f32_16x16x32_bf16 v[56:59], v[164:167], a[28:31], v[56:59]\n\t" \
    "ds_read_b128 v[204:207], v208 offset:7168\n\t" \
    "v_max_f32 v121, 0, v121\n\t" \
    "v_mfma_f32_16x16x32_bf16 v[56:59], v[168:171], a[44:47], v[56:59]\n\t" \
    "buffer_load_dwordx4 v211, s[40:43], s48 offen offset:0 lds\n\t" \
    "v_mfma_f32_16x16x32_bf16 v[56:59], v[172:175], a[60:63], v[56:59]\n\t" \
    "v_max_f32 v122, 0, v122\n\t" \
    "v_max_f32 v123, 0, v123\n\t" \
    "v_mfma_f32_16x16x32_bf16 v[56:59], v[176:179], a[76:79], v[56:59]\n\t" \
    "v_mul_f32 v120, v120, v124\n\t" \
    "v_mul_f32 v121, v121, v125\n\t" \
    "v_mfma_f32_16x16x32_bf16 v[56:59], v[180:183], a[92:95], v[56:59]\n\t" \
    "s_waitcnt lgkmcnt(4)\n\t" \
    "v_mul_f32 v122, v122, v126\n\t" \
    "v_mul_f32 v123, v123, v127\n\t" \
    "v_mfma_f32_16x16x32_bf16 v[12:15], v[184:187], a[0:3], 0\n\t" \
    "buffer_load_dwordx4 v211, s[40:43], s48 offen offset:1024 lds\n\t" \
    "v_mfma_f32_16x16x32_bf16 v[12:15], v[188:191], a[16:19], v[12:15]\n\t" \
    "s_waitcnt lgkmcnt(2)\n\t" \
    "v_cvt_pk_bf16_f32 v116, v112, v113\n\t" \
    "v_cvt_pk_bf16_f32 v117, v114, v115\n\t" \
    "v_mfma_f32_16x16x32_bf16 v[12:15], v[192:195], a[32:35], v[12:15]\n\t" \
    "v_cvt_pk_bf16_f32 v118, v120, v121\n\t" \
    "v_cvt_pk_bf16_f32 v119, v122, v123\n\t" \
    "v_mfma_f32_16x16x32_bf16 v[12:15], v[196:199], a[48:51], v[12:15]\n\t" \
    "s_waitcnt lgkmcnt(0)\n\t" \
    "v_mfma_f32_16x16x32_bf16 v[12:15], v[200:203], a[64:67], v[12:15]\n\t" \
    "v_mfma_f32_16x16x32_bf16 v[12:15], v[204:207], a[80:83], v[12:15]\n\t" \
    "buffer_store_dwordx4 v[116:119], v211, s[40:43], s50 offen offset:1024 sc1\n\t" \
    "v_mfma_f32_16x16x32_bf16 v[28:31], v[184:187], a[4:7], 0\n\t" \
    "s_add_u32 s54, %[clr], 0\n\t" \
    "v_mfma_f32_16x16x32_bf16 v[28:31], v[188:191], a[20:23], v[28:31]\n\t" \
    "v_lshrrev_b32 v240, 3, v215\n\t" \
    "v_and_b32 v241, 7, v215\n\t" \
    "v_mfma_f32_16x16x32_bf16 v[28:31], v[192:195], a[36:39], v[28:31]\n\t" \
    "v_lshlrev_b32 v240, 15, v240\n\t" \
    "v_lshl_add_u32 v240, v241, 4, v240\n\t" \
    "v_mfma_f32_16x16x32_bf16 v[28:31], v[196:199], a[52:55], v[28:31]\n\t" \
    "v_lshlrev_b32 v241, 5, v241\n\t" \
    "v_cmp_gt_u32 vcc, 16, v215\n\t" \
    "v_mfma_f32_16x16x32_bf16 v[28:31], v[200:203], a[68:71], v[28:31]\n\t" \
    "v_mfma_f32_16x16x32_bf16 v[28:31], v[204:207], a[84:87], v[28:31]\n\t" \
    "v_mov_b32 v242, 0x70000000\n\t" \
    "v_mov_b32 v244, 0\n\t" \
    "v_mfma_f32_16x16x32_bf16 v[44:47], v[184:187], a[8:11], 0\n\t" \
    "v_mov_b32 v245, 0\n\t" \
    "v_cndmask_b32 v242, v242, v240, vcc\n\t" \
    "v_mfma_f32_16x16x32_bf16 v[44:47], v[188:191], a[24:27], v[44:47]\n\t" \
    "s_add_u32 s49, %[stin], 0\n\t" \
    "ds_read_b128 v[160:163], v208 offset:8192\n\t" \
    "v_mfma_f32_16x16x32_bf16 v[44:47], v[192:195], a[40:43], v[44:47]\n\t" \
    "ds_read_b128 v[164:167], v208 offset:9216\n\t" \
    "v_mfma_f32_16x16x32_bf16 v[44:47], v[196:199], a[56:59], v[44:47]\n\t" \
    "buffer_load_dwordx4 v[220:223], v240, s[40:43], s49 offen\n\t" \
    "v_mfma_f32_16x16x32_bf16 v[44:47], v[200:203], a[72:75], v[44:47]\n\t" \
    "s_add_u32 s49, %[stin], 262144\n\t" \
    "ds_read_b128 v[168:171], v208 offset:10240\n\t" \
    "v_mfma_f32_16x16x32_bf16 v[44:47], v[204:207], a[88:91], v[44:47]\n\t" \
    "ds_read_b128 v[172:175], v208 offset:11264\n\t" \
    "v_mfma_f32_16x16x32_bf16 v[60:63], v[184:187], a[12:15], 0\n\t" \
    "ds_read_b128 v[176:179], v208 offset:12288\n\t" \
    "v_mfma_f32_16x16x32_bf16 v[60:63], v[188:191], a[28:31], v[60:63]\n\t" \
    "ds_read_b128 v[180:183], v208 offset:13312\n\t" \
    "v_mfma_f32_16x16x32_bf16 v[60:63], v[192:195], a[44:47], v[60:63]\n\t" \
    "buffer_load_dwordx4 v[224:227], v240, s[40:43], s49 offen\n\t" \
    "v_mfma_f32_16x16x32_bf16 v[60:63], v[196:199], a[60:63], v[60:63]\n\t" \
    "s_add_u32 s49, %[stin], 524288\n\t" \
    "v_mfma_f32_16x16x32_bf16 v[60:63], v[200:203], a[76:79], v[60:63]\n\t" \
    "v_mfma_f32_16x16x32_bf16 v[60:63], v[204:207], a[92:95], v[60:63]\n\t" \
    "s_waitcnt lgkmcnt(4)\n\t" \
    "v_mfma_f32_16x16x32_bf16 v[0:3], v[160:163], a[96:99], v[0:3]\n\t" \
    "buffer_load_dwordx4 v[228:231], v242, s[40:43], s49 offen\n\t" \
    "v_mfma_f32_16x16x32_bf16 v[0:3], v[164:167], a[112:115], v[0:3]\n\t" \
    "s_waitcnt lgkmcnt(2)\n\t" \
    "v_mfma_f32_16x16x32_bf16 v[0:3], v[168:171], a[128:131], v[0:3]\n\t" \
    "v_mfma_f32_16x16x32_bf16 v[0:3], v[172:175], a[144:147], v[0:3]\n\t" \
    "s_waitcnt lgkmcnt(0)\n\t" \
    "v_mfma_f32_16x16x32_bf16 v[0:3], v[176:179], a[160:163], v[0:3]\n\t" \
    "v_mfma_f32_16x16x32_bf16 v[0:3], v[180:183], a[176:179], v[0:3]\n\t" \
    "buffer_store_dwordx4 v[248:251], v211, s[40:43], s54 offen offset:0 sc1\n\t" \
    "v_mfma_f32_16x16x32_bf16 v[16:19], v[160:163], a[100:103], v[16:19]\n\t" \
    "v_mfma_f32_16x16x32_bf16 v[16:19], v[164:167], a[116:119], v[16:19]\n\t" \
    "v_mfma_f32_16x16x32_bf16 v[16:19], v[168:171], a[132:135], v[16:19]\n\t" \
    "v_mfma_f32_16x16x32_bf16 v[16:19], v[172:175], a[148:151], v[16:19]\n\t" \
    "v_mfma_f32_16x16x32_bf16 v[16:19], v[176:179], a[164:167], v[16:19]\n\t" \
    "v_mfma_f32_16x16x32_bf16 v[16:19], v[180:183], a[180:183], v[16:19]\n\t" \
    "v_mfma_f32_16x16x32_bf16 v[32:35], v[160:163], a[104:107], v[32:35]\n\t" \
    "v_mfma_f32_16x16x32_bf16 v[32:35], v[164:167], a[120:123], v[32:35]\n\t" \
    "ds_read_b128 v[184:187], v208 offset:14336\n\t" \
    "v_mfma_f32_16x16x32_bf16 v[32:35], v[168:171], a[136:139], v[32:35]\n\t" \
    "s_waitcnt vmcnt(38)\n\t" \
    "s_barrier\n\t" \
    "ds_read_b128 v[188:191], v208 offset:15360\n\t" \
    "v_mfma_f32_16x16x32_bf16 v[32:35], v[172:175], a[152:155], v[32:35]\n\t" \
    "buffer_load_dwordx4 v211, s[40:43], s48 offen offset:2048 lds\n\t" \
    "v_mfma_f32_16x16x32_bf16 v[32:35], v[176:179], a[168:171], v[32:35]\n\t" \
    "ds_read_b128 v[192:195], v208 offset:16384\n\t" \
    "v_mfma_f32_16x16x32_bf16 v[32:35], v[180:183], a[184:187], v[32:35]\n\t" \
    "ds_read_b128 v[196:199], v208 offset:17408\n\t" \
    "v_mfma_f32_16x16x32_bf16 v[48:51], v[160:163], a[108:111], v[48:51]\n\t" \
    "ds_read_b128 v[200:203], v208 offset:18432\n\t" \
    "v_mfma_f32_16x16x32_bf16 v[48:51], v[164:167], a[124:127], v[48:51]\n\t" \
    "ds_read_b128 v[204:207], v208 offset:19456\n\t" \
    "v_mfma_f32_16x16x32_bf16 v[48:51], v[168:171], a[140:143], v[48:51]\n\t" \
    "buffer_load_dwordx4 v211, s[40:43], s48 offen offset:3072 lds\n\t" \
    "v_mfma_f32_16x16x32_bf16 v[48:51], v[172:175], a[156:159], v[48:51]\n\t" \
    "s_add_u32 m0, %[m0base], 147456\n\t" \
    "s_add_u32 s48, %[w1off], 311296\n\t" \
    "v_mfma_f32_16x16x32_bf16 v[48:51], v[176:179], a[172:175], v[48:51]\n\t" \
    "v_mfma_f32_16x16x32_bf16 v[48:51], v[180:183], a[188:191], v[48:51]\n\t" \
    "s_waitcnt lgkmcnt(4)\n\t" \
    "v_mfma_f32_16x16x32_bf16 v[4:7], v[184:187], a[96:99], v[4:7]\n\t" \
    "buffer_store_dwordx4 v[248:251], v211, s[40:43], s54 offen offset:1024 sc1\n\t" \
    "v_mfma_f32_16x16x32_bf16 v[4:7], v[188:191], a[112:115], v[4:7]\n\t" \
    "s_waitcnt lgkmcnt(2)\n\t" \
    "v_mfma_f32_16x16x32_bf16 v[4:7], v[192:195], a[128:131], v[4:7]\n\t" \
    "v_mfma_f32_16x16x32_bf16 v[4:7], v[196:199], a[144:147], v[4:7]\n\t" \
    "s_waitcnt lgkmcnt(0)\n\t" \
    "v_mfma_f32_16x16x32_bf16 v[4:7], v[200:203], a[160:163], v[4:7]\n\t" \
    "v_mfma_f32_16x16x32_bf16 v[4:7], v[204:207], a[176:179], v[4:7]\n\t" \
    "v_mfma_f32_16x16x32_bf16 v[20:23], v[184:187], a[100:103], v[20:23]\n\t" \
    "v_mfma_f32_16x16x32_bf16 v[20:23], v[188:191], a[116:119], v[20:23]\n\t" \
    "v_mfma_f32_16x16x32_bf16 v[20:23], v[192:195], a[132:135], v[20:23]\n\t" \
    "v_mfma_f32_16x16x32_bf16 v[20:23], v[196:199], a[148:151], v[20:23]\n\t" \
    "v_mfma_f32_16x16x32_bf16 v[20:23], v[200:203], a[164:167], v[20:23]\n\t" \
    "buffer_load_dwordx4 v211, s[40:43], s48 offen offset:0 lds\n\t" \
    "v_mfma_f32_16x16x32_bf16 v[20:23], v[204:207], a[180:183], v[20:23]\n\t" \
    "v_mfma_f32_16x16x32_bf16 v[36:39], v[184:187], a[104:107], v[36:39]\n\t" \
    "v_mfma_f32_16x16x32_bf16 v[36:39], v[188:191], a[120:123], v[36:39]\n\t" \
    "ds_read_b128 v[160:163], v208 offset:20480\n\t" \
    "v_mfma_f32_16x16x32_bf16 v[36:39], v[192:195], a[136:139], v[36:39]\n\t" \
    "ds_read_b128 v[164:167], v208 offset:21504\n\t" \
    "v_mfma_f32_16x16x32_bf16 v[36:39], v[196:199], a[152:155], v[36:39]\n\t" \
    "buffer_load_dwordx4 v211, s[40:43], s48 offen offset:1024 lds\n\t" \
    "v_mfma_f32_16x16x32_bf16 v[36:39], v[200:203], a[168:171], v[36:39]\n\t" \
    "ds_read_b128 v[168:171], v208 offset:22528\n\t" \
    "v_mfma_f32_16x16x32_bf16 v[36:39], v[204:207], a[184:187], v[36:39]\n\t" \
    "ds_read_b128 v[172:175], v208 offset:23552\n\t" \
    "v_mfma_f32_16x16x32_bf16 v[52:55], v[184:187], a[108:111], v[52:55]\n\t" \
    "ds_read_b128 v[176:179], v208 offset:24576\n\t" \
    "v_mfma_f32_16x16x32_bf16 v[52:55], v[188:191], a[124:127], v[52:55]\n\t" \
    "ds_read_b128 v[180:183], v208 offset:25600\n\t" \
    "v_mfma_f32_16x16x32_bf16 v[52:55], v[192:195], a[140:143], v[52:55]\n\t" \
    "buffer_store_dwordx4 v[248:251], v211, s[40:43], s54 offen offset:2048 sc1\n\t" \
    "v_mfma_f32_16x16x32_bf16 v[52:55], v[196:199], a[156:159], v[52:55]\n\t" \
    "v_mfma_f32_16x16x32_bf16 v[52:55], v[200:203], a[172:175], v[52:55]\n\t" \
    "v_mfma_f32_16x16x32_bf16 v[52:55], v[204:207], a[188:191], v[52:55]\n\t" \
    "s_waitcnt lgkmcnt(4)\n\t" \
    "v_mfma_f32_16x16x32_bf16 v[8:11], v[160:163], a[96:99], v[8:11]\n\t" \
    "v_mfma_f32_16x16x32_bf16 v[8:11], v[164:167], a[112:115], v[8:11]\n\t" \
    "s_waitcnt lgkmcnt(2)\n\t" \
    "v_mfma_f32_16x16x32_bf16 v[8:11], v[168:171], a[128:131], v[8:11]\n\t" \
    "v_mfma_f32_16x16x32_bf16 v[8:11], v[172:175], a[144:147], v[8:11]\n\t" \
    "s_waitcnt lgkmcnt(0)\n\t" \
    "v_mfma_f32_16x16x32_bf16 v[8:11], v[176:179], a[160:163], v[8:11]\n\t" \
    "v_mfma_f32_16x16x32_bf16 v[8:11], v[180:183], a[176:179], v[8:11]\n\t" \
    "buffer_load_dwordx4 v211, s[40:43], s48 offen offset:2048 lds\n\t" \
    "v_mfma_f32_16x16x32_bf16 v[24:27], v[160:163], a[100:103], v[24:27]\n\t" \
    "v_mfma_f32_16x16x32_bf16 v[24:27], v[164:167], a[116:119], v[24:27]\n\t" \
    "v_mfma_f32_16x16x32_bf16 v[24:27], v[168:171], a[132:135], v[24:27]\n\t" \
    "v_mfma_f32_16x16x32_bf16 v[24:27], v[172:175], a[148:151], v[24:27]\n\t" \
    "v_mfma_f32_16x16x32_bf16 v[24:27], v[176:179], a[164:167], v[24:27]\n\t" \
    "buffer_load_dwordx4 v211, s[40:43], s48 offen offset:3072 lds\n\t" \
    "v_mfma_f32_16x16x32_bf16 v[24:27], v[180:183], a[180:183], v[24:27]\n\t" \
    "v_mfma_f32_16x16x32_bf16 v[40:43], v[160:163], a[104:107], v[40:43]\n\t" \
    "v_mfma_f32_16x16x32_bf16 v[40:43], v[164:167], a[120:123], v[40:43]\n\t" \
    "ds_read_b128 v[184:187], v208 offset:26624\n\t" \
    "v_mfma_f32_16x16x32_bf16 v[40:43], v[168:171], a[136:139], v[40:43]\n\t" \
    "ds_read_b128 v[188:191], v208 offset:27648\n\t" \
    "v_mfma_f32_16x16x32_bf16 v[40:43], v[172:175], a[152:155], v[40:43]\n\t" \
    "buffer_store_dwordx4 v[248:251], v211, s[40:43], s54 offen offset:3072 sc1\n\t" \
    "v_mfma_f32_16x16x32_bf16 v[40:43], v[176:179], a[168:171], v[40:43]\n\t" \
    "s_add_u32 s54, %[clr], 4096\n\t" \
    "ds_read_b128 v[192:195], v208 offset:28672\n\t" \
    "v_mfma_f32_16x16x32_bf16 v[40:43], v[180:183], a[184:187], v[40:43]\n\t" \
    "ds_read_b128 v[196:199], v208 offset:29696\n\t" \
    "v_mfma_f32_16x16x32_bf16 v[56:59], v[160:163], a[108:111], v[56:59]\n\t" \
    "ds_read_b128 v[200:203], v208 offset:30720\n\t" \
    "v_mfma_f32_16x16x32_bf16 v[56:59], v[164:167], a[124:127], v[56:59]\n\t" \
    "ds_read_b128 v[204:207], v208 offset:31744\n\t" \
    "v_mfma_f32_16x16x32_bf16 v[56:59], v[168:171], a[140:143], v[56:59]\n\t" \
    "v_mfma_f32_16x16x32_bf16 v[56:59], v[172:175], a[156:159], v[56:59]\n\t" \
    "v_mfma_f32_16x16x32_bf16 v[56:59], v[176:179], a[172:175], v[56:59]\n\t" \
    "v_mfma_f32_16x16x32_bf16 v[56:59], v[180:183], a[188:191], v[56:59]\n\t" \
    "s_waitcnt lgkmcnt(4)\n\t" \
    "v_mfma_f32_16x16x32_bf16 v[12:15], v[184:187], a[96:99], v[12:15]\n\t" \
    "v_mfma_f32_16x16x32_bf16 v[12:15], v[188:191], a[112:115], v[12:15]\n\t" \
    "s_waitcnt lgkmcnt(2)\n\t" \
    "v_mfma_f32_16x16x32_bf16 v[12:15], v[192:195], a[128:131], v[12:15]\n\t" \
    "v_mfma_f32_16x16x32_bf16 v[12:15], v[196:199], a[144:147], v[12:15]\n\t" \
    "s_waitcnt lgkmcnt(0)\n\t" \
    "v_mfma_f32_16x16x32_bf16 v[12:15], v[200:203], a[160:163], v[12:15]\n\t" \
    "v_mfma_f32_16x16x32_bf16 v[12:15], v[204:207], a[176:179], v[12:15]\n\t" \
    "v_mfma_f32_16x16x32_bf16 v[28:31], v[184:187], a[100:103], v[28:31]\n\t" \
    "v_mfma_f32_16x16x32_bf16 v[28:31], v[188:191], a[116:119], v[28:31]\n\t" \
    "v_mfma_f32_16x16x32_bf16 v[28:31], v[192:195], a[132:135], v[28:31]\n\t" \
    "v_mfma_f32_16x16x32_bf16 v[28:31], v[196:199], a[148:151], v[28:31]\n\t" \
    "v_mfma_f32_16x16x32_bf16 v[28:31], v[200:203], a[164:167], v[28:31]\n\t" \
    "buffer_store_dwordx4 v[248:251], v211, s[40:43], s54 offen offset:0 sc1\n\t" \
    "v_mfma_f32_16x16x32_bf16 v[28:31], v[204:207], a[180:183], v[28:31]\n\t" \
    "v_mfma_f32_16x16x32_bf16 v[44:47], v[184:187], a[104:107], v[44:47]\n\t" \
    "s_waitcnt vmcnt(44)\n\t" \
    "s_barrier\n\t" \
    "v_mfma_f32_16x16x32_bf16 v[44:47], v[188:191], a[120:123], v[44:47]\n\t" \
    "s_add_u32 m0, %[m0base], 0\n\t" \
    "ds_read_b128 v[160:163], v208 offset:32768\n\t" \
    "s_mov_b32 s48, %[wmix]\n\t" \
    "v_mfma_f32_16x16x32_bf16 v[44:47], v[192:195], a[136:139], v[44:47]\n\t" \
    "ds_read_b128 v[164:167], v208 offset:33792\n\t" \
    "v_mfma_f32_16x16x32_bf16 v[44:47], v[196:199], a[152:155], v[44:47]\n\t" \
    "v_mfma_f32_16x16x32_bf16 v[44:47], v[200:203], a[168:171], v[44:47]\n\t" \
    "ds_read_b128 v[168:171], v208 offset:34816\n\t" \
    "v_mfma_f32_16x16x32_bf16 v[44:47], v[204:207], a[184:187], v[44:47]\n\t" \
    "ds_read_b128 v[172:175], v208 offset:35840\n\t" \
    "v_mfma_f32_16x16x32_bf16 v[60:63], v[184:187], a[108:111], v[60:63]\n\t" \
    "ds_read_b128 v[176:179], v208 offset:36864\n\t" \
    "v_mfma_f32_16x16x32_bf16 v[60:63], v[188:191], a[124:127], v[60:63]\n\t" \
    "ds_read_b128 v[180:183], v208 offset:37888\n\t" \
    "v_mfma_f32_16x16x32_bf16 v[60:63], v[192:195], a[140:143], v[60:63]\n\t" \
    "buffer_load_dwordx4 v211, s[40:43], s48 offen offset:0 lds\n\t" \
    "v_mfma_f32_16x16x32_bf16 v[60:63], v[196:199], a[156:159], v[60:63]\n\t" \
    "v_mfma_f32_16x16x32_bf16 v[60:63], v[200:203], a[172:175], v[60:63]\n\t" \
    "v_mfma_f32_16x16x32_bf16 v[60:63], v[204:207], a[188:191], v[60:63]\n\t" \
    "s_waitcnt lgkmcnt(4)\n\t" \
    "v_mfma_f32_16x16x32_bf16 v[0:3], v[160:163], a[192:195], v[0:3]\n\t" \
    "buffer_load_dwordx4 v211, s[40:43], s48 offen offset:1024 lds\n\t" \
    "v_mfma_f32_16x16x32_bf16 v[0:3], v[164:167], a[208:211], v[0:3]\n\t" \
    "s_waitcnt lgkmcnt(2)\n\t" \
    "v_mfma_f32_16x16x32_bf16 v[0:3], v[168:171], a[224:227], v[0:3]\n\t" \
    "v_mfma_f32_16x16x32_bf16 v[0:3], v[172:175], a[240:243], v[0:3]\n\t" \
    "s_waitcnt lgkmcnt(0)\n\t" \
    "v_mfma_f32_16x16x32_bf16 v[0:3], v[176:179], v[128:131], v[0:3]\n\t" \
    "v_mfma_f32_16x16x32_bf16 v[0:3], v[180:183], v[144:147], v[0:3]\n\t" \
    "buffer_store_dwordx4 v[248:251], v211, s[40:43], s54 offen offset:1024 sc1\n\t" \
    "v_mfma_f32_16x16x32_bf16 v[16:19], v[160:163], a[196:199], v[16:19]\n\t" \
    "v_mfma_f32_16x16x32_bf16 v[16:19], v[164:167], a[212:215], v[16:19]\n\t" \
    "v_mfma_f32_16x16x32_bf16 v[16:19], v[168:171], a[228:231], v[16:19]\n\t" \
    "v_mfma_f32_16x16x32_bf16 v[16:19], v[172:175], a[244:247], v[16:19]\n\t" \
    "v_mfma_f32_16x16x32_bf16 v[16:19], v[176:179], v[132:135], v[16:19]\n\t" \
    "v_mfma_f32_16x16x32_bf16 v[16:19], v[180:183], v[148:151], v[16:19]\n\t" \
    "v_mfma_f32_16x16x32_bf16 v[32:35], v[160:163], a[200:203], v[32:35]\n\t" \
    "v_mfma_f32_16x16x32_bf16 v[32:35], v[164:167], a[216:219], v[32:35]\n\t" \
    "ds_read_b128 v[184:187], v208 offset:38912\n\t" \
    "v_mfma_f32_16x16x32_bf16 v[32:35], v[168:171], a[232:235], v[32:35]\n\t" \
    "ds_read_b128 v[188:191], v208 offset:39936\n\t" \
    "v_mfma_f32_16x16x32_bf16 v[32:35], v[172:175], a[248:251], v[32:35]\n\t" \
    "buffer_load_dwordx4 v211, s[40:43], s48 offen offset:2048 lds\n\t" \
    "v_mfma_f32_16x16x32_bf16 v[32:35], v[176:179], v[136:139], v[32:35]\n\t" \
    "ds_read_b128 v[192:195], v208 offset:40960\n\t" \
    "v_mfma_f32_16x16x32_bf16 v[32:35], v[180:183], v[152:155], v[32:35]\n\t" \
    "ds_read_b128 v[196:199], v208 offset:41984\n\t" \
    "v_mfma_f32_16x16x32_bf16 v[48:51], v[160:163], a[204:207], v[48:51]\n\t" \
    "ds_read_b128 v[200:203], v208 offset:43008\n\t" \
    "v_mfma_f32_16x16x32_bf16 v[48:51], v[164:167], a[220:223], v[48:51]\n\t" \
    "ds_read_b128 v[204:207], v208 offset:44032\n\t" \
    "v_mfma_f32_16x16x32_bf16 v[48:51], v[168:171], a[236:239], v[48:51]\n\t" \
    "buffer_load_dwordx4 v211, s[40:43], s48 offen offset:3072 lds\n\t" \
    "v_mfma_f32_16x16x32_bf16 v[48:51], v[172:175], a[252:255], v[48:51]\n\t" \
    "s_add_u32 m0, %[m0base], 16384\n\t" \
    "s_add_u32 s48, %[w2off], 12288\n\t" \
    "v_mfma_f32_16x16x32_bf16 v[48:51], v[176:179], v[140:143], v[48:51]\n\t" \
    "v_mfma_f32_16x16x32_bf16 v[48:51], v[180:183], v[156:159], v[48:51]\n\t" \
    "s_waitcnt lgkmcnt(4)\n\t" \
    "v_mfma_f32_16x16x32_bf16 v[4:7], v[184:187], a[192:195], v[4:7]\n\t" \
    "buffer_store_dwordx4 v[248:251], v211, s[40:43], s54 offen offset:2048 sc1\n\t" \
    "v_mfma_f32_16x16x32_bf16 v[4:7], v[188:191], a[208:211], v[4:7]\n\t" \
    "s_waitcnt lgkmcnt(2)\n\t" \
    "v_mfma_f32_16x16x32_bf16 v[4:7], v[192:195], a[224:227], v[4:7]\n\t" \
    "v_mfma_f32_16x16x32_bf16 v[4:7], v[196:199], a[240:243], v[4:7]\n\t" \
    "s_waitcnt lgkmcnt(0)\n\t" \
    "v_mfma_f32_16x16x32_bf16 v[4:7], v[200:203], v[128:131], v[4:7]\n\t" \
    "v_mfma_f32_16x16x32_bf16 v[4:7], v[204:207], v[144:147], v[4:7]\n\t" \
    "v_mfma_f32_16x16x32_bf16 v[20:23], v[184:187], a[196:199], v[20:23]\n\t" \
    "v_mfma_f32_16x16x32_bf16 v[20:23], v[188:191], a[212:215], v[20:23]\n\t" \
    "v_mfma_f32_16x16x32_bf16 v[20:23], v[192:195], a[228:231], v[20:23]\n\t" \
    "v_mfma_f32_16x16x32_bf16 v[20:23], v[196:199], a[244:247], v[20:23]\n\t" \
    "v_mfma_f32_16x16x32_bf16 v[20:23], v[200:203], v[132:135], v[20:23]\n\t" \
    "buffer_load_dwordx4 v211, s[40:43], s48 offen offset:0 lds\n\t" \
    "v_mfma_f32_16x16x32_bf16 v[20:23], v[204:207], v[148:151], v[20:23]\n\t" \
    "v_mfma_f32_16x16x32_bf16 v[36:39], v[184:187], a[200:203], v[36:39]\n\t" \
    "v_mfma_f32_16x16x32_bf16 v[36:39], v[188:191], a[216:219], v[36:39]\n\t" \
    "ds_read_b128 v[160:163], v208 offset:45056\n\t" \
    "v_mfma_f32_16x16x32_bf16 v[36:39], v[192:195], a[232:235], v[36:39]\n\t" \
    "ds_read_b128 v[164:167], v208 offset:46080\n\t" \
    "v_mfma_f32_16x16x32_bf16 v[36:39], v[196:199], a[248:251], v[36:39]\n\t" \
    "buffer_load_dwordx4 v211, s[40:43], s48 offen offset:1024 lds\n\t" \
    "v_mfma_f32_16x16x32_bf16 v[36:39], v[200:203], v[136:139], v[36:39]\n\t" \
    "ds_read_b128 v[168:171], v208 offset:47104\n\t" \
    "v_mfma_f32_16x16x32_bf16 v[36:39], v[204:207], v[152:155], v[36:39]\n\t" \
    "s_waitcnt vmcnt(48)\n\t" \
    "s_barrier\n\t" \
    "ds_read_b128 v[172:175], v208 offset:48128\n\t" \
    "v_mfma_f32_16x16x32_bf16 v[52:55], v[184:187], a[204:207], v[52:55]\n\t" \
    "ds_read_b128 v[176:179], v208 offset:49152\n\t" \
    "v_mfma_f32_16x16x32_bf16 v[52:55], v[188:191], a[220:223], v[52:55]\n\t" \
    "ds_read_b128 v[180:183], v208 offset:50176\n\t" \
    "v_mfma_f32_16x16x32_bf16 v[52:55], v[192:195], a[236:239], v[52:55]\n\t" \
    "buffer_load_dwordx4 v211, s[40:43], s48 offen offset:2048 lds\n\t" \
    "v_mfma_f32_16x16x32_bf16 v[52:55], v[196:199], a[252:255], v[52:55]\n\t" \
    "v_mfma_f32_16x16x32_bf16 v[52:55], v[200:203], v[140:143], v[52:55]\n\t" \
    "v_mfma_f32_16x16x32_bf16 v[52:55], v[204:207], v[156:159], v[52:55]\n\t" \
    "s_waitcnt lgkmcnt(4)\n\t" \
    "v_mfma_f32_16x16x32_bf16 v[8:11], v[160:163], a[192:195], v[8:11]\n\t" \
    "buffer_load_dwordx4 v211, s[40:43], s48 offen offset:3072 lds\n\t" \
    "v_mfma_f32_16x16x32_bf16 v[8:11], v[164:167], a[208:211], v[8:11]\n\t" \
    "s_waitcnt lgkmcnt(2)\n\t" \
    "v_mfma_f32_16x16x32_bf16 v[8:11], v[168:171], a[224:227], v[8:11]\n\t" \
    "v_mfma_f32_16x16x32_bf16 v[8:11], v[172:175], a[240:243], v[8:11]\n\t" \
    "s_waitcnt lgkmcnt(0)\n\t" \
    "v_mfma_f32_16x16x32_bf16 v[8:11], v[176:179], v[128:131], v[8:11]\n\t" \
    "v_mfma_f32_16x16x32_bf16 v[8:11], v[180:183], v[144:147], v[8:11]\n\t" \
    "v_mfma_f32_16x16x32_bf16 v[24:27], v[160:163], a[196:199], v[24:27]\n\t" \
    "v_mfma_f32_16x16x32_bf16 v[24:27], v[164:167], a[212:215], v[24:27]\n\t" \
    "v_mfma_f32_16x16x32_bf16 v[24:27], v[168:171], a[228:231], v[24:27]\n\t" \
    "v_mfma_f32_16x16x32_bf16 v[24:27], v[172:175], a[244:247], v[24:27]\n\t" \
    "v_mfma_f32_16x16x32_bf16 v[24:27], v[176:179], v[132:135], v[24:27]\n\t" \
    "v_mfma_f32_16x16x32_bf16 v[24:27], v[180:183], v[148:151], v[24:27]\n\t" \
    "v_mfma_f32_16x16x32_bf16 v[40:43], v[160:163], a[200:203], v[40:43]\n\t" \
    "v_mfma_f32_16x16x32_bf16 v[40:43], v[164:167], a[216:219], v[40:43]\n\t" \
    "ds_read_b128 v[184:187], v208 offset:51200\n\t" \
    "v_mfma_f32_16x16x32_bf16 v[40:43], v[168:171], a[232:235], v[40:43]\n\t" \
    "ds_read_b128 v[188:191], v208 offset:52224\n\t" \
    "v_mfma_f32_16x16x32_bf16 v[40:43], v[172:175], a[248:251], v[40:43]\n\t" \
    "s_waitcnt vmcnt(21)\n\t" \
    "v_mfma_f32_16x16x32_bf16 v[40:43], v[176:179], v[136:139], v[40:43]\n\t" \
    "ds_read_b128 v[192:195], v208 offset:53248\n\t" \
    "v_lshlrev_b32 v246, 16, v220\n\t" \
    "v_mfma_f32_16x16x32_bf16 v[40:43], v[180:183], v[152:155], v[40:43]\n\t" \
    "ds_read_b128 v[196:199], v208 offset:54272\n\t" \
    "v_and_b32 v247, 0xffff0000, v220\n\t" \
    "v_mfma_f32_16x16x32_bf16 v[56:59], v[160:163], a[204:207], v[56:59]\n\t" \
    "ds_read_b128 v[200:203], v208 offset:55296\n\t" \
    "v_add_f32 v244, v244, v246\n\t" \
    "v_mfma_f32_16x16x32_bf16 v[56:59], v[164:167], a[220:223], v[56:59]\n\t" \
    "ds_read_b128 v[204:207], v208 offset:56320\n\t" \
    "v_fmac_f32 v245, v246, v246\n\t" \
    "v_mfma_f32_16x16x32_bf16 v[56:59], v[168:171], a[236:239], v[56:59]\n\t" \
    "v_mfma_f32_16x16x32_bf16 v[56:59], v[172:175], a[252:255], v[56:59]\n\t" \
    "v_add_f32 v244, v244, v247\n\t" \
    "v_mfma_f32_16x16x32_bf16 v[56:59], v[176:179], v[140:143], v[56:59]\n\t" \
    "v_fmac_f32 v245, v247, v247\n\t" \
    "v_mfma_f32_16x16x32_bf16 v[56:59], v[180:183], v[156:159], v[56:59]\n\t" \
    "s_waitcnt lgkmcnt(4)\n\t" \
    "v_lshlrev_b32 v246, 16, v221\n\t" \
    "v_mfma_f32_16x16x32_bf16 v[12:15], v[184:187], a[192:195], v[12:15]\n\t" \
    "v_mfma_f32_16x16x32_bf16 v[12:15], v[188:191], a[208:211], v[12:15]\n\t" \
    "s_waitcnt lgkmcnt(2)\n\t" \
    "v_and_b32 v247, 0xffff0000, v221\n\t" \
    "v_mfma_f32_16x16x32_bf16 v[12:15], v[192:195], a[224:227], v[12:15]\n\t" \
    "v_add_f32 v244, v244, v246\n\t" \
    "v_mfma_f32_16x16x32_bf16 v[12:15], v[196:199], a[240:243], v[12:15]\n\t" \
    "s_waitcnt lgkmcnt(0)\n\t" \
    "v_fmac_f32 v245, v246, v246\n\t" \
    "v_mfma_f32_16x16x32_bf16 v[12:15], v[200:203], v[128:131], v[12:15]\n\t" \
    "v_add_f32 v244, v244, v247\n\t" \
    "v_mfma_f32_16x16x32_bf16 v[12:15], v[204:207], v[144:147], v[12:15]\n\t" \
    "v_mfma_f32_16x16x32_bf16 v[28:31], v[184:187], a[196:199], v[28:31]\n\t" \
    "v_fmac_f32 v245, v247, v247\n\t" \
    "v_mfma_f32_16x16x32_bf16 v[28:31], v[188:191], a[212:215], v[28:31]\n\t" \
    "v_lshlrev_b32 v246, 16, v222\n\t" \
    "v_mfma_f32_16x16x32_bf16 v[28:31], v[192:195], a[228:231], v[28:31]\n\t" \
    "v_and_b32 v247, 0xffff0000, v222\n\t" \
    "v_mfma_f32_16x16x32_bf16 v[28:31], v[196:199], a[244:247], v[28:31]\n\t" \
    "v_add_f32 v244, v244, v246\n\t" \
    "v_mfma_f32_16x16x32_bf16 v[28:31], v[200:203], v[132:135], v[28:31]\n\t" \
    "v_mfma_f32_16x16x32_bf16 v[28:31], v[204:207], v[148:151], v[28:31]\n\t" \
    "v_fmac_f32 v245, v246, v246\n\t" \
    "v_mfma_f32_16x16x32_bf16 v[44:47], v[184:187], a[200:203], v[44:47]\n\t" \
    "v_add_f32 v244, v244, v247\n\t" \
    "v_mfma_f32_16x16x32_bf16 v[44:47], v[188:191], a[216:219], v[44:47]\n\t" \
    "ds_read_b128 v[160:163], v208 offset:57344\n\t" \
    "v_fmac_f32 v245, v247, v247\n\t" \
    "v_mfma_f32_16x16x32_bf16 v[44:47], v[192:195], a[232:235], v[44:47]\n\t" \
    "ds_read_b128 v[164:167], v208 offset:58368\n\t" \
    "v_lshlrev_b32 v246, 16, v223\n\t" \
    "v_mfma_f32_16x16x32_bf16 v[44:47], v[196:199], a[248:251], v[44:47]\n\t" \
    "v_mfma_f32_16x16x32_bf16 v[44:47], v[200:203], v[136:139], v[44:47]\n\t" \
    "ds_read_b128 v[168:171], v208 offset:59392\n\t" \
    "v_and_b32 v247, 0xffff0000, v223\n\t" \
    "v_mfma_f32_16x16x32_bf16 v[44:47], v[204:207], v[152:155], v[44:47]\n\t" \
    "v_add_f32 v244, v244, v246\n\t" \
    "v_mov_b32 v214, 1\n\t" \
    "v_mfma_f32_16x16x32_bf16 v[60:63], v[184:187], a[204:207], v[60:63]\n\t" \
    "v_fmac_f32 v245, v246, v246\n\t" \
    "v_mfma_f32_16x16x32_bf16 v[60:63], v[188:191], a[220:223], v[60:63]\n\t" \
    "v_add_f32 v244, v244, v247\n\t" \
    "v_mfma_f32_16x16x32_bf16 v[60:63], v[192:195], a[236:239], v[60:63]\n\t" \
    "s_mov_b64 exec, 1\n\t" \
    "buffer_atomic_add v214, off, s[40:43], %[flagout]\n\t" \
    "s_mov_b64 exec, -1\n\t" \
    "v_mfma_f32_16x16x32_bf16 v[60:63], v[196:199], a[252:255], v[60:63]\n\t" \
    "v_fmac_f32 v245, v247, v247\n\t" \
    "v_mfma_f32_16x16x32_bf16 v[60:63], v[200:203], v[140:143], v[60:63]\n\t" \
    "v_lshlrev_b32 v246, 16, v224\n\t" \
    "v_mfma_f32_16x16x32_bf16 v[60:63], v[204:207], v[156:159], v[60:63]\n\t" \
    "s_waitcnt lgkmcnt(1)\n\t" \
    "v_and_b32 v247, 0xffff0000, v224\n\t" \
    "v_mfma_f32_16x16x32_bf16 v[64:67], v[160:163], a[0:3], 0\n\t" \
    "v_mfma_f32_16x16x32_bf16 v[64:67], v[164:167], a[16:19], v[64:67]\n\t" \
    "s_waitcnt lgkmcnt(0)\n\t" \
    "ds_read_b128 v[172:175], v208 offset:60416\n\t" \
    "v_add_f32 v244, v244, v246\n\t" \
    "v_mfma_f32_16x16x32_bf16 v[64:67], v[168:171], a[32:35], v[64:67]\n\t" \
    "ds_read_b128 v[176:179], v208 offset:61440\n\t" \
    "v_fmac_f32 v245, v246, v246\n\t" \
    "v_mfma_f32_16x16x32_bf16 v[80:83], v[160:163], a[4:7], 0\n\t" \
    "ds_read_b128 v[180:183], v208 offset:62464\n\t" \
    "v_max_f32 v0, 0, v0\n\t" \
    "v_mfma_f32_16x16x32_bf16 v[80:83], v[164:167], a[20:23], v[80:83]\n\t" \
    "v_max_f32 v1, 0, v1\n\t" \
    "v_max_f32 v2, 0, v2\n\t" \
    "v_mfma_f32_16x16x32_bf16 v[80:83], v[168:171], a[36:39], v[80:83]\n\t" \
    "v_mfma_f32_16x16x32_bf16 v[96:99], v[160:163], a[8:11], 0\n\t" \
    "v_max_f32 v3, 0, v3\n\t" \
    "v_mul_f32 v0, v0, v4\n\t" \
    "v_mfma_f32_16x16x32_bf16 v[96:99], v[164:167], a[24:27], v[96:99]\n\t" \
    "v_mul_f32 v1, v1, v5\n\t" \
    "v_mul_f32 v2, v2, v6\n\t" \
    "v_mfma_f32_16x16x32_bf16 v[96:99], v[168:171], a[40:43], v[96:99]\n\t" \
    "v_mul_f32 v3, v3, v7\n\t" \
    "v_max_f32 v8, 0, v8\n\t" \
    "v_mfma_f32_16x16x32_bf16 v[112:115], v[160:163], a[12:15], 0\n\t" \
    "v_max_f32 v9, 0, v9\n\t" \
    "v_max_f32 v10, 0, v10\n\t" \
    "v_mfma_f32_16x16x32_bf16 v[112:115], v[164:167], a[28:31], v[112:115]\n\t" \
    "v_mfma_f32_16x16x32_bf16 v[112:115], v[168:171], a[44:47], v[112:115]\n\t" \
    "s_waitcnt lgkmcnt(1)\n\t" \
    "v_max_f32 v11, 0, v11\n\t" \
    "v_mul_f32 v8, v8, v12\n\t" \
    "v_mfma_f32_16x16x32_bf16 v[68:71], v[172:175], a[0:3], 0\n\t" \
    "v_mul_f32 v9, v9, v13\n\t" \
    "v_mul_f32 v10, v10, v14\n\t" \
    "v_mfma_f32_16x16x32_bf16 v[68:71], v[176:179], a[16:19], v[68:71]\n\t" \
    "s_waitcnt lgkmcnt(0)\n\t" \
    "ds_read_b128 v[184:187], v208 offset:63488\n\t" \
    "v_mul_f32 v11, v11, v15\n\t" \
    "v_mfma_f32_16x16x32_bf16 v[68:71], v[180:183], a[32:35], v[68:71]\n\t" \
    "s_barrier\n\t" \
    "ds_read_b128 v[188:191], v208 offset:64512\n\t" \
    "v_mfma_f32_16x16x32_bf16 v[84:87], v[172:175], a[4:7], 0\n\t" \
    "s_add_u32 m0, %[m0base], 32768\n\t" \
    "v_mfma_f32_16x16x32_bf16 v[84:87], v[176:179], a[20:23], v[84:87]\n\t" \
    "s_add_u32 s48, %[w2off], 28672\n\t" \
    "ds_read_b128 v[192:195], v209 offset:0\n\t" \
    "v_cvt_pk_bf16_f32 v4, v0, v1\n\t" \
    "v_mfma_f32_16x16x32_bf16 v[84:87], v[180:183], a[36:39], v[84:87]\n\t" \
    "v_cvt_pk_bf16_f32 v5, v2, v3\n\t" \
    "v_cvt_pk_bf16_f32 v6, v8, v9\n\t" \
    "v_mfma_f32_16x16x32_bf16 v[100:103], v[172:175], a[8:11], 0\n\t" \
    "v_cvt_pk_bf16_f32 v7, v10, v11\n\t" \
    "s_add_u32 s50, %[pst1], 0\n\t" \
    "v_mfma_f32_16x16x32_bf16 v[100:103], v[176:179], a[24:27], v[100:103]\n\t" \
    "v_add_f32 v244, v244, v247\n\t" \
    "v_mfma_f32_16x16x32_bf16 v[100:103], v[180:183], a[40:43], v[100:103]\n\t" \
    "buffer_store_dwordx4 v[4:7], v211, s[40:43], s50 offen offset:0 sc1\n\t" \
    "v_mfma_f32_16x16x32_bf16 v[116:119], v[172:175], a[12:15], 0\n\t" \
    "v_max_f32 v16, 0, v16\n\t" \
    "v_max_f32 v17, 0, v17\n\t" \
    "v_mfma_f32_16x16x32_bf16 v[116:119], v[176:179], a[28:31], v[116:119]\n\t" \
    "v_max_f32 v18, 0, v18\n\t" \
    "v_max_f32 v19, 0, v19\n\t" \
    "v_mfma_f32_16x16x32_bf16 v[116:119], v[180:183], a[44:47], v[116:119]\n\t" \
    "s_waitcnt lgkmcnt(1)\n\t" \
    "v_mul_f32 v16, v16, v20\n\t" \
    "v_mul_f32 v17, v17, v21\n\t" \
    "v_mfma_f32_16x16x32_bf16 v[72:75], v[184:187], a[0:3], 0\n\t" \
    "v_mfma_f32_16x16x32_bf16 v[72:75], v[188:191], a[16:19], v[72:75]\n\t" \
    "s_waitcnt lgkmcnt(0)\n\t" \
    "ds_read_b128 v[196:199], v209 offset:1024\n\t" \
    "v_mul_f32 v18, v18, v22\n\t" \
    "v_mfma_f32_16x16x32_bf16 v[72:75], v[192:195], a[32:35], v[72:75]\n\t" \
    "ds_read_b128 v[200:203], v209 offset:2048\n\t" \
    "v_mul_f32 v19, v19, v23\n\t" \
    "v_mfma_f32_16x16x32_bf16 v[88:91], v[184:187], a[4:7], 0\n\t" \
    "ds_read_b128 v[204:207], v209 offset:3072\n\t" \
    "v_max_f32 v24, 0, v24\n\t" \
    "v_mfma_f32_16x16x32_bf16 v[88:91], v[188:191], a[20:23], v[88:91]\n\t" \
    "v_max_f32 v25, 0, v25\n\t" \
    "v_max_f32 v26, 0, v26\n\t" \
    "v_mfma_f32_16x16x32_bf16 v[88:91], v[192:195], a[36:39], v[88:91]\n\t" \
    "buffer_load_dwordx4 v211, s[40:43], s48 offen offset:0 lds\n\t" \
    "v_mfma_f32_16x16x32_bf16 v[104:107], v[184:187], a[8:11], 0\n\t" \
    "v_max_f32 v27, 0, v27\n\t" \
    "v_mul_f32 v24, v24, v28\n\t" \
    "v_mfma_f32_16x16x32_bf16 v[104:107], v[188:191], a[24:27], v[104:107]\n\t" \
    "v_mul_f32 v25, v25, v29\n\t" \
    "v_mul_f32 v26, v26, v30\n\t" \
    "v_mfma_f32_16x16x32_bf16 v[104:107], v[192:195], a[40:43], v[104:107]\n\t" \
    "v_mul_f32 v27, v27, v31\n\t" \
    "v_cvt_pk_bf16_f32 v20, v16, v17\n\t" \
    "v_mfma_f32_16x16x32_bf16 v[120:123], v[184:187], a[12:15], 0\n\t" \
    "v_cvt_pk_bf16_f32 v21, v18, v19\n\t" \
    "v_cvt_pk_bf16_f32 v22, v24, v25\n\t" \
    "v_mfma_f32_16x16x32_bf16 v[120:123], v[188:191], a[28:31], v[120:123]\n\t" \
    "buffer_load_dwordx4 v211, s[40:43], s48 offen offset:1024 lds\n\t" \
    "v_mfma_f32_16x16x32_bf16 v[120:123], v[192:195], a[44:47], v[120:123]\n\t" \
    "s_waitcnt lgkmcnt(1)\n\t" \
    "v_cvt_pk_bf16_f32 v23, v26, v27\n\t" \
    "v_fmac_f32 v245, v247, v247\n\t" \
    "v_mfma_f32_16x16x32_bf16 v[76:79], v[196:199], a[0:3], 0\n\t" \
    "v_lshlrev_b32 v246, 16, v225\n\t" \
    "v_mfma_f32_16x16x32_bf16 v[76:79], v[200:203], a[16:19], v[76:79]\n\t" \
    "s_waitcnt lgkmcnt(0)\n\t" \
    "ds_read_b128 v[160:163], v209 offset:4096\n\t" \
    "v_and_b32 v247, 0xffff0000, v225\n\t" \
    "v_mfma_f32_16x16x32_bf16 v[76:79], v[204:207], a[32:35], v[76:79]\n\t" \
    "ds_read_b128 v[164:167], v209 offset:5120\n\t" \
    "v_add_f32 v244, v244, v246\n\t" \
    "v_mfma_f32_16x16x32_bf16 v[92:95], v[196:199], a[4:7], 0\n\t" \
    "buffer_store_dwordx4 v[20:23], v211, s[40:43], s50 offen offset:1024 sc1\n\t" \
    "v_mfma_f32_16x16x32_bf16 v[92:95], v[200:203], a[20:23], v[92:95]\n\t" \
    "ds_read_b128 v[168:171], v209 offset:6144\n\t" \
    "v_max_f32 v32, 0, v32\n\t" \
    "v_mfma_f32_16x16x32_bf16 v[92:95], v[204:207], a[36:39], v[92:95]\n\t" \
    "v_max_f32 v33, 0, v33\n\t" \
    "v_max_f32 v34, 0, v34\n\t" \
    "v_mfma_f32_16x16x32_bf16 v[108:111], v[196:199], a[8:11], 0\n\t" \
    "v_max_f32 v35, 0, v35\n\t" \
    "v_fmac_f32 v245, v246, v246\n\t" \
    "v_mfma_f32_16x16x32_bf16 v[108:111], v[200:203], a[24:27], v[108:111]\n\t" \
    "v_mul_f32 v32, v32, v36\n\t" \
    "v_mul_f32 v33, v33, v37\n\t" \
    "v_mfma_f32_16x16x32_bf16 v[108:111], v[204:207], a[40:43], v[108:111]\n\t" \
    "v_mfma_f32_16x16x32_bf16 v[124:127], v[196:199], a[12:15], 0\n\t" \
    "v_mul_f32 v34, v34, v38\n\t" \
    "v_mul_f32 v35, v35, v39\n\t" \
    "v_mfma_f32_16x16x32_bf16 v[124:127], v[200:203], a[28:31], v[124:127]\n\t" \
    "v_max_f32 v40, 0, v40\n\t" \
    "v_max_f32 v41, 0, v41\n\t" \
    "v_mfma_f32_16x16x32_bf16 v[124:127], v[204:207], a[44:47], v[124:127]\n\t" \
    "s_waitcnt lgkmcnt(1)\n\t" \
    "v_max_f32 v42, 0, v42\n\t" \
    "v_add_f32 v244, v244, v247\n\t" \
    "v_mfma_f32_16x16x32_bf16 v[64:67], v[160:163], a[48:51], v[64:67]\n\t" \
    "buffer_load_dwordx4 v211, s[40:43], s48 offen offset:2048 lds\n\t" \
    "v_mfma_f32_16x16x32_bf16 v[64:67], v[164:167], a[64:67], v[64:67]\n\t" \
    "s_waitcnt lgkmcnt(0)\n\t" \
    "ds_read_b128 v[172:175], v209 offset:7168\n\t" \
    "v_max_f32 v43, 0, v43\n\t" \
    "v_mfma_f32_16x16x32_bf16 v[64:67], v[168:171], a[80:83], v[64:67]\n\t" \
    "ds_read_b128 v[176:179], v209 offset:8192\n\t" \
    "v_mul_f32 v40, v40, v44\n\t" \
    "v_mfma_f32_16x16x32_bf16 v[80:83], v[160:163], a[52:55], v[80:83]\n\t" \
    "ds_read_b128 v[180:183], v209 offset:9216\n\t" \
    "v_mul_f32 v41, v41, v45\n\t" \
    "v_mfma_f32_16x16x32_bf16 v[80:83], v[164:167], a[68:71], v[80:83]\n\t" \
    "v_mul_f32 v42, v42, v46\n\t" \
    "v_mul_f32 v43, v43, v47\n\t" \
    "v_mfma_f32_16x16x32_bf16 v[80:83], v[168:171], a[84:87], v[80:83]\n\t" \
    "buffer_load_dwordx4 v211, s[40:43], s48 offen offset:3072 lds\n\t" \
    "v_mfma_f32_16x16x32_bf16 v[96:99], v[160:163], a[56:59], v[96:99]\n\t" \
    "v_cvt_pk_bf16_f32 v36, v32, v33\n\t" \
    "v_cvt_pk_bf16_f32 v37, v34, v35\n\t" \
    "v_mfma_f32_16x16x32_bf16 v[96:99], v[164:167], a[72:75], v[96:99]\n\t" \
    "v_cvt_pk_bf16_f32 v38, v40, v41\n\t" \
    "v_cvt_pk_bf16_f32 v39, v42, v43\n\t" \
    "v_mfma_f32_16x16x32_bf16 v[96:99], v[168:171], a[88:91], v[96:99]\n\t" \
    "s_add_u32 s50, %[pst1], 294912\n\t" \
    "v_fmac_f32 v245, v247, v247\n\t" \
    "v_mfma_f32_16x16x32_bf16 v[112:115], v[160:163], a[60:63], v[112:115]\n\t" \
    "v_lshlrev_b32 v246, 16, v226\n\t" \
    "v_mfma_f32_16x16x32_bf16 v[112:115], v[164:167], a[76:79], v[112:115]\n\t" \
    "buffer_store_dwordx4 v[36:39], v211, s[40:43], s50 offen offset:0 sc1\n\t" \
    "v_mfma_f32_16x16x32_bf16 v[112:115], v[168:171], a[92:95], v[112:115]\n\t" \
    "s_waitcnt lgkmcnt(1)\n\t" \
    "v_max_f32 v48, 0, v48\n\t" \
    "v_max_f32 v49, 0, v49\n\t" \
    "v_mfma_f32_16x16x32_bf16 v[68:71], v[172:175], a[48:51], v[68:71]\n\t" \
    "v_max_f32 v50, 0, v50\n\t" \
    "v_max_f32 v51, 0, v51\n\t" \
    "v_mfma_f32_16x16x32_bf16 v[68:71], v[176:179], a[64:67], v[68:71]\n\t" \
    "s_waitcnt lgkmcnt(0)\n\t" \
    "ds_read_b128 v[184:187], v209 offset:10240\n\t" \
    "v_mul_f32 v48, v48, v52\n\t" \
    "v_mfma_f32_16x16x32_bf16 v[68:71], v[180:183], a[80:83], v[68:71]\n\t" \
    "ds_read_b128 v[188:191], v209 offset:11264\n\t" \
    "v_mul_f32 v49, v49, v53\n\t" \
    "v_mfma_f32_16x16x32_bf16 v[84:87], v[172:175], a[52:55], v[84:87]\n\t" \
    "v_mfma_f32_16x16x32_bf16 v[84:87], v[176:179], a[68:71], v[84:87]\n\t" \
    "ds_read_b128 v[192:195], v209 offset:12288\n\t" \
    "v_mul_f32 v50, v50, v54\n\t" \
    "v_mfma_f32_16x16x32_bf16 v[84:87], v[180:183], a[84:87], v[84:87]\n\t" \
    "v_mul_f32 v51, v51, v55\n\t" \
    "v_max_f32 v56, 0, v56\n\t" \
    "v_mfma_f32_16x16x32_bf16 v[100:103], v[172:175], a[56:59], v[100:103]\n\t" \
    "v_max_f32 v57, 0, v57\n\t" \
    "v_max_f32 v58, 0, v58\n\t" \
    "v_mfma_f32_16x16x32_bf16 v[100:103], v[176:179], a[72:75], v[100:103]\n\t" \
    "v_max_f32 v59, 0, v59\n\t" \
    "v_mul_f32 v56, v56, v60\n\t" \
    "v_mfma_f32_16x16x32_bf16 v[100:103], v[180:183], a[88:91], v[100:103]\n\t" \
    "v_mfma_f32_16x16x32_bf16 v[116:119], v[172:175], a[60:63], v[116:119]\n\t" \
    "v_mul_f32 v57, v57, v61\n\t" \
    "v_mul_f32 v58, v58, v62\n\t" \
    "v_mfma_f32_16x16x32_bf16 v[116:119], v[176:179], a[76:79], v[116:119]\n\t" \
    "v_mul_f32 v59, v59, v63\n\t" \
    "v_cvt_pk_bf16_f32 v52, v48, v49\n\t" \
    "v_mfma_f32_16x16x32_bf16 v[116:119], v[180:183], a[92:95], v[116:119]\n\t" \
    "s_waitcnt lgkmcnt(1)\n\t" \
    "v_cvt_pk_bf16_f32 v53, v50, v51\n\t" \
    "v_cvt_pk_bf16_f32 v54, v56, v57\n\t" \
    "v_mfma_f32_16x16x32_bf16 v[72:75], v[184:187], a[48:51], v[72:75]\n\t" \
    "v_mfma_f32_16x16x32_bf16 v[72:75], v[188:191], a[64:67], v[72:75]\n\t" \
    "s_waitcnt lgkmcnt(0)\n\t" \
    "ds_read_b128 v[196:199], v209 offset:13312\n\t" \
    "v_cvt_pk_bf16_f32 v55, v58, v59\n\t" \
    "v_mfma_f32_16x16x32_bf16 v[72:75], v[192:195], a[80:83], v[72:75]\n\t" \
    "ds_read_b128 v[200:203], v209 offset:14336\n\t" \
    "v_and_b32 v247, 0xffff0000, v226\n\t" \
    "v_mfma_f32_16x16x32_bf16 v[88:91], v[184:187], a[52:55], v[88:91]\n\t" \
    "ds_read_b128 v[204:207], v209 offset:15360\n\t" \
    "v_add_f32 v244, v244, v246\n\t" \
    "v_mfma_f32_16x16x32_bf16 v[88:91], v[188:191], a[68:71], v[88:91]\n\t" \
    "v_fmac_f32 v245, v246, v246\n\t" \
    "v_mfma_f32_16x16x32_bf16 v[88:91], v[192:195], a[84:87], v[88:91]\n\t" \
    "buffer_store_dwordx4 v[52:55], v211, s[40:43], s50 offen offset:1024 sc1\n\t" \
    "v_mfma_f32_16x16x32_bf16 v[104:107], v[184:187], a[56:59], v[104:107]\n\t" \
    "v_add_f32 v244, v244, v247\n\t" \
    "v_mfma_f32_16x16x32_bf16 v[104:107], v[188:191], a[72:75], v[104:107]\n\t" \
    "v_fmac_f32 v245, v247, v247\n\t" \
    "v_mfma_f32_16x16x32_bf16 v[104:107], v[192:195], a[88:91], v[104:107]\n\t" \
    "v_lshlrev_b32 v246, 16, v227\n\t" \
    "v_mfma_f32_16x16x32_bf16 v[120:123], v[184:187], a[60:63], v[120:123]\n\t" \
    "v_and_b32 v247, 0xffff0000, v227\n\t" \
    "v_mfma_f32_16x16x32_bf16 v[120:123], v[188:191], a[76:79], v[120:123]\n\t" \
    "v_mfma_f32_16x16x32_bf16 v[120:123], v[192:195], a[92:95], v[120:123]\n\t" \
    "s_waitcnt lgkmcnt(1)\n\t" \
    "v_add_f32 v244, v244, v246\n\t" \
    "v_mfma_f32_16x16x32_bf16 v[76:79], v[196:199], a[48:51], v[76:79]\n\t" \
    "s_barrier\n\t" \
    "v_fmac_f32 v245, v246, v246\n\t" \
    "v_mfma_f32_16x16x32_bf16 v[76:79], v[200:203], a[64:67], v[76:79]\n\t" \
    "s_waitcnt lgkmcnt(0)\n\t" \
    "ds_read_b128 v[160:163], v209 offset:16384\n\t" \
    "v_add_f32 v244, v244, v247\n\t" \
    "v_mfma_f32_16x16x32_bf16 v[76:79], v[204:207], a[80:83], v[76:79]\n\t" \
    "s_add_u32 m0, %[m0base], 49152\n\t" \
    "ds_read_b128 v[164:167], v209 offset:17408\n\t" \
    "v_fmac_f32 v245, v247, v247\n\t" \
    "v_mfma_f32_16x16x32_bf16 v[92:95], v[196:199], a[52:55], v[92:95]\n\t" \
    "s_add_u32 s48, %[w2off], 45056\n\t" \
    "buffer_load_dword v213, off, s[40:43], %[flagin] sc1\n\t" \
    "v_mfma_f32_16x16x32_bf16 v[92:95], v[200:203], a[68:71], v[92:95]\n\t" \
    "ds_read_b128 v[168:171], v209 offset:18432\n\t" \
    "v_lshlrev_b32 v246, 16, v228\n\t" \
    "v_mfma_f32_16x16x32_bf16 v[92:95], v[204:207], a[84:87], v[92:95]\n\t" \
    "v_and_b32 v247, 0xffff0000, v228\n\t" \
    "v_mfma_f32_16x16x32_bf16 v[108:111], v[196:199], a[56:59], v[108:111]\n\t" \
    "v_add_f32 v244, v244, v246\n\t" \
    "v_mfma_f32_16x16x32_bf16 v[108:111], v[200:203], a[72:75], v[108:111]\n\t" \
    "v_fmac_f32 v245, v246, v246\n\t" \
    "v_mfma_f32_16x16x32_bf16 v[108:111], v[204:207], a[88:91], v[108:111]\n\t" \
    "buffer_load_dwordx4 v211, s[40:43], s48 offen offset:0 lds\n\t" \
    "v_mfma_f32_16x16x32_bf16 v[124:127], v[196:199], a[60:63], v[124:127]\n\t" \
    "v_add_f32 v244, v244, v247\n\t" \
    "v_mfma_f32_16x16x32_bf16 v[124:127], v[200:203], a[76:79], v[124:127]\n\t" \
    "v_fmac_f32 v245, v247, v247\n\t" \
    "v_mfma_f32_16x16x32_bf16 v[124:127], v[204:207], a[92:95], v[124:127]\n\t" \
    "s_waitcnt lgkmcnt(1)\n\t" \
    "v_lshlrev_b32 v246, 16, v229\n\t" \
    "v_mfma_f32_16x16x32_bf16 v[64:67], v[160:163], a[96:99], v[64:67]\n\t" \
    "buffer_load_dwordx4 v211, s[40:43], s48 offen offset:1024 lds\n\t" \
    "v_mfma_f32_16x16x32_bf16 v[64:67], v[164:167], a[112:115], v[64:67]\n\t" \
    "s_waitcnt lgkmcnt(0)\n\t" \
    "ds_read_b128 v[172:175], v209 offset:19456\n\t" \
    "v_and_b32 v247, 0xffff0000, v229\n\t" \
    "v_mfma_f32_16x16x32_bf16 v[64:67], v[168:171], a[128:131], v[64:67]\n\t" \
    "ds_read_b128 v[176:179], v209 offset:20480\n\t" \
    "v_add_f32 v244, v244, v246\n\t" \
    "v_mfma_f32_16x16x32_bf16 v[80:83], v[160:163], a[100:103], v[80:83]\n\t" \
    "ds_read_b128 v[180:183], v209 offset:21504\n\t" \
    "v_fmac_f32 v245, v246, v246\n\t" \
    "v_mfma_f32_16x16x32_bf16 v[80:83], v[164:167], a[116:119], v[80:83]\n\t" \
    "v_add_f32 v244, v244, v247\n\t" \
    "v_mfma_f32_16x16x32_bf16 v[80:83], v[168:171], a[132:135], v[80:83]\n\t" \
    "buffer_load_dwordx4 v211, s[40:43], s48 offen offset:2048 lds\n\t" \
    "v_mfma_f32_16x16x32_bf16 v[96:99], v[160:163], a[104:107], v[96:99]\n\t" \
    "v_fmac_f32 v245, v247, v247\n\t" \
    "v_mfma_f32_16x16x32_bf16 v[96:99], v[164:167], a[120:123], v[96:99]\n\t" \
    "v_lshlrev_b32 v246, 16, v230\n\t" \
    "v_mfma_f32_16x16x32_bf16 v[96:99], v[168:171], a[136:139], v[96:99]\n\t" \
    "v_and_b32 v247, 0xffff0000, v230\n\t" \
    "v_mfma_f32_16x16x32_bf16 v[112:115], v[160:163], a[108:111], v[112:115]\n\t" \
    "v_add_f32 v244, v244, v246\n\t" \
    "v_mfma_f32_16x16x32_bf16 v[112:115], v[164:167], a[124:127], v[112:115]\n\t" \
    "buffer_load_dwordx4 v211, s[40:43], s48 offen offset:3072 lds\n\t" \
    "v_mfma_f32_16x16x32_bf16 v[112:115], v[168:171], a[140:143], v[112:115]\n\t" \
    "s_waitcnt lgkmcnt(1)\n\t" \
    "v_fmac_f32 v245, v246, v246\n\t" \
    "v_mfma_f32_16x16x32_bf16 v[68:71], v[172:175], a[96:99], v[68:71]\n\t" \
    "v_add_f32 v244, v244, v247\n\t" \
    "v_mfma_f32_16x16x32_bf16 v[68:71], v[176:179], a[112:115], v[68:71]\n\t" \
    "s_waitcnt lgkmcnt(0)\n\t" \
    "ds_read_b128 v[184:187], v209 offset:22528\n\t" \
    "v_fmac_f32 v245, v247, v247\n\t" \
    "v_mfma_f32_16x16x32_bf16 v[68:71], v[180:183], a[128:131], v[68:71]\n\t" \
    "ds_read_b128 v[188:191], v209 offset:23552\n\t" \
    "v_lshlrev_b32 v246, 16, v231\n\t" \
    "v_mfma_f32_16x16x32_bf16 v[84:87], v[172:175], a[100:103], v[84:87]\n\t" \
    "v_mfma_f32_16x16x32_bf16 v[84:87], v[176:179], a[116:119], v[84:87]\n\t" \
    "ds_read_b128 v[192:195], v209 offset:24576\n\t" \
    "v_and_b32 v247, 0xffff0000, v231\n\t" \
    "v_mfma_f32_16x16x32_bf16 v[84:87], v[180:183], a[132:135], v[84:87]\n\t" \
    "v_add_f32 v244, v244, v246\n\t" \
    "v_mfma_f32_16x16x32_bf16 v[100:103], v[172:175], a[104:107], v[100:103]\n\t" \
    "v_fmac_f32 v245, v246, v246\n\t" \
    "v_mfma_f32_16x16x32_bf16 v[100:103], v[176:179], a[120:123], v[100:103]\n\t" \
    "v_add_f32 v244, v244, v247\n\t" \
    "v_mfma_f32_16x16x32_bf16 v[100:103], v[180:183], a[136:139], v[100:103]\n\t" \
    "v_mfma_f32_16x16x32_bf16 v[116:119], v[172:175], a[108:111], v[116:119]\n\t" \
    "v_fmac_f32 v245, v247, v247\n\t" \
    "v_mfma_f32_16x16x32_bf16 v[116:119], v[176:179], a[124:127], v[116:119]\n\t" \
    "v_add_f32_dpp v244, v244, v244 row_ror:8 row_mask:0xf bank_mask:0xf\n\t" \
    "v_mfma_f32_16x16x32_bf16 v[116:119], v[180:183], a[140:143], v[116:119]\n\t" \
    "s_waitcnt lgkmcnt(1)\n\t" \
    "v_add_f32_dpp v245, v245, v245 row_ror:8 row_mask:0xf bank_mask:0xf\n\t" \
    "v_mfma_f32_16x16x32_bf16 v[72:75], v[184:187], a[96:99], v[72:75]\n\t" \
    "v_mfma_f32_16x16x32_bf16 v[72:75], v[188:191], a[112:115], v[72:75]\n\t" \
    "s_waitcnt lgkmcnt(0)\n\t" \
    "ds_read_b128 v[196:199], v209 offset:25600\n\t" \
    "v_mov_b32 v246, v244\n\t" \
    "v_mfma_f32_16x16x32_bf16 v[72:75], v[192:195], a[128:131], v[72:75]\n\t" \
    "ds_read_b128 v[200:203], v209 offset:26624\n\t" \
    "v_mov_b32 v247, v245\n\t" \
    "v_mfma_f32_16x16x32_bf16 v[88:91], v[184:187], a[100:103], v[88:91]\n\t" \
    "ds_read_b128 v[204:207], v209 offset:27648\n\t" \
    "v_permlane16_swap_b32 v244, v246\n\t" \
    "v_mfma_f32_16x16x32_bf16 v[88:91], v[188:191], a[116:119], v[88:91]\n\t" \
    "v_permlane16_swap_b32 v245, v247\n\t" \
    "v_mfma_f32_16x16x32_bf16 v[88:91], v[192:195], a[132:135], v[88:91]\n\t" \
    "v_mfma_f32_16x16x32_bf16 v[104:107], v[184:187], a[104:107], v[104:107]\n\t" \
    "v_add_f32 v244, v244, v246\n\t" \
    "v_mfma_f32_16x16x32_bf16 v[104:107], v[188:191], a[120:123], v[104:107]\n\t" \
    "v_add_f32 v245, v245, v247\n\t" \
    "v_mfma_f32_16x16x32_bf16 v[104:107], v[192:195], a[136:139], v[104:107]\n\t" \
    "v_mov_b32 v246, v244\n\t" \
    "v_mfma_f32_16x16x32_bf16 v[120:123], v[184:187], a[108:111], v[120:123]\n\t" \
    "v_mov_b32 v247, v245\n\t" \
    "v_mfma_f32_16x16x32_bf16 v[120:123], v[188:191], a[124:127], v[120:123]\n\t" \
    "v_mfma_f32_16x16x32_bf16 v[120:123], v[192:195], a[140:143], v[120:123]\n\t" \
    "s_waitcnt lgkmcnt(1)\n\t" \
    "v_permlane32_swap_b32 v244, v246\n\t" \
    "v_mfma_f32_16x16x32_bf16 v[76:79], v[196:199], a[96:99], v[76:79]\n\t" \
    "v_permlane32_swap_b32 v245, v247\n\t" \
    "v_mfma_f32_16x16x32_bf16 v[76:79], v[200:203], a[112:115], v[76:79]\n\t" \
    "s_waitcnt lgkmcnt(0)\n\t" \
    "ds_read_b128 v[160:163], v209 offset:28672\n\t" \
    "v_add_f32 v244, v244, v246\n\t" \
    "v_mfma_f32_16x16x32_bf16 v[76:79], v[204:207], a[128:131], v[76:79]\n\t" \
    "ds_read_b128 v[164:167], v209 offset:29696\n\t" \
    "v_add_f32 v245, v245, v247\n\t" \
    "v_mfma_f32_16x16x32_bf16 v[92:95], v[196:199], a[100:103], v[92:95]\n\t" \
    "s_mov_b64 exec, 0xff\n\t" \
    "buffer_store_dwordx2 v[244:245], v241, s[40:43], %[stout] offen sc1\n\t" \
    "s_mov_b64 exec, -1\n\t" \
    "v_mfma_f32_16x16x32_bf16 v[92:95], v[200:203], a[116:119], v[92:95]\n\t" \
    "ds_read_b128 v[168:171], v209 offset:30720\n\t" \
    "v_and_b32 v240, 15, v215\n\t" \
    "v_mfma_f32_16x16x32_bf16 v[92:95], v[204:207], a[132:135], v[92:95]\n\t" \
    "v_lshrrev_b32 v241, 4, v215\n\t" \
    "v_mfma_f32_16x16x32_bf16 v[108:111], v[196:199], a[104:107], v[108:111]\n\t" \
    "v_mfma_f32_16x16x32_bf16 v[108:111], v[200:203], a[120:123], v[108:111]\n\t" \
    "v_mfma_f32_16x16x32_bf16 v[108:111], v[204:207], a[136:139], v[108:111]\n\t" \
    "buffer_load_dwordx4 v[220:223], v212, s[40:43], %[hown] offen offset:0\n\t" \
    "v_mfma_f32_16x16x32_bf16 v[124:127], v[196:199], a[108:111], v[124:127]\n\t" \
    "v_mfma_f32_16x16x32_bf16 v[124:127], v[200:203], a[124:127], v[124:127]\n\t" \
    "s_barrier\n\t" \
    "v_mfma_f32_16x16x32_bf16 v[124:127], v[204:207], a[140:143], v[124:127]\n\t" \
    "s_waitcnt lgkmcnt(1)\n\t" \
    "s_add_u32 m0, %[m0base], 65536\n\t" \
    "s_add_u32 s48, %[w2off], 61440\n\t" \
    "v_mfma_f32_16x16x32_bf16 v[0:3], v[160:163], a[0:3], 0\n\t" \
    "buffer_load_dwordx4 v[224:227], v212, s[40:43], %[hown] offen offset:256\n\t" \
    "v_mfma_f32_16x16x32_bf16 v[0:3], v[164:167], a[16:19], v[0:3]\n\t" \
    "s_waitcnt lgkmcnt(0)\n\t" \
    "ds_read_b128 v[172:175], v209 offset:31744\n\t" \
    "v_lshrrev_b32 v242, 1, v241\n\t" \
    "v_mfma_f32_16x16x32_bf16 v[0:3], v[168:171], a[32:35], v[0:3]\n\t" \
    "ds_read_b128 v[176:179], v209 offset:32768\n\t" \
    "v_lshlrev_b32 v242, 15, v242\n\t" \
    "v_mfma_f32_16x16x32_bf16 v[8:11], v[160:163], a[4:7], 0\n\t" \
    "ds_read_b128 v[180:183], v209 offset:33792\n\t" \
    "v_and_b32 v243, 1, v241\n\t" \
    "v_mfma_f32_16x16x32_bf16 v[8:11], v[164:167], a[20:23], v[8:11]\n\t" \
    "v_lshl_add_u32 v242, v243, 3, v242\n\t" \
    "v_lshl_add_u32 v218, v240, 4, v242\n\t" \
    "v_mfma_f32_16x16x32_bf16 v[8:11], v[168:171], a[36:39], v[8:11]\n\t" \
    "buffer_load_dwordx2 v[228:229], v218, s[40:43], %[h8] offen offset:0\n\t" \
    "v_mfma_f32_16x16x32_bf16 v[16:19], v[160:163], a[8:11], 0\n\t" \
    "v_mfma_f32_16x16x32_bf16 v[16:19], v[164:167], a[24:27], v[16:19]\n\t" \
    "v_mfma_f32_16x16x32_bf16 v[16:19], v[168:171], a[40:43], v[16:19]\n\t" \
    "v_mfma_f32_16x16x32_bf16 v[24:27], v[160:163], a[12:15], 0\n\t" \
    "v_mfma_f32_16x16x32_bf16 v[24:27], v[164:167], a[28:31], v[24:27]\n\t" \
    "buffer_load_dwordx2 v[230:231], v218, s[40:43], %[h8] offen offset:256\n\t" \
    "v_mfma_f32_16x16x32_bf16 v[24:27], v[168:171], a[44:47], v[24:27]\n\t" \
    "s_waitcnt lgkmcnt(1)\n\t" \
    "v_mul_u32_u24 v240, 1152, v240\n\t" \
    "v_lshl_add_u32 v219, v241, 4, v240\n\t" \
    "v_mfma_f32_16x16x32_bf16 v[4:7], v[172:175], a[0:3], 0\n\t" \
    "v_lshl_add_u32 v218, v241, 3, v240\n\t" \
    "v_mfma_f32_16x16x32_bf16 v[4:7], v[176:179], a[16:19], v[4:7]\n\t" \
    "s_waitcnt lgkmcnt(0)\n\t" \
    "ds_read_b128 v[184:187], v209 offset:34816\n\t" \
    "v_mfma_f32_16x16x32_bf16 v[4:7], v[180:183], a[32:35], v[4:7]\n\t" \
    "ds_read_b128 v[188:191], v209 offset:35840\n\t" \
    "v_mfma_f32_16x16x32_bf16 v[12:15], v[172:175], a[4:7], 0\n\t" \
    "buffer_load_dwordx4 v211, s[40:43], s48 offen offset:0 lds\n\t" \
    "v_mfma_f32_16x16x32_bf16 v[12:15], v[176:179], a[20:23], v[12:15]\n\t" \
    "ds_read_b128 v[192:195], v209 offset:36864\n\t" \
    "v_mfma_f32_16x16x32_bf16 v[12:15], v[180:183], a[36:39], v[12:15]\n\t" \
    "v_mfma_f32_16x16x32_bf16 v[20:23], v[172:175], a[8:11], 0\n\t" \
    "s_waitcnt vmcnt(10)\n\t" \
    "v_mfma_f32_16x16x32_bf16 v[20:23], v[176:179], a[24:27], v[20:23]\n\t" \
    "v_readfirstlane_b32 s51, v213\n\t" \
    "v_mfma_f32_16x16x32_bf16 v[20:23], v[180:183], a[40:43], v[20:23]\n\t" \
    "s_bfe_u32 s51, s51, 0x80000\n\t" \
    "buffer_load_dwordx4 v211, s[40:43], s48 offen offset:1024 lds\n\t" \
    "v_mfma_f32_16x16x32_bf16 v[28:31], v[172:175], a[12:15], 0\n\t" \
    "s_cmp_ge_u32 s51, 8\n\t" \
    "s_cbranch_scc0 Lsl3_%=\n\t" \
    "Lok1_%=:\n\t" \
    "v_mfma_f32_16x16x32_bf16 v[28:31], v[176:179], a[28:31], v[28:31]\n\t" \
    "v_mfma_f32_16x16x32_bf16 v[28:31], v[180:183], a[44:47], v[28:31]\n\t" \
    "s_waitcnt lgkmcnt(1)\n\t" \
    "v_mfma_f32_16x16x32_bf16 v[64:67], v[184:187], a[144:147], v[64:67]\n\t" \
    "s_add_u32 s49, %[pld], 0\n\t" \
    "buffer_load_dwordx4 a[0:3], v211, s[40:43], s49 offen offset:0 sc1\n\t" \
    "v_mfma_f32_16x16x32_bf16 v[64:67], v[188:191], a[160:163], v[64:67]\n\t" \
    "s_waitcnt lgkmcnt(0)\n\t" \
    "ds_read_b128 v[196:199], v209 offset:37888\n\t" \
    "v_mfma_f32_16x16x32_bf16 v[64:67], v[192:195], a[176:179], v[64:67]\n\t" \
    "ds_read_b128 v[200:203], v209 offset:38912\n\t" \
    "v_mfma_f32_16x16x32_bf16 v[80:83], v[184:187], a[148:151], v[80:83]\n\t" \
    "ds_read_b128 v[204:207], v209 offset:39936\n\t" \
    "v_mfma_f32_16x16x32_bf16 v[80:83], v[188:191], a[164:167], v[80:83]\n\t" \
    "v_mfma_f32_16x16x32_bf16 v[80:83], v[192:195], a[180:183], v[80:83]\n\t" \
    "buffer_load_dwordx4 a[4:7], v211, s[40:43], s49 offen offset:1024 sc1\n\t" \
    "v_mfma_f32_16x16x32_bf16 v[96:99], v[184:187], a[152:155], v[96:99]\n\t" \
    "v_mfma_f32_16x16x32_bf16 v[96:99], v[188:191], a[168:171], v[96:99]\n\t" \
    "v_mfma_f32_16x16x32_bf16 v[96:99], v[192:195], a[184:187], v[96:99]\n\t" \
    "v_mfma_f32_16x16x32_bf16 v[112:115], v[184:187], a[156:159], v[112:115]\n\t" \
    "v_mfma_f32_16x16x32_bf16 v[112:115], v[188:191], a[172:175], v[112:115]\n\t" \
    "buffer_load_dwordx4 a[8:11], v211, s[40:43], s49 offen offset:2048 sc1\n\t" \
    "v_mfma_f32_16x16x32_bf16 v[112:115], v[192:195], a[188:191], v[112:115]\n\t" \
    "s_waitcnt lgkmcnt(1)\n\t" \
    "v_mfma_f32_16x16x32_bf16 v[68:71], v[196:199], a[144:147], v[68:71]\n\t" \
    "v_mfma_f32_16x16x32_bf16 v[68:71], v[200:203], a[160:163], v[68:71]\n\t" \
    "s_waitcnt lgkmcnt(0)\n\t" \
    "ds_read_b128 v[160:163], v209 offset:40960\n\t" \
    "v_mfma_f32_16x16x32_bf16 v[68:71], v[204:207], a[176:179], v[68:71]\n\t" \
    "ds_read_b128 v[164:167], v209 offset:41984\n\t" \
    "v_mfma_f32_16x16x32_bf16 v[84:87], v[196:199], a[148:151], v[84:87]\n\t" \
    "buffer_load_dwordx4 a[12:15], v211, s[40:43], s49 offen offset:3072 sc1\n\t" \
    "v_mfma_f32_16x16x32_bf16 v[84:87], v[200:203], a[164:167], v[84:87]\n\t" \
    "s_add_u32 s49, %[pld], 4096\n\t" \
    "ds_read_b128 v[168:171], v209 offset:43008\n\t" \
    "v_mfma_f32_16x16x32_bf16 v[84:87], v[204:207], a[180:183], v[84:87]\n\t" \
    "v_mfma_f32_16x16x32_bf16 v[100:103], v[196:199], a[152:155], v[100:103]\n\t" \
    "v_mfma_f32_16x16x32_bf16 v[100:103], v[200:203], a[168:171], v[100:103]\n\t" \
    "v_mfma_f32_16x16x32_bf16 v[100:103], v[204:207], a[184:187], v[100:103]\n\t" \
    "buffer_load_dwordx4 a[16:19], v211, s[40:43], s49 offen offset:0 sc1\n\t" \
    "v_mfma_f32_16x16x32_bf16 v[116:119], v[196:199], a[156:159], v[116:119]\n\t" \
    "v_mfma_f32_16x16x32_bf16 v[116:119], v[200:203], a[172:175], v[116:119]\n\t" \
    "v_mfma_f32_16x16x32_bf16 v[116:119], v[204:207], a[188:191], v[116:119]\n\t" \
    "s_waitcnt lgkmcnt(1)\n\t" \
    "v_mfma_f32_16x16x32_bf16 v[72:75], v[160:163], a[144:147], v[72:75]\n\t" \
    "buffer_load_dwordx4 a[20:23], v211, s[40:43], s49 offen offset:1024 sc1\n\t" \
    "v_mfma_f32_16x16x32_bf16 v[72:75], v[164:167], a[160:163], v[72:75]\n\t" \
    "s_waitcnt lgkmcnt(0)\n\t" \
    "ds_read_b128 v[172:175], v209 offset:44032\n\t" \
    "v_mfma_f32_16x16x32_bf16 v[72:75], v[168:171], a[176:179], v[72:75]\n\t" \
    "ds_read_b128 v[176:179], v209 offset:45056\n\t" \
    "v_mfma_f32_16x16x32_bf16 v[88:91], v[160:163], a[148:151], v[88:91]\n\t" \
    "ds_read_b128 v[180:183], v209 offset:46080\n\t" \
    "v_mfma_f32_16x16x32_bf16 v[88:91], v[164:167], a[164:167], v[88:91]\n\t" \
    "v_mfma_f32_16x16x32_bf16 v[88:91], v[168:171], a[180:183], v[88:91]\n\t" \
    "buffer_load_dwordx4 a[24:27], v211, s[40:43], s49 offen offset:2048 sc1\n\t" \
    "v_mfma_f32_16x16x32_bf16 v[104:107], v[160:163], a[152:155], v[104:107]\n\t" \
    "v_mfma_f32_16x16x32_bf16 v[104:107], v[164:167], a[168:171], v[104:107]\n\t" \
    "v_mfma_f32_16x16x32_bf16 v[104:107], v[168:171], a[184:187], v[104:107]\n\t" \
    "v_mfma_f32_16x16x32_bf16 v[120:123], v[160:163], a[156:159], v[120:123]\n\t" \
    "v_mfma_f32_16x16x32_bf16 v[120:123], v[164:167], a[172:175], v[120:123]\n\t" \
    "buffer_load_dwordx4 a[28:31], v211, s[40:43], s49 offen offset:3072 sc1\n\t" \
    "v_mfma_f32_16x16x32_bf16 v[120:123], v[168:171], a[188:191], v[120:123]\n\t" \
    "s_waitcnt lgkmcnt(1)\n\t" \
    "v_mov_b32 v214, 256\n\t" \
    "s_add_u32 s49, %[pld], 8192\n\t" \
    "v_mfma_f32_16x16x32_bf16 v[76:79], v[172:175], a[144:147], v[76:79]\n\t" \
    "v_mfma_f32_16x16x32_bf16 v[76:79], v[176:179], a[160:163], v[76:79]\n\t" \
    "s_waitcnt lgkmcnt(0)\n\t" \
    "ds_read_b128 v[184:187], v209 offset:47104\n\t" \
    "v_mfma_f32_16x16x32_bf16 v[76:79], v[180:183], a[176:179], v[76:79]\n\t" \
    "s_barrier\n\t" \
    "ds_read_b128 v[188:191], v209 offset:48128\n\t" \
    "v_mfma_f32_16x16x32_bf16 v[92:95], v[172:175], a[148:151], v[92:95]\n\t" \
    "s_mov_b64 exec, 1\n\t" \
    "buffer_atomic_add v214, off, s[40:43], %[flagout]\n\t" \
    "s_mov_b64 exec, -1\n\t" \
    "v_mfma_f32_16x16x32_bf16 v[92:95], v[176:179], a[164:167], v[92:95]\n\t" \
    "ds_read_b128 v[192:195], v209 offset:49152\n\t" \
    "v_mfma_f32_16x16x32_bf16 v[92:95], v[180:183], a[180:183], v[92:95]\n\t" \
    "v_mfma_f32_16x16x32_bf16 v[108:111], v[172:175], a[152:155], v[108:111]\n\t" \
    "v_mfma_f32_16x16x32_bf16 v[108:111], v[176:179], a[168:171], v[108:111]\n\t" \
    "v_mov_b32 v214, 1\n\t" \
    "v_mfma_f32_16x16x32_bf16 v[108:111], v[180:183], a[184:187], v[108:111]\n\t" \
    "buffer_load_dwordx4 a[32:35], v211, s[40:43], s49 offen offset:0 sc1\n\t" \
    "v_mfma_f32_16x16x32_bf16 v[124:127], v[172:175], a[156:159], v[124:127]\n\t" \
    "v_mfma_f32_16x16x32_bf16 v[124:127], v[176:179], a[172:175], v[124:127]\n\t" \
    "v_mfma_f32_16x16x32_bf16 v[124:127], v[180:183], a[188:191], v[124:127]\n\t" \
    "s_waitcnt lgkmcnt(1)\n\t" \
    "v_mfma_f32_16x16x32_bf16 v[0:3], v[184:187], a[48:51], v[0:3]\n\t" \
    "s_mov_b64 exec, 1\n\t" \
    "buffer_atomic_add v214, off, s[40:43], %[cflag]\n\t" \
    "s_mov_b64 exec, -1\n\t" \
    "v_mfma_f32_16x16x32_bf16 v[0:3], v[188:191], a[64:67], v[0:3]\n\t" \
    "s_waitcnt lgkmcnt(0)\n\t" \
    "ds_read_b128 v[196:199], v209 offset:50176\n\t" \
    "v_mfma_f32_16x16x32_bf16 v[0:3], v[192:195], a[80:83], v[0:3]\n\t" \
    "ds_read_b128 v[200:203], v209 offset:51200\n\t" \
    "v_mfma_f32_16x16x32_bf16 v[8:11], v[184:187], a[52:55], v[8:11]\n\t" \
    "ds_read_b128 v[204:207], v209 offset:52224\n\t" \
    "v_mfma_f32_16x16x32_bf16 v[8:11], v[188:191], a[68:71], v[8:11]\n\t" \
    "v_mfma_f32_16x16x32_bf16 v[8:11], v[192:195], a[84:87], v[8:11]\n\t" \
    "buffer_load_dwordx4 a[36:39], v211, s[40:43], s49 offen offset:1024 sc1\n\t" \
    "v_mfma_f32_16x16x32_bf16 v[16:19], v[184:187], a[56:59], v[16:19]\n\t" \
    "v_mfma_f32_16x16x32_bf16 v[16:19], v[188:191], a[72:75], v[16:19]\n\t" \
    "v_mfma_f32_16x16x32_bf16 v[16:19], v[192:195], a[88:91], v[16:19]\n\t" \
    "v_mfma_f32_16x16x32_bf16 v[24:27], v[184:187], a[60:63], v[24:27]\n\t" \
    "v_mfma_f32_16x16x32_bf16 v[24:27], v[188:191], a[76:79], v[24:27]\n\t" \
    "buffer_load_dwordx4 a[40:43], v211, s[40:43], s49 offen offset:2048 sc1\n\t" \
    "v_mfma_f32_16x16x32_bf16 v[24:27], v[192:195], a[92:95], v[24:27]\n\t" \
    "s_waitcnt lgkmcnt(1)\n\t" \
    "v_mfma_f32_16x16x32_bf16 v[4:7], v[196:199], a[48:51], v[4:7]\n\t" \
    "v_mfma_f32_16x16x32_bf16 v[4:7], v[200:203], a[64:67], v[4:7]\n\t" \
    "s_waitcnt lgkmcnt(0)\n\t" \
    "ds_read_b128 v[160:163], v209 offset:53248\n\t" \
    "v_mfma_f32_16x16x32_bf16 v[4:7], v[204:207], a[80:83], v[4:7]\n\t" \
    "ds_read_b128 v[164:167], v209 offset:54272\n\t" \
    "v_mfma_f32_16x16x32_bf16 v[12:15], v[196:199], a[52:55], v[12:15]\n\t" \
    "buffer_load_dwordx4 a[44:47], v211, s[40:43], s49 offen offset:3072 sc1\n\t" \
    "v_mfma_f32_16x16x32_bf16 v[12:15], v[200:203], a[68:71], v[12:15]\n\t" \
    "ds_read_b128 v[168:171], v209 offset:55296\n\t" \
    "v_mfma_f32_16x16x32_bf16 v[12:15], v[204:207], a[84:87], v[12:15]\n\t" \
    "v_mfma_f32_16x16x32_bf16 v[20:23], v[196:199], a[56:59], v[20:23]\n\t" \
    "v_mfma_f32_16x16x32_bf16 v[20:23], v[200:203], a[72:75], v[20:23]\n\t" \
    "v_mfma_f32_16x16x32_bf16 v[20:23], v[204:207], a[88:91], v[20:23]\n\t" \
    "buffer_load_dwordx4 v211, s[40:43], s48 offen offset:2048 lds\n\t" \
    "v_mfma_f32_16x16x32_bf16 v[28:31], v[196:199], a[60:63], v[28:31]\n\t" \
    "v_mfma_f32_16x16x32_bf16 v[28:31], v[200:203], a[76:79], v[28:31]\n\t" \
    "v_mfma_f32_16x16x32_bf16 v[28:31], v[204:207], a[92:95], v[28:31]\n\t" \
    "s_waitcnt lgkmcnt(1)\n\t" \
    "v_mfma_f32_16x16x32_bf16 v[64:67], v[160:163], a[192:195], v[64:67]\n\t" \
    "s_add_u32 s49, %[pld], 12288\n\t" \
    "buffer_load_dwordx4 a[48:51], v211, s[40:43], s49 offen offset:0 sc1\n\t" \
    "v_mfma_f32_16x16x32_bf16 v[64:67], v[164:167], a[208:211], v[64:67]\n\t" \
    "s_waitcnt lgkmcnt(0)\n\t" \
    "ds_read_b128 v[172:175], v209 offset:56320\n\t" \
    "v_mfma_f32_16x16x32_bf16 v[64:67], v[168:171], a[224:227], v[64:67]\n\t" \
    "ds_read_b128 v[176:179], v209 offset:57344\n\t" \
    "v_mfma_f32_16x16x32_bf16 v[80:83], v[160:163], a[196:199], v[80:83]\n\t" \
    "ds_read_b128 v[180:183], v209 offset:58368\n\t" \
    "v_mfma_f32_16x16x32_bf16 v[80:83], v[164:167], a[212:215], v[80:83]\n\t" \
    "v_mfma_f32_16x16x32_bf16 v[80:83], v[168:171], a[228:231], v[80:83]\n\t" \
    "buffer_load_dwordx4 a[52:55], v211, s[40:43], s49 offen offset:1024 sc1\n\t" \
    "v_mfma_f32_16x16x32_bf16 v[96:99], v[160:163], a[200:203], v[96:99]\n\t" \
    "v_mfma_f32_16x16x32_bf16 v[96:99], v[164:167], a[216:219], v[96:99]\n\t" \
    "v_mfma_f32_16x16x32_bf16 v[96:99], v[168:171], a[232:235], v[96:99]\n\t" \
    "v_mfma_f32_16x16x32_bf16 v[112:115], v[160:163], a[204:207], v[112:115]\n\t" \
    "v_mfma_f32_16x16x32_bf16 v[112:115], v[164:167], a[220:223], v[112:115]\n\t" \
    "buffer_load_dwordx4 a[56:59], v211, s[40:43], s49 offen offset:2048 sc1\n\t" \
    "v_mfma_f32_16x16x32_bf16 v[112:115], v[168:171], a[236:239], v[112:115]\n\t" \
    "s_waitcnt lgkmcnt(1)\n\t" \
    "v_mfma_f32_16x16x32_bf16 v[68:71], v[172:175], a[192:195], v[68:71]\n\t" \
    "v_mfma_f32_16x16x32_bf16 v[68:71], v[176:179], a[208:211], v[68:71]\n\t" \
    "s_waitcnt lgkmcnt(0)\n\t" \
    "ds_read_b128 v[184:187], v209 offset:59392\n\t" \
    "v_mfma_f32_16x16x32_bf16 v[68:71], v[180:183], a[224:227], v[68:71]\n\t" \
    "ds_read_b128 v[188:191], v209 offset:60416\n\t" \
    "v_mfma_f32_16x16x32_bf16 v[84:87], v[172:175], a[196:199], v[84:87]\n\t" \
    "buffer_load_dwordx4 a[60:63], v211, s[40:43], s49 offen offset:3072 sc1\n\t" \
    "v_mfma_f32_16x16x32_bf16 v[84:87], v[176:179], a[212:215], v[84:87]\n\t" \
    "s_add_u32 s49, %[pld], 16384\n\t" \
    "ds_read_b128 v[192:195], v209 offset:61440\n\t" \
    "v_mfma_f32_16x16x32_bf16 v[84:87], v[180:183], a[228:231], v[84:87]\n\t" \
    "v_mfma_f32_16x16x32_bf16 v[100:103], v[172:175], a[200:203], v[100:103]\n\t" \
    "v_mfma_f32_16x16x32_bf16 v[100:103], v[176:179], a[216:219], v[100:103]\n\t" \
    "v_mfma_f32_16x16x32_bf16 v[100:103], v[180:183], a[232:235], v[100:103]\n\t" \
    "buffer_load_dword v213, off, s[40:43], %[flagin] sc1\n\t" \
    "v_mfma_f32_16x16x32_bf16 v[116:119], v[172:175], a[204:207], v[116:119]\n\t" \
    "v_mfma_f32_16x16x32_bf16 v[116:119], v[176:179], a[220:223], v[116:119]\n\t" \
    "v_mfma_f32_16x16x32_bf16 v[116:119], v[180:183], a[236:239], v[116:119]\n\t" \
    "s_waitcnt lgkmcnt(1)\n\t" \
    "v_mfma_f32_16x16x32_bf16 v[72:75], v[184:187], a[192:195], v[72:75]\n\t" \
    "buffer_load_dwordx4 v211, s[40:43], s48 offen offset:3072 lds\n\t" \
    "v_mfma_f32_16x16x32_bf16 v[72:75], v[188:191], a[208:211], v[72:75]\n\t" \
    "s_waitcnt lgkmcnt(0)\n\t" \
    "ds_read_b128 v[196:199], v209 offset:62464\n\t" \
    "s_add_u32 m0, %[m0base], 81920\n\t" \
    "v_mfma_f32_16x16x32_bf16 v[72:75], v[192:195], a[224:227], v[72:75]\n\t" \
    "s_add_u32 s48, %[w2off], 77824\n\t" \
    "ds_read_b128 v[200:203], v209 offset:63488\n\t" \
    "v_mfma_f32_16x16x32_bf16 v[88:91], v[184:187], a[196:199], v[88:91]\n\t" \
    "ds_read_b128 v[204:207], v209 offset:64512\n\t" \
    "v_mfma_f32_16x16x32_bf16 v[88:91], v[188:191], a[212:215], v[88:91]\n\t" \
    "v_mfma_f32_16x16x32_bf16 v[88:91], v[192:195], a[228:231], v[88:91]\n\t" \
    "buffer_load_dwordx4 a[64:67], v211, s[40:43], s49 offen offset:0 sc1\n\t" \
    "v_mfma_f32_16x16x32_bf16 v[104:107], v[184:187], a[200:203], v[104:107]\n\t" \
    "v_mfma_f32_16x16x32_bf16 v[104:107], v[188:191], a[216:219], v[104:107]\n\t" \
    "v_mfma_f32_16x16x32_bf16 v[104:107], v[192:195], a[232:235], v[104:107]\n\t" \
    "v_mfma_f32_16x16x32_bf16 v[120:123], v[184:187], a[204:207], v[120:123]\n\t" \
    "v_mfma_f32_16x16x32_bf16 v[120:123], v[188:191], a[220:223], v[120:123]\n\t" \
    "buffer_load_dwordx4 a[68:71], v211, s[40:43], s49 offen offset:1024 sc1\n\t" \
    "v_mfma_f32_16x16x32_bf16 v[120:123], v[192:195], a[236:239], v[120:123]\n\t" \
    "s_waitcnt lgkmcnt(1)\n\t" \
    "v_mfma_f32_16x16x32_bf16 v[76:79], v[196:199], a[192:195], v[76:79]\n\t" \
    "s_barrier\n\t" \
    "v_mfma_f32_16x16x32_bf16 v[76:79], v[200:203], a[208:211], v[76:79]\n\t" \
    "s_waitcnt lgkmcnt(0)\n\t" \
    "ds_read_b128 v[160:163], v210 offset:0\n\t" \
    "v_mfma_f32_16x16x32_bf16 v[76:79], v[204:207], a[224:227], v[76:79]\n\t" \
    "ds_read_b128 v[164:167], v210 offset:1024\n\t" \
    "v_mfma_f32_16x16x32_bf16 v[92:95], v[196:199], a[196:199], v[92:95]\n\t" \
    "buffer_load_dwordx4 a[72:75], v211, s[40:43], s49 offen offset:2048 sc1\n\t" \
    "v_mfma_f32_16x16x32_bf16 v[92:95], v[200:203], a[212:215], v[92:95]\n\t" \
    "ds_read_b128 v[168:171], v210 offset:2048\n\t" \
    "v_mfma_f32_16x16x32_bf16 v[92:95], v[204:207], a[228:231], v[92:95]\n\t" \
    "v_mfma_f32_16x16x32_bf16 v[108:111], v[196:199], a[200:203], v[108:111]\n\t" \
    "v_mfma_f32_16x16x32_bf16 v[108:111], v[200:203], a[216:219], v[108:111]\n\t" \
    "v_mfma_f32_16x16x32_bf16 v[108:111], v[204:207], a[232:235], v[108:111]\n\t" \
    "buffer_load_dwordx4 a[76:79], v211, s[40:43], s49 offen offset:3072 sc1\n\t" \
    "v_mfma_f32_16x16x32_bf16 v[124:127], v[196:199], a[204:207], v[124:127]\n\t" \
    "s_add_u32 s49, %[pld], 20480\n\t" \
    "v_mfma_f32_16x16x32_bf16 v[124:127], v[200:203], a[220:223], v[124:127]\n\t" \
    "v_mfma_f32_16x16x32_bf16 v[124:127], v[204:207], a[236:239], v[124:127]\n\t" \
    "s_waitcnt lgkmcnt(1)\n\t" \
    "v_mfma_f32_16x16x32_bf16 v[0:3], v[160:163], a[96:99], v[0:3]\n\t" \
    "buffer_load_dwordx4 a[80:83], v211, s[40:43], s49 offen offset:0 sc1\n\t" \
    "v_mfma_f32_16x16x32_bf16 v[0:3], v[164:167], a[112:115], v[0:3]\n\t" \
    "s_waitcnt lgkmcnt(0)\n\t" \
    "ds_read_b128 v[172:175], v210 offset:3072\n\t" \
    "v_mfma_f32_16x16x32_bf16 v[0:3], v[168:171], a[128:131], v[0:3]\n\t" \
    "ds_read_b128 v[176:179], v210 offset:4096\n\t" \
    "v_mfma_f32_16x16x32_bf16 v[8:11], v[160:163], a[100:103], v[8:11]\n\t" \
    "ds_read_b128 v[180:183], v210 offset:5120\n\t" \
    "v_mfma_f32_16x16x32_bf16 v[8:11], v[164:167], a[116:119], v[8:11]\n\t" \
    "v_mfma_f32_16x16x32_bf16 v[8:11], v[168:171], a[132:135], v[8:11]\n\t" \
    "buffer_load_dwordx4 a[84:87], v211, s[40:43], s49 offen offset:1024 sc1\n\t" \
    "v_mfma_f32_16x16x32_bf16 v[16:19], v[160:163], a[104:107], v[16:19]\n\t" \
    "v_mfma_f32_16x16x32_bf16 v[16:19], v[164:167], a[120:123], v[16:19]\n\t" \
    "v_mfma_f32_16x16x32_bf16 v[16:19], v[168:171], a[136:139], v[16:19]\n\t" \
    "v_mfma_f32_16x16x32_bf16 v[24:27], v[160:163], a[108:111], v[24:27]\n\t" \
    "v_mfma_f32_16x16x32_bf16 v[24:27], v[164:167], a[124:127], v[24:27]\n\t" \
    "buffer_load_dwordx4 a[88:91], v211, s[40:43], s49 offen offset:2048 sc1\n\t" \
    "v_mfma_f32_16x16x32_bf16 v[24:27], v[168:171], a[140:143], v[24:27]\n\t" \
    "s_waitcnt lgkmcnt(1)\n\t" \
    "v_mfma_f32_16x16x32_bf16 v[4:7], v[172:175], a[96:99], v[4:7]\n\t" \
    "v_mfma_f32_16x16x32_bf16 v[4:7], v[176:179], a[112:115], v[4:7]\n\t" \
    "s_waitcnt lgkmcnt(0)\n\t" \
    "ds_read_b128 v[184:187], v210 offset:6144\n\t" \
    "v_mfma_f32_16x16x32_bf16 v[4:7], v[180:183], a[128:131], v[4:7]\n\t" \
    "ds_read_b128 v[188:191], v210 offset:7168\n\t" \
    "v_mfma_f32_16x16x32_bf16 v[12:15], v[172:175], a[100:103], v[12:15]\n\t" \
    "buffer_load_dwordx4 a[92:95], v211, s[40:43], s49 offen offset:3072 sc1\n\t" \
    "v_mfma_f32_16x16x32_bf16 v[12:15], v[176:179], a[116:119], v[12:15]\n\t" \
    "ds_read_b128 v[192:195], v210 offset:8192\n\t" \
    "v_mfma_f32_16x16x32_bf16 v[12:15], v[180:183], a[132:135], v[12:15]\n\t" \
    "v_mfma_f32_16x16x32_bf16 v[20:23], v[172:175], a[104:107], v[20:23]\n\t" \
    "v_mfma_f32_16x16x32_bf16 v[20:23], v[176:179], a[120:123], v[20:23]\n\t" \
    "v_mfma_f32_16x16x32_bf16 v[20:23], v[180:183], a[136:139], v[20:23]\n\t" \
    "buffer_load_dwordx4 v211, s[40:43], s48 offen offset:0 lds\n\t" \
    "v_mfma_f32_16x16x32_bf16 v[28:31], v[172:175], a[108:111], v[28:31]\n\t" \
    "v_mfma_f32_16x16x32_bf16 v[28:31], v[176:179], a[124:127], v[28:31]\n\t" \
    "v_mfma_f32_16x16x32_bf16 v[28:31], v[180:183], a[140:143], v[28:31]\n\t" \
    "s_waitcnt lgkmcnt(1)\n\t" \
    "v_mfma_f32_16x16x32_bf16 v[64:67], v[184:187], a[240:243], v[64:67]\n\t" \
    "s_add_u32 s49, %[pld], 24576\n\t" \
    "buffer_load_dwordx4 v211, s[40:43], s48 offen offset:1024 lds\n\t" \
    "v_mfma_f32_16x16x32_bf16 v[64:67], v[188:191], v[128:131], v[64:67]\n\t" \
    "s_waitcnt lgkmcnt(0)\n\t" \
    "ds_read_b128 v[196:199], v210 offset:9216\n\t" \
    "v_mfma_f32_16x16x32_bf16 v[64:67], v[192:195], v[144:147], v[64:67]\n\t" \
    "ds_read_b128 v[200:203], v210 offset:10240\n\t" \
    "v_mfma_f32_16x16x32_bf16 v[80:83], v[184:187], a[244:247], v[80:83]\n\t" \
    "ds_read_b128 v[204:207], v210 offset:11264\n\t" \
    "v_mfma_f32_16x16x32_bf16 v[80:83], v[188:191], v[132:135], v[80:83]\n\t" \
    "v_mfma_f32_16x16x32_bf16 v[80:83], v[192:195], v[148:151], v[80:83]\n\t" \
    "buffer_load_dwordx4 v211, s[40:43], s48 offen offset:2048 lds\n\t" \
    "v_mfma_f32_16x16x32_bf16 v[96:99], v[184:187], a[248:251], v[96:99]\n\t" \
    "v_mfma_f32_16x16x32_bf16 v[96:99], v[188:191], v[136:139], v[96:99]\n\t" \
    "v_mfma_f32_16x16x32_bf16 v[96:99], v[192:195], v[152:155], v[96:99]\n\t" \
    "v_mfma_f32_16x16x32_bf16 v[112:115], v[184:187], a[252:255], v[112:115]\n\t" \
    "v_mfma_f32_16x16x32_bf16 v[112:115], v[188:191], v[140:143], v[112:115]\n\t" \
    "buffer_load_dwordx4 v211, s[40:43], s48 offen offset:3072 lds\n\t" \
    "v_mfma_f32_16x16x32_bf16 v[112:115], v[192:195], v[156:159], v[112:115]\n\t" \
    "s_waitcnt lgkmcnt(1)\n\t" \
    "s_add_u32 m0, %[m0base], 98304\n\t" \
    "s_add_u32 s48, %[w2off], 94208\n\t" \
    "v_mfma_f32_16x16x32_bf16 v[68:71], v[196:199], a[240:243], v[68:71]\n\t" \
    "v_mfma_f32_16x16x32_bf16 v[68:71], v[200:203], v[128:131], v[68:71]\n\t" \
    "s_waitcnt lgkmcnt(0)\n\t" \
    "ds_read_b128 v[160:163], v210 offset:12288\n\t" \
    "v_mfma_f32_16x16x32_bf16 v[68:71], v[204:207], v[144:147], v[68:71]\n\t" \
    "ds_read_b128 v[164:167], v210 offset:13312\n\t" \
    "v_mfma_f32_16x16x32_bf16 v[84:87], v[196:199], a[244:247], v[84:87]\n\t" \
    "buffer_load_dwordx4 v211, s[40:43], s48 offen offset:0 lds\n\t" \
    "v_mfma_f32_16x16x32_bf16 v[84:87], v[200:203], v[132:135], v[84:87]\n\t" \
    "ds_read_b128 v[168:171], v210 offset:14336\n\t" \
    "v_mfma_f32_16x16x32_bf16 v[84:87], v[204:207], v[148:151], v[84:87]\n\t" \
    "v_mfma_f32_16x16x32_bf16 v[100:103], v[196:199], a[248:251], v[100:103]\n\t" \
    "v_mfma_f32_16x16x32_bf16 v[100:103], v[200:203], v[136:139], v[100:103]\n\t" \
    "v_mfma_f32_16x16x32_bf16 v[100:103], v[204:207], v[152:155], v[100:103]\n\t" \
    "buffer_load_dwordx4 v211, s[40:43], s48 offen offset:1024 lds\n\t" \
    "v_mfma_f32_16x16x32_bf16 v[116:119], v[196:199], a[252:255], v[116:119]\n\t" \
    "v_mfma_f32_16x16x32_bf16 v[116:119], v[200:203], v[140:143], v[116:119]\n\t" \
    "s_barrier\n\t" \
    "v_mfma_f32_16x16x32_bf16 v[116:119], v[204:207], v[156:159], v[116:119]\n\t" \
    "s_waitcnt lgkmcnt(1)\n\t" \
    "s_waitcnt vmcnt(15)\n\t" \
    "v_mfma_f32_16x16x32_bf16 v[72:75], v[160:163], a[240:243], v[72:75]\n\t" \
    "buffer_load_dwordx4 v211, s[40:43], s48 offen offset:2048 lds\n\t" \
    "v_mfma_f32_16x16x32_bf16 v[72:75], v[164:167], v[128:131], v[72:75]\n\t" \
    "s_waitcnt lgkmcnt(0)\n\t" \
    "ds_read_b128 v[172:175], v210 offset:15360\n\t" \
    "v_readfirstlane_b32 s51, v213\n\t" \
    "v_mfma_f32_16x16x32_bf16 v[72:75], v[168:171], v[144:147], v[72:75]\n\t" \
    "s_bfe_u32 s51, s51, 0x80008\n\t" \
    "ds_read_b128 v[176:179], v210 offset:16384\n\t" \
    "v_mfma_f32_16x16x32_bf16 v[88:91], v[160:163], a[244:247], v[88:91]\n\t" \
    "ds_read_b128 v[180:183], v210 offset:17408\n\t" \
    "s_cmp_ge_u32 s51, 8\n\t" \
    "s_cbranch_scc0 Lsl6_%=\n\t" \
    "Lok4_%=:\n\t" \
    "v_mfma_f32_16x16x32_bf16 v[88:91], v[164:167], v[132:135], v[88:91]\n\t" \
    "v_mfma_f32_16x16x32_bf16 v[88:91], v[168:171], v[148:151], v[88:91]\n\t" \
    "buffer_load_dword v213, off, s[40:43], %[cflag] sc1\n\t" \
    "v_mfma_f32_16x16x32_bf16 v[104:107], v[160:163], a[248:251], v[104:107]\n\t" \
    "v_mfma_f32_16x16x32_bf16 v[104:107], v[164:167], v[136:139], v[104:107]\n\t" \
    "v_mfma_f32_16x16x32_bf16 v[104:107], v[168:171], v[152:155], v[104:107]\n\t" \
    "v_mfma_f32_16x16x32_bf16 v[120:123], v[160:163], a[252:255], v[120:123]\n\t" \
    "v_mfma_f32_16x16x32_bf16 v[120:123], v[164:167], v[140:143], v[120:123]\n\t" \
    "buffer_load_dwordx4 v211, s[40:43], s48 offen offset:3072 lds\n\t" \
    "v_mfma_f32_16x16x32_bf16 v[120:123], v[168:171], v[156:159], v[120:123]\n\t" \
    "s_waitcnt lgkmcnt(1)\n\t" \
    "s_add_u32 m0, %[m0base], 114688\n\t" \
    "s_add_u32 s48, %[w2off], 110592\n\t" \
    "v_mfma_f32_16x16x32_bf16 v[76:79], v[172:175], a[240:243], v[76:79]\n\t" \
    "v_mfma_f32_16x16x32_bf16 v[76:79], v[176:179], v[128:131], v[76:79]\n\t" \
    "s_waitcnt lgkmcnt(0)\n\t" \
    "ds_read_b128 v[184:187], v210 offset:18432\n\t" \
    "v_mfma_f32_16x16x32_bf16 v[76:79], v[180:183], v[144:147], v[76:79]\n\t" \
    "ds_read_b128 v[188:191], v210 offset:19456\n\t" \
    "v_mfma_f32_16x16x32_bf16 v[92:95], v[172:175], a[244:247], v[92:95]\n\t" \
    "buffer_load_dwordx4 a[96:99], v211, s[40:43], s49 offen offset:0 sc1\n\t" \
    "v_mfma_f32_16x16x32_bf16 v[92:95], v[176:179], v[132:135], v[92:95]\n\t" \
    "ds_read_b128 v[192:195], v210 offset:20480\n\t" \
    "v_mfma_f32_16x16x32_bf16 v[92:95], v[180:183], v[148:151], v[92:95]\n\t" \
    "v_mfma_f32_16x16x32_bf16 v[108:111], v[172:175], a[248:251], v[108:111]\n\t" \
    "v_mfma_f32_16x16x32_bf16 v[108:111], v[176:179], v[136:139], v[108:111]\n\t" \
    "v_mfma_f32_16x16x32_bf16 v[108:111], v[180:183], v[152:155], v[108:111]\n\t" \
    "buffer_load_dwordx4 a[100:103], v211, s[40:43], s49 offen offset:1024 sc1\n\t" \
    "v_mfma_f32_16x16x32_bf16 v[124:127], v[172:175], a[252:255], v[124:127]\n\t" \
    "v_mfma_f32_16x16x32_bf16 v[124:127], v[176:179], v[140:143], v[124:127]\n\t" \
    "v_mfma_f32_16x16x32_bf16 v[124:127], v[180:183], v[156:159], v[124:127]\n\t" \
    "s_waitcnt lgkmcnt(1)\n\t" \
    "v_mfma_f32_16x16x32_bf16 v[0:3], v[184:187], a[144:147], v[0:3]\n\t" \
    "buffer_load_dwordx4 a[104:107], v211, s[40:43], s49 offen offset:2048 sc1\n\t" \
    "v_mfma_f32_16x16x32_bf16 v[0:3], v[188:191], a[160:163], v[0:3]\n\t" \
    "s_waitcnt lgkmcnt(0)\n\t" \
    "ds_read_b128 v[196:199], v210 offset:21504\n\t" \
    "v_mfma_f32_16x16x32_bf16 v[0:3], v[192:195], a[176:179], v[0:3]\n\t" \
    "ds_read_b128 v[200:203], v210 offset:22528\n\t" \
    "v_mfma_f32_16x16x32_bf16 v[8:11], v[184:187], a[148:151], v[8:11]\n\t" \
    "ds_read_b128 v[204:207], v210 offset:23552\n\t" \
    "v_max_f32 v64, 0, v64\n\t" \
    "v_mfma_f32_16x16x32_bf16 v[8:11], v[188:191], a[164:167], v[8:11]\n\t" \
    "v_max_f32 v65, 0, v65\n\t" \
    "v_max_f32 v66, 0, v66\n\t" \
    "v_mfma_f32_16x16x32_bf16 v[8:11], v[192:195], a[180:183], v[8:11]\n\t" \
    "buffer_load_dwordx4 a[108:111], v211, s[40:43], s49 offen offset:3072 sc1\n\t" \
    "v_mfma_f32_16x16x32_bf16 v[16:19], v[184:187], a[152:155], v[16:19]\n\t" \
    "s_add_u32 s49, %[pld], 28672\n\t" \
    "v_max_f32 v67, 0, v67\n\t" \
    "v_mul_f32 v64, v64, v68\n\t" \
    "v_mfma_f32_16x16x32_bf16 v[16:19], v[188:191], a[168:171], v[16:19]\n\t" \
    "v_mul_f32 v65, v65, v69\n\t" \
    "v_mul_f32 v66, v66, v70\n\t" \
    "v_mfma_f32_16x16x32_bf16 v[16:19], v[192:195], a[184:187], v[16:19]\n\t" \
    "v_mul_f32 v67, v67, v71\n\t" \
    "v_max_f32 v72, 0, v72\n\t" \
    "v_mfma_f32_16x16x32_bf16 v[24:27], v[184:187], a[156:159], v[24:27]\n\t" \
    "v_max_f32 v73, 0, v73\n\t" \
    "v_max_f32 v74, 0, v74\n\t" \
    "v_mfma_f32_16x16x32_bf16 v[24:27], v[188:191], a[172:175], v[24:27]\n\t" \
    "buffer_load_dwordx4 a[112:115], v211, s[40:43], s49 offen offset:0 sc1\n\t" \
    "v_mfma_f32_16x16x32_bf16 v[24:27], v[192:195], a[188:191], v[24:27]\n\t" \
    "s_waitcnt lgkmcnt(1)\n\t" \
    "v_max_f32 v75, 0, v75\n\t" \
    "v_mul_f32 v72, v72, v76\n\t" \
    "v_mfma_f32_16x16x32_bf16 v[4:7], v[196:199], a[144:147], v[4:7]\n\t" \
    "v_mul_f32 v73, v73, v77\n\t" \
    "v_mul_f32 v74, v74, v78\n\t" \
    "v_mfma_f32_16x16x32_bf16 v[4:7], v[200:203], a[160:163], v[4:7]\n\t" \
    "s_waitcnt lgkmcnt(0)\n\t" \
    "ds_read_b128 v[160:163], v210 offset:24576\n\t" \
    "v_mul_f32 v75, v75, v79\n\t" \
    "v_mfma_f32_16x16x32_bf16 v[4:7], v[204:207], a[176:179], v[4:7]\n\t" \
    "ds_read_b128 v[164:167], v210 offset:25600\n\t" \
    "v_cvt_pk_bf16_f32 v68, v64, v65\n\t" \
    "v_mfma_f32_16x16x32_bf16 v[12:15], v[196:199], a[148:151], v[12:15]\n\t" \
    "buffer_load_dwordx4 a[116:119], v211, s[40:43], s49 offen offset:1024 sc1\n\t" \
    "v_mfma_f32_16x16x32_bf16 v[12:15], v[200:203], a[164:167], v[12:15]\n\t" \
    "ds_read_b128 v[168:171], v210 offset:26624\n\t" \
    "v_cvt_pk_bf16_f32 v69, v66, v67\n\t" \
    "v_mfma_f32_16x16x32_bf16 v[12:15], v[204:207], a[180:183], v[12:15]\n\t" \
    "v_cvt_pk_bf16_f32 v70, v72, v73\n\t" \
    "v_cvt_pk_bf16_f32 v71, v74, v75\n\t" \
    "v_mfma_f32_16x16x32_bf16 v[20:23], v[196:199], a[152:155], v[20:23]\n\t" \
    "s_add_u32 s50, %[pst2], 0\n\t" \
    "v_mfma_f32_16x16x32_bf16 v[20:23], v[200:203], a[168:171], v[20:23]\n\t" \
    "v_mfma_f32_16x16x32_bf16 v[20:23], v[204:207], a[184:187], v[20:23]\n\t" \
    "buffer_store_dwordx4 v[68:71], v211, s[40:43], s50 offen offset:0 sc1\n\t" \
    "v_mfma_f32_16x16x32_bf16 v[28:31], v[196:199], a[156:159], v[28:31]\n\t" \
    "v_max_f32 v80, 0, v80\n\t" \
    "v_max_f32 v81, 0, v81\n\t" \
    "v_mfma_f32_16x16x32_bf16 v[28:31], v[200:203], a[172:175], v[28:31]\n\t" \
    "v_max_f32 v82, 0, v82\n\t" \
    "v_max_f32 v83, 0, v83\n\t" \
    "v_mfma_f32_16x16x32_bf16 v[28:31], v[204:207], a[188:191], v[28:31]\n\t" \
    "s_waitcnt lgkmcnt(1)\n\t" \
    "v_mul_f32 v80, v80, v84\n\t" \
    "v_mul_f32 v81, v81, v85\n\t" \
    "v_mfma_f32_16x16x32_bf16 v[0:3], v[160:163], a[192:195], v[0:3]\n\t" \
    "v_mfma_f32_16x16x32_bf16 v[0:3], v[164:167], a[208:211], v[0:3]\n\t" \
    "s_waitcnt lgkmcnt(0)\n\t" \
    "ds_read_b128 v[172:175], v210 offset:27648\n\t" \
    "v_mul_f32 v82, v82, v86\n\t" \
    "v_mfma_f32_16x16x32_bf16 v[0:3], v[168:171], a[224:227], v[0:3]\n\t" \
    "ds_read_b128 v[176:179], v210 offset:28672\n\t" \
    "v_mul_f32 v83, v83, v87\n\t" \
    "v_mfma_f32_16x16x32_bf16 v[8:11], v[160:163], a[196:199], v[8:11]\n\t" \
    "ds_read_b128 v[180:183], v210 offset:29696\n\t" \
    "v_max_f32 v88, 0, v88\n\t" \
    "v_mfma_f32_16x16x32_bf16 v[8:11], v[164:167], a[212:215], v[8:11]\n\t" \
    "v_max_f32 v89, 0, v89\n\t" \
    "v_max_f32 v90, 0, v90\n\t" \
    "v_mfma_f32_16x16x32_bf16 v[8:11], v[168:171], a[228:231], v[8:11]\n\t" \
    "buffer_load_dwordx4 a[120:123], v211, s[40:43], s49 offen offset:2048 sc1\n\t" \
    "v_mfma_f32_16x16x32_bf16 v[16:19], v[160:163], a[200:203], v[16:19]\n\t" \
    "v_max_f32 v91, 0, v91\n\t" \
    "v_mul_f32 v88, v88, v92\n\t" \
    "v_mfma_f32_16x16x32_bf16 v[16:19], v[164:167], a[216:219], v[16:19]\n\t" \
    "v_mul_f32 v89, v89, v93\n\t" \
    "v_mul_f32 v90, v90, v94\n\t" \
    "v_mfma_f32_16x16x32_bf16 v[16:19], v[168:171], a[232:235], v[16:19]\n\t" \
    "v_mul_f32 v91, v91, v95\n\t" \
    "v_cvt_pk_bf16_f32 v84, v80, v81\n\t" \
    "v_mfma_f32_16x16x32_bf16 v[24:27], v[160:163], a[204:207], v[24:27]\n\t" \
    "v_cvt_pk_bf16_f32 v85, v82, v83\n\t" \
    "v_cvt_pk_bf16_f32 v86, v88, v89\n\t" \
    "v_mfma_f32_16x16x32_bf16 v[24:27], v[164:167], a[220:223], v[24:27]\n\t" \
    "buffer_load_dwordx4 a[124:127], v211, s[40:43], s49 offen offset:3072 sc1\n\t" \
    "v_mfma_f32_16x16x32_bf16 v[24:27], v[168:171], a[236:239], v[24:27]\n\t" \
    "s_waitcnt lgkmcnt(1)\n\t" \
    "v_cvt_pk_bf16_f32 v87, v90, v91\n\t" \
    "s_add_u32 s49, %[pld], 32768\n\t" \
    "v_mfma_f32_16x16x32_bf16 v[4:7], v[172:175], a[192:195], v[4:7]\n\t" \
    "v_mfma_f32_16x16x32_bf16 v[4:7], v[176:179], a[208:211], v[4:7]\n\t" \
    "s_waitcnt lgkmcnt(0)\n\t" \
    "ds_read_b128 v[184:187], v210 offset:30720\n\t" \
    "v_mfma_f32_16x16x32_bf16 v[4:7], v[180:183], a[224:227], v[4:7]\n\t" \
    "s_barrier\n\t" \
    "ds_read_b128 v[188:191], v210 offset:31744\n\t" \
    "v_mfma_f32_16x16x32_bf16 v[12:15], v[172:175], a[196:199], v[12:15]\n\t" \
    "buffer_store_dwordx4 v[84:87], v211, s[40:43], s50 offen offset:1024 sc1\n\t" \
    "v_mfma_f32_16x16x32_bf16 v[12:15], v[176:179], a[212:215], v[12:15]\n\t" \
    "ds_read_b128 v[192:195], v208 offset:0\n\t" \
    "v_max_f32 v96, 0, v96\n\t" \
    "v_mfma_f32_16x16x32_bf16 v[12:15], v[180:183], a[228:231], v[12:15]\n\t" \
    "v_max_f32 v97, 0, v97\n\t" \
    "v_max_f32 v98, 0, v98\n\t" \
    "v_mfma_f32_16x16x32_bf16 v[20:23], v[172:175], a[200:203], v[20:23]\n\t" \
    "v_max_f32 v99, 0, v99\n\t" \
    "v_mul_f32 v96, v96, v100\n\t" \
    "v_mfma_f32_16x16x32_bf16 v[20:23], v[176:179], a[216:219], v[20:23]\n\t" \
    "v_mul_f32 v97, v97, v101\n\t" \
    "v_mul_f32 v98, v98, v102\n\t" \
    "v_mfma_f32_16x16x32_bf16 v[20:23], v[180:183], a[232:235], v[20:23]\n\t" \
    "v_mfma_f32_16x16x32_bf16 v[28:31], v[172:175], a[204:207], v[28:31]\n\t" \
    "v_mul_f32 v99, v99, v103\n\t" \
    "v_max_f32 v104, 0, v104\n\t" \
    "v_mfma_f32_16x16x32_bf16 v[28:31], v[176:179], a[220:223], v[28:31]\n\t" \
    "v_max_f32 v105, 0, v105\n\t" \
    "v_max_f32 v106, 0, v106\n\t" \
    "v_mfma_f32_16x16x32_bf16 v[28:31], v[180:183], a[236:239], v[28:31]\n\t" \
    "s_waitcnt lgkmcnt(1)\n\t" \
    "v_max_f32 v107, 0, v107\n\t" \
    "v_mul_f32 v104, v104, v108\n\t" \
    "v_mfma_f32_16x16x32_bf16 v[0:3], v[184:187], a[240:243], v[0:3]\n\t" \
    "buffer_load_dwordx4 v211, s[40:43], s48 offen offset:0 lds\n\t" \
    "v_mfma_f32_16x16x32_bf16 v[0:3], v[188:191], v[128:131], v[0:3]\n\t" \
    "s_waitcnt lgkmcnt(0)\n\t" \
    "ds_read_b128 v[196:199], v208 offset:1024\n\t" \
    "v_mul_f32 v105, v105, v109\n\t" \
    "v_mfma_f32_16x16x32_bf16 v[0:3], v[192:195], v[144:147], v[0:3]\n\t" \
    "ds_read_b128 v[200:203], v208 offset:2048\n\t" \
    "v_mul_f32 v106, v106, v110\n\t" \
    "v_mfma_f32_16x16x32_bf16 v[8:11], v[184:187], a[244:247], v[8:11]\n\t" \
    "ds_read_b128 v[204:207], v208 offset:3072\n\t" \
    "v_mul_f32 v107, v107, v111\n\t" \
    "v_mfma_f32_16x16x32_bf16 v[8:11], v[188:191], v[132:135], v[8:11]\n\t" \
    "v_cvt_pk_bf16_f32 v100, v96, v97\n\t" \
    "v_cvt_pk_bf16_f32 v101, v98, v99\n\t" \
    "v_mfma_f32_16x16x32_bf16 v[8:11], v[192:195], v[148:151], v[8:11]\n\t" \
    "buffer_load_dwordx4 v211, s[40:43], s48 offen offset:1024 lds\n\t" \
    "v_mfma_f32_16x16x32_bf16 v[16:19], v[184:187], a[248:251], v[16:19]\n\t" \
    "v_cvt_pk_bf16_f32 v102, v104, v105\n\t" \
    "v_cvt_pk_bf16_f32 v103, v106, v107\n\t" \
    "v_mfma_f32_16x16x32_bf16 v[16:19], v[188:191], v[136:139], v[16:19]\n\t" \
    "s_add_u32 s50, %[pst2], 294912\n\t" \
    "v_mfma_f32_16x16x32_bf16 v[16:19], v[192:195], v[152:155], v[16:19]\n\t" \
    "s_waitcnt vmcnt(13)\n\t" \
    "v_mfma_f32_16x16x32_bf16 v[24:27], v[184:187], a[252:255], v[24:27]\n\t" \
    "v_readfirstlane_b32 s51, v213\n\t" \
    "v_mfma_f32_16x16x32_bf16 v[24:27], v[188:191], v[140:143], v[24:27]\n\t" \
    "s_bfe_u32 s51, s51, 0x80000\n\t" \
    "buffer_store_dwordx4 v[100:103], v211, s[40:43], s50 offen offset:0 sc1\n\t" \
    "v_mfma_f32_16x16x32_bf16 v[24:27], v[192:195], v[156:159], v[24:27]\n\t" \
    "s_waitcnt lgkmcnt(1)\n\t" \
    "v_max_f32 v112, 0, v112\n\t" \
    "v_max_f32 v113, 0, v113\n\t" \
    "v_mfma_f32_16x16x32_bf16 v[4:7], v[196:199], a[240:243], v[4:7]\n\t" \
    "v_max_f32 v114, 0, v114\n\t" \
    "v_max_f32 v115, 0, v115\n\t" \
    "v_mfma_f32_16x16x32_bf16 v[4:7], v[200:203], v[128:131], v[4:7]\n\t" \
    "s_waitcnt lgkmcnt(0)\n\t" \
    "ds_read_b128 v[160:163], v208 offset:4096\n\t" \
    "ds_read_b128 v[164:167], v208 offset:5120\n\t" \
    "v_mfma_f32_16x16x32_bf16 v[4:7], v[204:207], v[144:147], v[4:7]\n\t" \
    "v_mul_f32 v112, v112, v116\n\t" \
    "v_mul_f32 v113, v113, v117\n\t" \
    "v_mfma_f32_16x16x32_bf16 v[12:15], v[196:199], a[244:247], v[12:15]\n\t" \
    "v_mfma_f32_16x16x32_bf16 v[12:15], v[200:203], v[132:135], v[12:15]\n\t" \
    "ds_read_b128 v[168:171], v208 offset:6144\n\t" \
    "ds_read_b128 v[172:175], v208 offset:7168\n\t" \
    "v_mfma_f32_16x16x32_bf16 v[12:15], v[204:207], v[148:151], v[12:15]\n\t" \
    "v_mul_f32 v114, v114, v118\n\t" \
    "v_mul_f32 v115, v115, v119\n\t" \
    "v_mfma_f32_16x16x32_bf16 v[20:23], v[196:199], a[248:251], v[20:23]\n\t" \
    "v_max_f32 v120, 0, v120\n\t" \
    "v_max_f32 v121, 0, v121\n\t" \
    "v_mfma_f32_16x16x32_bf16 v[20:23], v[200:203], v[136:139], v[20:23]\n\t" \
    "v_max_f32 v122, 0, v122\n\t" \
    "v_max_f32 v123, 0, v123\n\t" \
    "v_mfma_f32_16x16x32_bf16 v[20:23], v[204:207], v[152:155], v[20:23]\n\t" \
    "buffer_load_dwordx4 v211, s[40:43], s48 offen offset:2048 lds\n\t" \
    "v_mfma_f32_16x16x32_bf16 v[28:31], v[196:199], a[252:255], v[28:31]\n\t" \
    "ds_read_b128 v[176:179], v208 offset:8192\n\t" \
    "ds_read_b128 v[180:183], v208 offset:9216\n\t" \
    "v_mfma_f32_16x16x32_bf16 v[28:31], v[200:203], v[140:143], v[28:31]\n\t" \
    "v_mul_f32 v120, v120, v124\n\t" \
    "v_mul_f32 v121, v121, v125\n\t" \
    "v_mfma_f32_16x16x32_bf16 v[28:31], v[204:207], v[156:159], v[28:31]\n\t" \
    "s_waitcnt lgkmcnt(4)\n\t" \
    "ds_read_b128 v[184:187], v208 offset:10240\n\t" \
    "ds_read_b128 v[188:191], v208 offset:11264\n\t" \
    "v_mfma_f32_16x16x32_bf16 v[32:35], v[160:163], a[0:3], 0\n\t" \
    "buffer_load_dwordx4 v211, s[40:43], s48 offen offset:3072 lds\n\t" \
    "v_mfma_f32_16x16x32_bf16 v[32:35], v[164:167], a[8:11], v[32:35]\n\t" \
    "s_waitcnt lgkmcnt(4)\n\t" \
    "v_mul_f32 v122, v122, v126\n\t" \
    "v_mul_f32 v123, v123, v127\n\t" \
    "v_mfma_f32_16x16x32_bf16 v[32:35], v[168:171], a[16:19], v[32:35]\n\t" \
    "s_add_u32 m0, %[m0base], 131072\n\t" \
    "v_cvt_pk_bf16_f32 v116, v112, v113\n\t" \
    "v_cvt_pk_bf16_f32 v117, v114, v115\n\t" \
    "v_mfma_f32_16x16x32_bf16 v[32:35], v[172:175], a[24:27], v[32:35]\n\t" \
    "s_add_u32 s48, %[w2off], 126976\n\t" \
    "v_cvt_pk_bf16_f32 v118, v120, v121\n\t" \
    "v_cvt_pk_bf16_f32 v119, v122, v123\n\t" \
    "v_mfma_f32_16x16x32_bf16 v[36:39], v[160:163], a[4:7], 0\n\t" \
    "s_cmp_ge_u32 s51, 16\n\t" \
    "s_cbranch_scc0 Lsl9_%=\n\t" \
    "Lok7_%=:\n\t" \
    "v_mfma_f32_16x16x32_bf16 v[36:39], v[164:167], a[12:15], v[36:39]\n\t" \
    "buffer_store_dwordx4 v[116:119], v211, s[40:43], s50 offen offset:1024 sc1\n\t" \
    "v_mfma_f32_16x16x32_bf16 v[36:39], v[168:171], a[20:23], v[36:39]\n\t" \
    "ds_read_b128 v[192:195], v208 offset:12288\n\t" \
    "ds_read_b128 v[196:199], v208 offset:13312\n\t" \
    "v_mfma_f32_16x16x32_bf16 v[36:39], v[172:175], a[28:31], v[36:39]\n\t" \
    "s_waitcnt lgkmcnt(4)\n\t" \
    "ds_read_b128 v[200:203], v208 offset:14336\n\t" \
    "ds_read_b128 v[204:207], v208 offset:15360\n\t" \
    "v_mfma_f32_16x16x32_bf16 v[40:43], v[176:179], a[0:3], 0\n\t" \
    "v_max_f32 v0, 0, v0\n\t" \
    "v_max_f32 v1, 0, v1\n\t" \
    "v_mfma_f32_16x16x32_bf16 v[40:43], v[180:183], a[8:11], v[40:43]\n\t" \
    "s_waitcnt lgkmcnt(4)\n\t" \
    "v_max_f32 v2, 0, v2\n\t" \
    "v_max_f32 v3, 0, v3\n\t" \
    "v_mfma_f32_16x16x32_bf16 v[40:43], v[184:187], a[16:19], v[40:43]\n\t" \
    "v_mfma_f32_16x16x32_bf16 v[40:43], v[188:191], a[24:27], v[40:43]\n\t" \
    "v_mul_f32 v0, v0, v4\n\t" \
    "v_mul_f32 v1, v1, v5\n\t" \
    "v_mfma_f32_16x16x32_bf16 v[44:47], v[176:179], a[4:7], 0\n\t" \
    "s_barrier\n\t" \
    "v_mul_f32 v2, v2, v6\n\t" \
    "v_mfma_f32_16x16x32_bf16 v[44:47], v[180:183], a[12:15], v[44:47]\n\t" \
    "ds_read_b128 v[160:163], v208 offset:16384\n\t" \
    "ds_read_b128 v[164:167], v208 offset:17408\n\t" \
    "v_mfma_f32_16x16x32_bf16 v[44:47], v[184:187], a[20:23], v[44:47]\n\t" \
    "v_mul_f32 v3, v3, v7\n\t" \
    "v_cvt_pk_bf16_f32 v232, v0, v1\n\t" \
    "v_mfma_f32_16x16x32_bf16 v[44:47], v[188:191], a[28:31], v[44:47]\n\t" \
    "s_waitcnt lgkmcnt(4)\n\t" \
    "buffer_load_dwordx4 v211, s[40:43], s48 offen offset:0 lds\n\t" \
    "v_mfma_f32_16x16x32_bf16 v[48:51], v[192:195], a[0:3], 0\n\t" \
    "ds_read_b128 v[168:171], v208 offset:18432\n\t" \
    "ds_read_b128 v[172:175], v208 offset:19456\n\t" \
    "v_mfma_f32_16x16x32_bf16 v[48:51], v[196:199], a[8:11], v[48:51]\n\t" \
    "s_waitcnt lgkmcnt(4)\n\t" \
    "v_cvt_pk_bf16_f32 v233, v2, v3\n\t" \
    "v_max_f32 v8, 0, v8\n\t" \
    "v_mfma_f32_16x16x32_bf16 v[48:51], v[200:203], a[16:19], v[48:51]\n\t" \
    "v_max_f32 v9, 0, v9\n\t" \
    "v_max_f32 v10, 0, v10\n\t" \
    "v_mfma_f32_16x16x32_bf16 v[48:51], v[204:207], a[24:27], v[48:51]\n\t" \
    "v_max_f32 v11, 0, v11\n\t" \
    "v_mul_f32 v8, v8, v12\n\t" \
    "v_mfma_f32_16x16x32_bf16 v[52:55], v[192:195], a[4:7], 0\n\t" \
    "buffer_load_dwordx4 v211, s[40:43], s48 offen offset:1024 lds\n\t" \
    "v_mfma_f32_16x16x32_bf16 v[52:55], v[196:199], a[12:15], v[52:55]\n\t" \
    "ds_read_b128 v[176:179], v208 offset:20480\n\t" \
    "ds_read_b128 v[180:183], v208 offset:21504\n\t" \
    "v_mfma_f32_16x16x32_bf16 v[52:55], v[200:203], a[20:23], v[52:55]\n\t" \
    "v_mul_f32 v9, v9, v13\n\t" \
    "v_mul_f32 v10, v10, v14\n\t" \
    "v_mfma_f32_16x16x32_bf16 v[52:55], v[204:207], a[28:31], v[52:55]\n\t" \
    "s_waitcnt lgkmcnt(4)\n\t" \
    "ds_read_b128 v[184:187], v208 offset:22528\n\t" \
    "ds_read_b128 v[188:191], v208 offset:23552\n\t" \
    "v_mfma_f32_16x16x32_bf16 v[56:59], v[160:163], a[0:3], 0\n\t" \
    "buffer_load_dwordx4 v211, s[40:43], s48 offen offset:2048 lds\n\t" \
    "v_mfma_f32_16x16x32_bf16 v[56:59], v[164:167], a[8:11], v[56:59]\n\t" \
    "s_waitcnt lgkmcnt(4)\n\t" \
    "v_mul_f32 v11, v11, v15\n\t" \
    "v_cvt_pk_bf16_f32 v234, v8, v9\n\t" \
    "v_mfma_f32_16x16x32_bf16 v[56:59], v[168:171], a[16:19], v[56:59]\n\t" \
    "v_cvt_pk_bf16_f32 v235, v10, v11\n\t" \
    "s_add_u32 s50, %[pst3], 0\n\t" \
    "v_mfma_f32_16x16x32_bf16 v[56:59], v[172:175], a[24:27], v[56:59]\n\t" \
    "v_mfma_f32_16x16x32_bf16 v[60:63], v[160:163], a[4:7], 0\n\t" \
    "v_mfma_f32_16x16x32_bf16 v[60:63], v[164:167], a[12:15], v[60:63]\n\t" \
    "buffer_store_dwordx4 v[232:235], v211, s[40:43], s50 offen sc1\n\t" \
    "v_mfma_f32_16x16x32_bf16 v[60:63], v[168:171], a[20:23], v[60:63]\n\t" \
    "ds_read_b128 v[192:195], v208 offset:24576\n\t" \
    "ds_read_b128 v[196:199], v208 offset:25600\n\t" \
    "v_mfma_f32_16x16x32_bf16 v[60:63], v[172:175], a[28:31], v[60:63]\n\t" \
    "s_waitcnt lgkmcnt(4)\n\t" \
    "ds_read_b128 v[200:203], v208 offset:26624\n\t" \
    "ds_read_b128 v[204:207], v208 offset:27648\n\t" \
    "v_mfma_f32_16x16x32_bf16 v[64:67], v[176:179], a[0:3], 0\n\t" \
    "v_max_f32 v16, 0, v16\n\t" \
    "v_max_f32 v17, 0, v17\n\t" \
    "v_mfma_f32_16x16x32_bf16 v[64:67], v[180:183], a[8:11], v[64:67]\n\t" \
    "s_waitcnt lgkmcnt(4)\n\t" \
    "v_max_f32 v18, 0, v18\n\t" \
    "v_max_f32 v19, 0, v19\n\t" \
    "v_mfma_f32_16x16x32_bf16 v[64:67], v[184:187], a[16:19], v[64:67]\n\t" \
    "v_mfma_f32_16x16x32_bf16 v[64:67], v[188:191], a[24:27], v[64:67]\n\t" \
    "v_mul_f32 v16, v16, v20\n\t" \
    "v_mul_f32 v17, v17, v21\n\t" \
    "v_mfma_f32_16x16x32_bf16 v[68:71], v[176:179], a[4:7], 0\n\t" \
    "v_mul_f32 v18, v18, v22\n\t" \
    "v_mul_f32 v19, v19, v23\n\t" \
    "v_mfma_f32_16x16x32_bf16 v[68:71], v[180:183], a[12:15], v[68:71]\n\t" \
    "ds_read_b128 v[160:163], v208 offset:28672\n\t" \
    "ds_read_b128 v[164:167], v208 offset:29696\n\t" \
    "v_mfma_f32_16x16x32_bf16 v[68:71], v[184:187], a[20:23], v[68:71]\n\t" \
    "v_cvt_pk_bf16_f32 v236, v16, v17\n\t" \
    "v_cvt_pk_bf16_f32 v237, v18, v19\n\t" \
    "v_mfma_f32_16x16x32_bf16 v[68:71], v[188:191], a[28:31], v[68:71]\n\t" \
    "s_waitcnt lgkmcnt(4)\n\t" \
    "buffer_load_dwordx4 v211, s[40:43], s48 offen offset:3072 lds\n\t" \
    "v_mfma_f32_16x16x32_bf16 v[72:75], v[192:195], a[0:3], 0\n\t" \
    "s_add_u32 m0, %[m0base], 147456\n\t" \
    "ds_read_b128 v[168:171], v208 offset:30720\n\t" \
    "ds_read_b128 v[172:175], v208 offset:31744\n\t" \
    "v_mfma_f32_16x16x32_bf16 v[72:75], v[196:199], a[8:11], v[72:75]\n\t" \
    "s_waitcnt lgkmcnt(4)\n\t" \
    "v_max_f32 v24, 0, v24\n\t" \
    "v_max_f32 v25, 0, v25\n\t" \
    "v_mfma_f32_16x16x32_bf16 v[72:75], v[200:203], a[16:19], v[72:75]\n\t" \
    "s_add_u32 s48, %[w2off], 143360\n\t" \
    "v_max_f32 v26, 0, v26\n\t" \
    "v_max_f32 v27, 0, v27\n\t" \
    "v_mfma_f32_16x16x32_bf16 v[72:75], v[204:207], a[24:27], v[72:75]\n\t" \
    "s_barrier\n\t" \
    "v_mul_f32 v24, v24, v28\n\t" \
    "v_mfma_f32_16x16x32_bf16 v[76:79], v[192:195], a[4:7], 0\n\t" \
    "buffer_load_dwordx4 a[128:131], v211, s[40:43], s49 offen offset:0 sc1\n\t" \
    "v_mfma_f32_16x16x32_bf16 v[76:79], v[196:199], a[12:15], v[76:79]\n\t" \
    "ds_read_b128 v[176:179], v208 offset:32768\n\t" \
    "ds_read_b128 v[180:183], v208 offset:33792\n\t" \
    "v_mfma_f32_16x16x32_bf16 v[76:79], v[200:203], a[20:23], v[76:79]\n\t" \
    "v_mul_f32 v25, v25, v29\n\t" \
    "v_mul_f32 v26, v26, v30\n\t" \
    "v_mfma_f32_16x16x32_bf16 v[76:79], v[204:207], a[28:31], v[76:79]\n\t" \
    "s_waitcnt lgkmcnt(4)\n\t" \
    "ds_read_b128 v[184:187], v208 offset:34816\n\t" \
    "ds_read_b128 v[188:191], v208 offset:35840\n\t" \
    "v_mfma_f32_16x16x32_bf16 v[80:83], v[160:163], a[0:3], 0\n\t" \
    "buffer_load_dwordx4 a[132:135], v211, s[40:43], s49 offen offset:1024 sc1\n\t" \
    "v_mfma_f32_16x16x32_bf16 v[80:83], v[164:167], a[8:11], v[80:83]\n\t" \
    "s_waitcnt lgkmcnt(4)\n\t" \
    "v_mul_f32 v27, v27, v31\n\t" \
    "v_cvt_pk_bf16_f32 v238, v24, v25\n\t" \
    "v_mfma_f32_16x16x32_bf16 v[80:83], v[168:171], a[16:19], v[80:83]\n\t" \
    "v_cvt_pk_bf16_f32 v239, v26, v27\n\t" \
    "s_add_u32 s50, %[pst3], 294912\n\t" \
    "v_mfma_f32_16x16x32_bf16 v[80:83], v[172:175], a[24:27], v[80:83]\n\t" \
    "v_mfma_f32_16x16x32_bf16 v[84:87], v[160:163], a[4:7], 0\n\t" \
    "v_mfma_f32_16x16x32_bf16 v[84:87], v[164:167], a[12:15], v[84:87]\n\t" \
    "buffer_store_dwordx4 v[236:239], v211, s[40:43], s50 offen sc1\n\t" \
    "v_mfma_f32_16x16x32_bf16 v[84:87], v[168:171], a[20:23], v[84:87]\n\t" \
    "ds_read_b128 v[192:195], v208 offset:36864\n\t" \
    "ds_read_b128 v[196:199], v208 offset:37888\n\t" \
    "v_mfma_f32_16x16x32_bf16 v[84:87], v[172:175], a[28:31], v[84:87]\n\t" \
    "s_waitcnt lgkmcnt(4)\n\t" \
    "ds_read_b128 v[200:203], v208 offset:38912\n\t" \
    "ds_read_b128 v[204:207], v208 offset:39936\n\t" \
    "v_mfma_f32_16x16x32_bf16 v[32:35], v[176:179], a[32:35], v[32:35]\n\t" \
    "v_and_b32 v243, 15, v215\n\t" \
    "v_lshlrev_b32 v243, 5, v243\n\t" \
    "v_mfma_f32_16x16x32_bf16 v[32:35], v[180:183], a[40:43], v[32:35]\n\t" \
    "s_waitcnt lgkmcnt(4)\n\t" \
    "v_mfma_f32_16x16x32_bf16 v[32:35], v[184:187], a[48:51], v[32:35]\n\t" \
    "v_mfma_f32_16x16x32_bf16 v[32:35], v[188:191], a[56:59], v[32:35]\n\t" \
    "v_mfma_f32_16x16x32_bf16 v[36:39], v[176:179], a[36:39], v[36:39]\n\t" \
    "v_mfma_f32_16x16x32_bf16 v[36:39], v[180:183], a[44:47], v[36:39]\n\t" \
    "ds_read_b128 v[160:163], v208 offset:40960\n\t" \
    "ds_read_b128 v[164:167], v208 offset:41984\n\t" \
    "v_mfma_f32_16x16x32_bf16 v[36:39], v[184:187], a[52:55], v[36:39]\n\t" \
    "v_mfma_f32_16x16x32_bf16 v[36:39], v[188:191], a[60:63], v[36:39]\n\t" \
    "s_waitcnt lgkmcnt(4)\n\t" \
    "buffer_load_dwordx4 a[136:139], v211, s[40:43], s49 offen offset:2048 sc1\n\t" \
    "v_mfma_f32_16x16x32_bf16 v[40:43], v[192:195], a[32:35], v[40:43]\n\t" \
    "ds_read_b128 v[168:171], v208 offset:43008\n\t" \
    "ds_read_b128 v[172:175], v208 offset:44032\n\t" \
    "v_mfma_f32_16x16x32_bf16 v[40:43], v[196:199], a[40:43], v[40:43]\n\t" \
    "s_waitcnt lgkmcnt(4)\n\t" \
    "v_mfma_f32_16x16x32_bf16 v[40:43], v[200:203], a[48:51], v[40:43]\n\t" \
    "v_mfma_f32_16x16x32_bf16 v[40:43], v[204:207], a[56:59], v[40:43]\n\t" \
    "v_mfma_f32_16x16x32_bf16 v[44:47], v[192:195], a[36:39], v[44:47]\n\t" \
    "buffer_load_dwordx4 a[140:143], v211, s[40:43], s49 offen offset:3072 sc1\n\t" \
    "v_mfma_f32_16x16x32_bf16 v[44:47], v[196:199], a[44:47], v[44:47]\n\t" \
    "s_add_u32 s49, %[pld], 36864\n\t" \
    "ds_read_b128 v[176:179], v208 offset:45056\n\t" \
    "ds_read_b128 v[180:183], v208 offset:46080\n\t" \
    "v_mfma_f32_16x16x32_bf16 v[44:47], v[200:203], a[52:55], v[44:47]\n\t" \
    "v_mfma_f32_16x16x32_bf16 v[44:47], v[204:207], a[60:63], v[44:47]\n\t" \
    "s_waitcnt lgkmcnt(4)\n\t" \
    "ds_read_b128 v[184:187], v208 offset:47104\n\t" \
    "ds_read_b128 v[188:191], v208 offset:48128\n\t" \
    "v_mfma_f32_16x16x32_bf16 v[48:51], v[160:163], a[32:35], v[48:51]\n\t" \
    "buffer_load_dwordx4 v[108:111], v243, s[40:43], %[stld] offen offset:0 sc1\n\t" \
    "v_mfma_f32_16x16x32_bf16 v[48:51], v[164:167], a[40:43], v[48:51]\n\t" \
    "s_waitcnt lgkmcnt(4)\n\t" \
    "v_mfma_f32_16x16x32_bf16 v[48:51], v[168:171], a[48:51], v[48:51]\n\t" \
    "v_mfma_f32_16x16x32_bf16 v[48:51], v[172:175], a[56:59], v[48:51]\n\t" \
    "v_mfma_f32_16x16x32_bf16 v[52:55], v[160:163], a[36:39], v[52:55]\n\t" \
    "s_barrier\n\t" \
    "v_mfma_f32_16x16x32_bf16 v[52:55], v[164:167], a[44:47], v[52:55]\n\t" \
    "buffer_load_dwordx4 v[112:115], v243, s[40:43], %[stld] offen offset:16 sc1\n\t" \
    "v_mfma_f32_16x16x32_bf16 v[52:55], v[168:171], a[52:55], v[52:55]\n\t" \
    "ds_read_b128 v[192:195], v208 offset:49152\n\t" \
    "ds_read_b128 v[196:199], v208 offset:50176\n\t" \
    "v_mfma_f32_16x16x32_bf16 v[52:55], v[172:175], a[60:63], v[52:55]\n\t" \
    "s_waitcnt lgkmcnt(4)\n\t" \
    "ds_read_b128 v[200:203], v208 offset:51200\n\t" \
    "ds_read_b128 v[204:207], v208 offset:52224\n\t" \
    "v_mfma_f32_16x16x32_bf16 v[56:59], v[176:179], a[32:35], v[56:59]\n\t" \
    "v_mfma_f32_16x16x32_bf16 v[56:59], v[180:183], a[40:43], v[56:59]\n\t" \
    "s_waitcnt lgkmcnt(4)\n\t" \
    "v_mfma_f32_16x16x32_bf16 v[56:59], v[184:187], a[48:51], v[56:59]\n\t" \
    "buffer_load_dwordx4 v[116:119], v243, s[40:43], %[stld] offen offset:512 sc1\n\t" \
    "v_mfma_f32_16x16x32_bf16 v[56:59], v[188:191], a[56:59], v[56:59]\n\t" \
    "v_mfma_f32_16x16x32_bf16 v[60:63], v[176:179], a[36:39], v[60:63]\n\t" \
    "v_mfma_f32_16x16x32_bf16 v[60:63], v[180:183], a[44:47], v[60:63]\n\t" \
    "ds_read_b128 v[160:163], v208 offset:53248\n\t" \
    "ds_read_b128 v[164:167], v208 offset:54272\n\t" \
    "v_mfma_f32_16x16x32_bf16 v[60:63], v[184:187], a[52:55], v[60:63]\n\t" \
    "v_mfma_f32_16x16x32_bf16 v[60:63], v[188:191], a[60:63], v[60:63]\n\t" \
    "s_waitcnt lgkmcnt(4)\n\t" \
    "buffer_load_dwordx4 v[120:123], v243, s[40:43], %[stld] offen offset:528 sc1\n\t" \
    "v_mfma_f32_16x16x32_bf16 v[64:67], v[192:195], a[32:35], v[64:67]\n\t" \
    "ds_read_b128 v[168:171], v208 offset:55296\n\t" \
    "ds_read_b128 v[172:175], v208 offset:56320\n\t" \
    "v_mfma_f32_16x16x32_bf16 v[64:67], v[196:199], a[40:43], v[64:67]\n\t" \
    "s_waitcnt lgkmcnt(4)\n\t" \
    "v_mfma_f32_16x16x32_bf16 v[64:67], v[200:203], a[48:51], v[64:67]\n\t" \
    "v_mfma_f32_16x16x32_bf16 v[64:67], v[204:207], a[56:59], v[64:67]\n\t" \
    "v_mfma_f32_16x16x32_bf16 v[68:71], v[192:195], a[36:39], v[68:71]\n\t" \
    "buffer_load_dwordx4 a[144:147], v211, s[40:43], s49 offen offset:0 sc1\n\t" \
    "v_mfma_f32_16x16x32_bf16 v[68:71], v[196:199], a[44:47], v[68:71]\n\t" \
    "ds_read_b128 v[176:179], v208 offset:57344\n\t" \
    "ds_read_b128 v[180:183], v208 offset:58368\n\t" \
    "v_mfma_f32_16x16x32_bf16 v[68:71], v[200:203], a[52:55], v[68:71]\n\t" \
    "v_mfma_f32_16x16x32_bf16 v[68:71], v[204:207], a[60:63], v[68:71]\n\t" \
    "s_waitcnt lgkmcnt(4)\n\t" \
    "ds_read_b128 v[184:187], v208 offset:59392\n\t" \
    "ds_read_b128 v[188:191], v208 offset:60416\n\t" \
    "v_mfma_f32_16x16x32_bf16 v[72:75], v[160:163], a[32:35], v[72:75]\n\t" \
    "buffer_load_dwordx4 a[148:151], v211, s[40:43], s49 offen offset:1024 sc1\n\t" \
    "v_mfma_f32_16x16x32_bf16 v[72:75], v[164:167], a[40:43], v[72:75]\n\t" \
    "s_waitcnt lgkmcnt(4)\n\t" \
    "v_mfma_f32_16x16x32_bf16 v[72:75], v[168:171], a[48:51], v[72:75]\n\t" \
    "v_mfma_f32_16x16x32_bf16 v[72:75], v[172:175], a[56:59], v[72:75]\n\t" \
    "v_mfma_f32_16x16x32_bf16 v[76:79], v[160:163], a[36:39], v[76:79]\n\t" \
    "v_mfma_f32_16x16x32_bf16 v[76:79], v[164:167], a[44:47], v[76:79]\n\t" \
    "buffer_load_dwordx4 a[152:155], v211, s[40:43], s49 offen offset:2048 sc1\n\t" \
    "v_mfma_f32_16x16x32_bf16 v[76:79], v[168:171], a[52:55], v[76:79]\n\t" \
    "ds_read_b128 v[192:195], v208 offset:61440\n\t" \
    "ds_read_b128 v[196:199], v208 offset:62464\n\t" \
    "v_mfma_f32_16x16x32_bf16 v[76:79], v[172:175], a[60:63], v[76:79]\n\t" \
    "s_waitcnt lgkmcnt(4)\n\t" \
    "ds_read_b128 v[200:203], v208 offset:63488\n\t" \
    "ds_read_b128 v[204:207], v208 offset:64512\n\t" \
    "v_mfma_f32_16x16x32_bf16 v[80:83], v[176:179], a[32:35], v[80:83]\n\t" \
    "v_mfma_f32_16x16x32_bf16 v[80:83], v[180:183], a[40:43], v[80:83]\n\t" \
    "s_waitcnt lgkmcnt(4)\n\t" \
    "v_mfma_f32_16x16x32_bf16 v[80:83], v[184:187], a[48:51], v[80:83]\n\t" \
    "buffer_load_dwordx4 a[156:159], v211, s[40:43], s49 offen offset:3072 sc1\n\t" \
    "v_mfma_f32_16x16x32_bf16 v[80:83], v[188:191], a[56:59], v[80:83]\n\t" \
    "s_add_u32 s49, %[pld], 40960\n\t" \
    "v_mfma_f32_16x16x32_bf16 v[84:87], v[176:179], a[36:39], v[84:87]\n\t" \
    "s_barrier\n\t" \
    "v_mfma_f32_16x16x32_bf16 v[84:87], v[180:183], a[44:47], v[84:87]\n\t" \
    "ds_read_b128 v[160:163], v209 offset:0\n\t" \
    "ds_read_b128 v[164:167], v209 offset:1024\n\t" \
    "v_mfma_f32_16x16x32_bf16 v[84:87], v[184:187], a[52:55], v[84:87]\n\t" \
    "v_mfma_f32_16x16x32_bf16 v[84:87], v[188:191], a[60:63], v[84:87]\n\t" \
    "s_waitcnt lgkmcnt(4)\n\t" \
    "buffer_load_dwordx4 v211, s[40:43], s48 offen offset:0 lds\n\t" \
    "v_mfma_f32_16x16x32_bf16 v[32:35], v[192:195], a[64:67], v[32:35]\n\t" \
    "ds_read_b128 v[168:171], v209 offset:2048\n\t" \
    "ds_read_b128 v[172:175], v209 offset:3072\n\t" \
    "v_mfma_f32_16x16x32_bf16 v[32:35], v[196:199], a[72:75], v[32:35]\n\t" \
    "s_waitcnt lgkmcnt(4)\n\t" \
    "v_mfma_f32_16x16x32_bf16 v[32:35], v[200:203], a[80:83], v[32:35]\n\t" \
    "v_mfma_f32_16x16x32_bf16 v[32:35], v[204:207], a[88:91], v[32:35]\n\t" \
    "v_mfma_f32_16x16x32_bf16 v[36:39], v[192:195], a[68:71], v[36:39]\n\t" \
    "buffer_load_dwordx4 v211, s[40:43], s48 offen offset:1024 lds\n\t" \
    "v_mfma_f32_16x16x32_bf16 v[36:39], v[196:199], a[76:79], v[36:39]\n\t" \
    "ds_read_b128 v[176:179], v209 offset:4096\n\t" \
    "ds_read_b128 v[180:183], v209 offset:5120\n\t" \
    "v_mfma_f32_16x16x32_bf16 v[36:39], v[200:203], a[84:87], v[36:39]\n\t" \
    "v_mfma_f32_16x16x32_bf16 v[36:39], v[204:207], a[92:95], v[36:39]\n\t" \
    "s_waitcnt lgkmcnt(4)\n\t" \
    "ds_read_b128 v[184:187], v209 offset:6144\n\t" \
    "ds_read_b128 v[188:191], v209 offset:7168\n\t" \
    "v_mfma_f32_16x16x32_bf16 v[40:43], v[160:163], a[64:67], v[40:43]\n\t" \
    "buffer_load_dwordx4 v211, s[40:43], s48 offen offset:2048 lds\n\t" \
    "v_mfma_f32_16x16x32_bf16 v[40:43], v[164:167], a[72:75], v[40:43]\n\t" \
    "s_waitcnt lgkmcnt(4)\n\t" \
    "v_mfma_f32_16x16x32_bf16 v[40:43], v[168:171], a[80:83], v[40:43]\n\t" \
    "v_mfma_f32_16x16x32_bf16 v[40:43], v[172:175], a[88:91], v[40:43]\n\t" \
    "v_mfma_f32_16x16x32_bf16 v[44:47], v[160:163], a[68:71], v[44:47]\n\t" \
    "v_mfma_f32_16x16x32_bf16 v[44:47], v[164:167], a[76:79], v[44:47]\n\t" \
    "buffer_load_dwordx4 v211, s[40:43], s48 offen offset:3072 lds\n\t" \
    "v_mfma_f32_16x16x32_bf16 v[44:47], v[168:171], a[84:87], v[44:47]\n\t" \
    "s_add_u32 m0, %[m0base], 0\n\t" \
    "ds_read_b128 v[192:195], v209 offset:8192\n\t" \
    "ds_read_b128 v[196:199], v209 offset:9216\n\t" \
    "v_mfma_f32_16x16x32_bf16 v[44:47], v[172:175], a[92:95], v[44:47]\n\t" \
    "s_waitcnt lgkmcnt(4)\n\t" \
    "ds_read_b128 v[200:203], v209 offset:10240\n\t" \
    "ds_read_b128 v[204:207], v209 offset:11264\n\t" \
    "v_mfma_f32_16x16x32_bf16 v[48:51], v[176:179], a[64:67], v[48:51]\n\t" \
    "s_add_u32 s48, %[w2off], 159744\n\t" \
    "v_mfma_f32_16x16x32_bf16 v[48:51], v[180:183], a[72:75], v[48:51]\n\t" \
    "s_waitcnt lgkmcnt(4)\n\t" \
    "v_mfma_f32_16x16x32_bf16 v[48:51], v[184:187], a[80:83], v[48:51]\n\t" \
    "buffer_load_dwordx4 v211, s[40:43], s48 offen offset:0 lds\n\t" \
    "v_mfma_f32_16x16x32_bf16 v[48:51], v[188:191], a[88:91], v[48:51]\n\t" \
    "v_mov_b32 v214, 65536\n\t" \
    "v_mfma_f32_16x16x32_bf16 v[52:55], v[176:179], a[68:71], v[52:55]\n\t" \
    "s_waitcnt vmcnt(23)\n\t" \
    "v_mfma_f32_16x16x32_bf16 v[52:55], v[180:183], a[76:79], v[52:55]\n\t" \
    "ds_read_b128 v[160:163], v209 offset:12288\n\t" \
    "ds_read_b128 v[164:167], v209 offset:13312\n\t" \
    "v_mfma_f32_16x16x32_bf16 v[52:55], v[184:187], a[84:87], v[52:55]\n\t" \
    "v_mfma_f32_16x16x32_bf16 v[52:55], v[188:191], a[92:95], v[52:55]\n\t" \
    "s_waitcnt lgkmcnt(4)\n\t" \
    "s_mov_b64 exec, 1\n\t" \
    "buffer_atomic_add v214, off, s[40:43], %[flagout]\n\t" \
    "s_mov_b64 exec, -1\n\t" \
    "v_mfma_f32_16x16x32_bf16 v[56:59], v[192:195], a[64:67], v[56:59]\n\t" \
    "ds_read_b128 v[168:171], v209 offset:14336\n\t" \
    "ds_read_b128 v[172:175], v209 offset:15360\n\t" \
    "v_mfma_f32_16x16x32_bf16 v[56:59], v[196:199], a[72:75], v[56:59]\n\t" \
    "s_waitcnt lgkmcnt(4)\n\t" \
    "v_mfma_f32_16x16x32_bf16 v[56:59], v[200:203], a[80:83], v[56:59]\n\t" \
    "v_mfma_f32_16x16x32_bf16 v[56:59], v[204:207], a[88:91], v[56:59]\n\t" \
    "s_barrier\n\t" \
    "v_mfma_f32_16x16x32_bf16 v[60:63], v[192:195], a[68:71], v[60:63]\n\t" \
    "buffer_load_dwordx4 v211, s[40:43], s48 offen offset:1024 lds\n\t" \
    "v_mfma_f32_16x16x32_bf16 v[60:63], v[196:199], a[76:79], v[60:63]\n\t" \
    "ds_read_b128 v[176:179], v209 offset:16384\n\t" \
    "ds_read_b128 v[180:183], v209 offset:17408\n\t" \
    "v_mfma_f32_16x16x32_bf16 v[60:63], v[200:203], a[84:87], v[60:63]\n\t" \
    "v_mfma_f32_16x16x32_bf16 v[60:63], v[204:207], a[92:95], v[60:63]\n\t" \
    "s_waitcnt lgkmcnt(4)\n\t" \
    "ds_read_b128 v[184:187], v209 offset:18432\n\t" \
    "ds_read_b128 v[188:191], v209 offset:19456\n\t" \
    "v_mfma_f32_16x16x32_bf16 v[64:67], v[160:163], a[64:67], v[64:67]\n\t" \
    "buffer_load_dwordx4 v211, s[40:43], s48 offen offset:2048 lds\n\t" \
    "v_mfma_f32_16x16x32_bf16 v[64:67], v[164:167], a[72:75], v[64:67]\n\t" \
    "s_waitcnt lgkmcnt(4)\n\t" \
    "v_mfma_f32_16x16x32_bf16 v[64:67], v[168:171], a[80:83], v[64:67]\n\t" \
    "v_mfma_f32_16x16x32_bf16 v[64:67], v[172:175], a[88:91], v[64:67]\n\t" \
    "v_mfma_f32_16x16x32_bf16 v[68:71], v[160:163], a[68:71], v[68:71]\n\t" \
    "v_mfma_f32_16x16x32_bf16 v[68:71], v[164:167], a[76:79], v[68:71]\n\t" \
    "buffer_load_dwordx4 v211, s[40:43], s48 offen offset:3072 lds\n\t" \
    "v_mfma_f32_16x16x32_bf16 v[68:71], v[168:171], a[84:87], v[68:71]\n\t" \
    "s_add_u32 m0, %[m0base], 16384\n\t" \
    "ds_read_b128 v[192:195], v209 offset:20480\n\t" \
    "ds_read_b128 v[196:199], v209 offset:21504\n\t" \
    "v_mfma_f32_16x16x32_bf16 v[68:71], v[172:175], a[92:95], v[68:71]\n\t" \
    "s_waitcnt lgkmcnt(4)\n\t" \
    "ds_read_b128 v[200:203], v209 offset:22528\n\t" \
    "ds_read_b128 v[204:207], v209 offset:23552\n\t" \
    "v_mfma_f32_16x16x32_bf16 v[72:75], v[176:179], a[64:67], v[72:75]\n\t" \
    "s_add_u32 s48, %[w2off], 176128\n\t" \
    "v_mfma_f32_16x16x32_bf16 v[72:75], v[180:183], a[72:75], v[72:75]\n\t" \
    "s_waitcnt lgkmcnt(4)\n\t" \
    "v_mfma_f32_16x16x32_bf16 v[72:75], v[184:187], a[80:83], v[72:75]\n\t" \
    "buffer_load_dwordx4 a[160:163], v211, s[40:43], s49 offen offset:0 sc1\n\t" \
    "v_mfma_f32_16x16x32_bf16 v[72:75], v[188:191], a[88:91], v[72:75]\n\t" \
    "v_mfma_f32_16x16x32_bf16 v[76:79], v[176:179], a[68:71], v[76:79]\n\t" \
    "v_mfma_f32_16x16x32_bf16 v[76:79], v[180:183], a[76:79], v[76:79]\n\t" \
    "ds_read_b128 v[160:163], v209 offset:24576\n\t" \
    "ds_read_b128 v[164:167], v209 offset:25600\n\t" \
    "v_mfma_f32_16x16x32_bf16 v[76:79], v[184:187], a[84:87], v[76:79]\n\t" \
    "v_mfma_f32_16x16x32_bf16 v[76:79], v[188:191], a[92:95], v[76:79]\n\t" \
    "s_waitcnt lgkmcnt(4)\n\t" \
    "buffer_load_dwordx4 a[164:167], v211, s[40:43], s49 offen offset:1024 sc1\n\t" \
    "v_mfma_f32_16x16x32_bf16 v[80:83], v[192:195], a[64:67], v[80:83]\n\t" \
    "ds_read_b128 v[168:171], v209 offset:26624\n\t" \
    "ds_read_b128 v[172:175], v209 offset:27648\n\t" \
    "v_mfma_f32_16x16x32_bf16 v[80:83], v[196:199], a[72:75], v[80:83]\n\t" \
    "s_waitcnt lgkmcnt(4)\n\t" \
    "v_mfma_f32_16x16x32_bf16 v[80:83], v[200:203], a[80:83], v[80:83]\n\t" \
    "s_waitcnt vmcnt(15)\n\t" \
    "v_max_u32 v240, v108, v109\n\t" \
    "v_max_u32 v241, v110, v111\n\t" \
    "v_mfma_f32_16x16x32_bf16 v[80:83], v[204:207], a[88:91], v[80:83]\n\t" \
    "v_max3_u32 v240, v112, v113, v240\n\t" \
    "v_max3_u32 v241, v114, v115, v241\n\t" \
    "v_mfma_f32_16x16x32_bf16 v[84:87], v[192:195], a[68:71], v[84:87]\n\t" \
    "buffer_load_dwordx4 a[168:171], v211, s[40:43], s49 offen offset:2048 sc1\n\t" \
    "v_mfma_f32_16x16x32_bf16 v[84:87], v[196:199], a[76:79], v[84:87]\n\t" \
    "ds_read_b128 v[176:179], v209 offset:28672\n\t" \
    "ds_read_b128 v[180:183], v209 offset:29696\n\t" \
    "v_mfma_f32_16x16x32_bf16 v[84:87], v[200:203], a[84:87], v[84:87]\n\t" \
    "v_max3_u32 v240, v116, v117, v240\n\t" \
    "v_max3_u32 v241, v118, v119, v241\n\t" \
    "v_mfma_f32_16x16x32_bf16 v[84:87], v[204:207], a[92:95], v[84:87]\n\t" \
    "s_waitcnt lgkmcnt(4)\n\t" \
    "ds_read_b128 v[184:187], v209 offset:30720\n\t" \
    "ds_read_b128 v[188:191], v209 offset:31744\n\t" \
    "v_mfma_f32_16x16x32_bf16 v[32:35], v[160:163], a[96:99], v[32:35]\n\t" \
    "buffer_load_dwordx4 a[172:175], v211, s[40:43], s49 offen offset:3072 sc1\n\t" \
    "v_mfma_f32_16x16x32_bf16 v[32:35], v[164:167], a[104:107], v[32:35]\n\t" \
    "s_waitcnt lgkmcnt(4)\n\t" \
    "s_add_u32 s49, %[pld], 45056\n\t" \
    "v_max3_u32 v240, v120, v121, v240\n\t" \
    "v_mfma_f32_16x16x32_bf16 v[32:35], v[168:171], a[112:115], v[32:35]\n\t" \
    "v_max3_u32 v241, v122, v123, v241\n\t" \
    "v_max_u32 v240, v240, v241\n\t" \
    "v_mfma_f32_16x16x32_bf16 v[32:35], v[172:175], a[120:123], v[32:35]\n\t" \
    "v_cmp_eq_u32 vcc, -1, v240\n\t" \
    "v_mfma_f32_16x16x32_bf16 v[36:39], v[160:163], a[100:103], v[36:39]\n\t" \
    "s_barrier\n\t" \
    "v_mfma_f32_16x16x32_bf16 v[36:39], v[164:167], a[108:111], v[36:39]\n\t" \
    "buffer_load_dwordx4 a[176:179], v211, s[40:43], s49 offen offset:0 sc1\n\t" \
    "v_mfma_f32_16x16x32_bf16 v[36:39], v[168:171], a[116:119], v[36:39]\n\t" \
    "ds_read_b128 v[192:195], v209 offset:32768\n\t" \
    "ds_read_b128 v[196:199], v209 offset:33792\n\t" \
    "v_mfma_f32_16x16x32_bf16 v[36:39], v[172:175], a[124:127], v[36:39]\n\t" \
    "s_waitcnt lgkmcnt(4)\n\t" \
    "ds_read_b128 v[200:203], v209 offset:34816\n\t" \
    "ds_read_b128 v[204:207], v209 offset:35840\n\t" \
    "v_mfma_f32_16x16x32_bf16 v[40:43], v[176:179], a[96:99], v[40:43]\n\t" \
    "s_cbranch_vccnz Lstsl12_%=\n\t" \
    "Lstok11_%=:\n\t" \
    "v_mfma_f32_16x16x32_bf16 v[40:43], v[180:183], a[104:107], v[40:43]\n\t" \
    "s_waitcnt lgkmcnt(4)\n\t" \
    "buffer_load_dwordx4 a[180:183], v211, s[40:43], s49 offen offset:1024 sc1\n\t" \
    "v_mfma_f32_16x16x32_bf16 v[40:43], v[184:187], a[112:115], v[40:43]\n\t" \
    "v_mfma_f32_16x16x32_bf16 v[40:43], v[188:191], a[120:123], v[40:43]\n\t" \
    "v_mov_b32 v214, 16777216\n\t" \
    "v_add_f32 v108, v108, v112\n\t" \
    "v_mfma_f32_16x16x32_bf16 v[44:47], v[176:179], a[100:103], v[44:47]\n\t" \
    "v_add_f32 v109, v109, v113\n\t" \
    "v_add_f32 v110, v110, v114\n\t" \
    "v_mfma_f32_16x16x32_bf16 v[44:47], v[180:183], a[108:111], v[44:47]\n\t" \
    "ds_read_b128 v[160:163], v209 offset:36864\n\t" \
    "ds_read_b128 v[164:167], v209 offset:37888\n\t" \
    "v_mfma_f32_16x16x32_bf16 v[44:47], v[184:187], a[116:119], v[44:47]\n\t" \
    "v_add_f32 v111, v111, v115\n\t" \
    "v_add_f32 v108, v108, v110\n\t" \
    "v_mfma_f32_16x16x32_bf16 v[44:47], v[188:191], a[124:127], v[44:47]\n\t" \
    "s_waitcnt lgkmcnt(4)\n\t" \
    "s_mov_b64 exec, 1\n\t" \
    "buffer_atomic_add v214, off, s[40:43], %[flagout]\n\t" \
    "s_mov_b64 exec, -1\n\t" \
    "v_mfma_f32_16x16x32_bf16 v[48:51], v[192:195], a[96:99], v[48:51]\n\t" \
    "ds_read_b128 v[168:171], v209 offset:38912\n\t" \
    "ds_read_b128 v[172:175], v209 offset:39936\n\t" \
    "v_mfma_f32_16x16x32_bf16 v[48:51], v[196:199], a[104:107], v[48:51]\n\t" \
    "s_waitcnt lgkmcnt(4)\n\t" \
    "v_add_f32 v109, v109, v111\n\t" \
    "v_add_f32 v116, v116, v120\n\t" \
    "v_mfma_f32_16x16x32_bf16 v[48:51], v[200:203], a[112:115], v[48:51]\n\t" \
    "v_add_f32 v117, v117, v121\n\t" \
    "v_add_f32 v118, v118, v122\n\t" \
    "v_mfma_f32_16x16x32_bf16 v[48:51], v[204:207], a[120:123], v[48:51]\n\t" \
    "v_add_f32 v119, v119, v123\n\t" \
    "v_add_f32 v116, v116, v118\n\t" \
    "v_mfma_f32_16x16x32_bf16 v[52:55], v[192:195], a[100:103], v[52:55]\n\t" \
    "buffer_load_dword v213, off, s[40:43], %[flagin] sc1\n\t" \
    "v_mfma_f32_16x16x32_bf16 v[52:55], v[196:199], a[108:111], v[52:55]\n\t" \
    "ds_read_b128 v[176:179], v209 offset:40960\n\t" \
    "ds_read_b128 v[180:183], v209 offset:41984\n\t" \
    "v_mfma_f32_16x16x32_bf16 v[52:55], v[200:203], a[116:119], v[52:55]\n\t" \
    "v_add_f32 v117, v117, v119\n\t" \
    "v_mul_f32 v108, 0x3ae38e39, v108\n\t" \
    "v_mfma_f32_16x16x32_bf16 v[52:55], v[204:207], a[124:127], v[52:55]\n\t" \
    "s_waitcnt lgkmcnt(4)\n\t" \
    "ds_read_b128 v[184:187], v209 offset:43008\n\t" \
    "ds_read_b128 v[188:191], v209 offset:44032\n\t" \
    "v_mfma_f32_16x16x32_bf16 v[56:59], v[160:163], a[96:99], v[56:59]\n\t" \
    "buffer_load_dwordx4 a[184:187], v211, s[40:43], s49 offen offset:2048 sc1\n\t" \
    "v_mfma_f32_16x16x32_bf16 v[56:59], v[164:167], a[104:107], v[56:59]\n\t" \
    "s_waitcnt lgkmcnt(4)\n\t" \
    "v_mul_f32 v116, 0x3ae38e39, v116\n\t" \
    "v_mul_f32 v109, 0x3ae38e39, v109\n\t" \
    "v_mfma_f32_16x16x32_bf16 v[56:59], v[168:171], a[112:115], v[56:59]\n\t" \
    "v_mul_f32 v117, 0x3ae38e39, v117\n\t" \
    "v_fma_f32 v109, -v108, v108, v109\n\t" \
    "v_mfma_f32_16x16x32_bf16 v[56:59], v[172:175], a[120:123], v[56:59]\n\t" \
    "v_fma_f32 v117, -v116, v116, v117\n\t" \
    "v_max_f32 v109, 0, v109\n\t" \
    "v_mfma_f32_16x16x32_bf16 v[60:63], v[160:163], a[100:103], v[60:63]\n\t" \
    "buffer_load_dwordx4 a[188:191], v211, s[40:43], s49 offen offset:3072 sc1\n\t" \
    "v_mfma_f32_16x16x32_bf16 v[60:63], v[164:167], a[108:111], v[60:63]\n\t" \
    "v_mfma_f32_16x16x32_bf16 v[60:63], v[168:171], a[116:119], v[60:63]\n\t" \
    "ds_read_b128 v[192:195], v209 offset:45056\n\t" \
    "ds_read_b128 v[196:199], v209 offset:46080\n\t" \
    "v_mfma_f32_16x16x32_bf16 v[60:63], v[172:175], a[124:127], v[60:63]\n\t" \
    "s_waitcnt lgkmcnt(4)\n\t" \
    "ds_read_b128 v[200:203], v209 offset:47104\n\t" \
    "ds_read_b128 v[204:207], v209 offset:48128\n\t" \
    "v_mfma_f32_16x16x32_bf16 v[64:67], v[176:179], a[96:99], v[64:67]\n\t" \
    "buffer_load_dwordx4 v211, s[40:43], s48 offen offset:0 lds\n\t" \
    "v_mfma_f32_16x16x32_bf16 v[64:67], v[180:183], a[104:107], v[64:67]\n\t" \
    "s_waitcnt lgkmcnt(4)\n\t" \
    "v_max_f32 v117, 0, v117\n\t" \
    "v_cvt_pk_bf16_f32 v109, v109, v109\n\t" \
    "v_mfma_f32_16x16x32_bf16 v[64:67], v[184:187], a[112:115], v[64:67]\n\t" \
    "v_mfma_f32_16x16x32_bf16 v[64:67], v[188:191], a[120:123], v[64:67]\n\t" \
    "v_cvt_pk_bf16_f32 v117, v117, v117\n\t" \
    "v_lshlrev_b32 v109, 16, v109\n\t" \
    "v_mfma_f32_16x16x32_bf16 v[68:71], v[176:179], a[100:103], v[68:71]\n\t" \
    "s_barrier\n\t" \
    "v_lshlrev_b32 v117, 16, v117\n\t" \
    "v_mfma_f32_16x16x32_bf16 v[68:71], v[180:183], a[108:111], v[68:71]\n\t" \
    "ds_read_b128 v[160:163], v209 offset:49152\n\t" \
    "ds_read_b128 v[164:167], v209 offset:50176\n\t" \
    "v_mfma_f32_16x16x32_bf16 v[68:71], v[184:187], a[116:119], v[68:71]\n\t" \
    "buffer_load_dwordx4 v211, s[40:43], s48 offen offset:1024 lds\n\t" \
    "v_mfma_f32_16x16x32_bf16 v[68:71], v[188:191], a[124:127], v[68:71]\n\t" \
    "s_waitcnt lgkmcnt(4)\n\t" \
    "v_mfma_f32_16x16x32_bf16 v[72:75], v[192:195], a[96:99], v[72:75]\n\t" \
    "ds_read_b128 v[168:171], v209 offset:51200\n\t" \
    "ds_read_b128 v[172:175], v209 offset:52224\n\t" \
    "v_mfma_f32_16x16x32_bf16 v[72:75], v[196:199], a[104:107], v[72:75]\n\t" \
    "s_waitcnt lgkmcnt(4)\n\t" \
    "v_add_f32 v109, 0x3727c5ac, v109\n\t" \
    "v_add_f32 v117, 0x3727c5ac, v117\n\t" \
    "v_mfma_f32_16x16x32_bf16 v[72:75], v[200:203], a[112:115], v[72:75]\n\t" \
    "buffer_load_dwordx4 v211, s[40:43], s48 offen offset:2048 lds\n\t" \
    "v_mfma_f32_16x16x32_bf16 v[72:75], v[204:207], a[120:123], v[72:75]\n\t" \
    "v_cvt_pk_bf16_f32 v109, v109, v109\n\t" \
    "v_cvt_pk_bf16_f32 v117, v117, v117\n\t" \
    "v_mfma_f32_16x16x32_bf16 v[76:79], v[192:195], a[100:103], v[76:79]\n\t" \
    "v_mfma_f32_16x16x32_bf16 v[76:79], v[196:199], a[108:111], v[76:79]\n\t" \
    "ds_read_b128 v[176:179], v209 offset:53248\n\t" \
    "ds_read_b128 v[180:183], v209 offset:54272\n\t" \
    "v_mfma_f32_16x16x32_bf16 v[76:79], v[200:203], a[116:119], v[76:79]\n\t" \
    "buffer_load_dwordx4 v211, s[40:43], s48 offen offset:3072 lds\n\t" \
    "v_mfma_f32_16x16x32_bf16 v[76:79], v[204:207], a[124:127], v[76:79]\n\t" \
    "s_waitcnt lgkmcnt(4)\n\t" \
    "ds_read_b128 v[184:187], v209 offset:55296\n\t" \
    "ds_read_b128 v[188:191], v209 offset:56320\n\t" \
    "v_mfma_f32_16x16x32_bf16 v[80:83], v[160:163], a[96:99], v[80:83]\n\t" \
    "s_add_u32 m0, %[m0base], 32768\n\t" \
    "v_mfma_f32_16x16x32_bf16 v[80:83], v[164:167], a[104:107], v[80:83]\n\t" \
    "s_waitcnt lgkmcnt(4)\n\t" \
    "s_add_u32 s48, %[w2off], 192512\n\t" \
    "v_lshlrev_b32 v109, 16, v109\n\t" \
    "v_mfma_f32_16x16x32_bf16 v[80:83], v[168:171], a[112:115], v[80:83]\n\t" \
    "v_lshlrev_b32 v117, 16, v117\n\t" \
    "v_sqrt_f32 v109, v109\n\t" \
    "v_mfma_f32_16x16x32_bf16 v[80:83], v[172:175], a[120:123], v[80:83]\n\t" \
    "v_sqrt_f32 v117, v117\n\t" \
    "v_cvt_pk_bf16_f32 v109, v109, v109\n\t" \
    "v_mfma_f32_16x16x32_bf16 v[84:87], v[160:163], a[100:103], v[84:87]\n\t" \
    "buffer_load_dwordx4 v211, s[40:43], s48 offen offset:0 lds\n\t" \
    "v_mfma_f32_16x16x32_bf16 v[84:87], v[164:167], a[108:111], v[84:87]\n\t" \
    "v_mfma_f32_16x16x32_bf16 v[84:87], v[168:171], a[116:119], v[84:87]\n\t" \
    "ds_read_b128 v[192:195], v209 offset:57344\n\t" \
    "ds_read_b128 v[196:199], v209 offset:58368\n\t" \
    "v_mfma_f32_16x16x32_bf16 v[84:87], v[172:175], a[124:127], v[84:87]\n\t" \
    "s_waitcnt lgkmcnt(4)\n\t" \
    "ds_read_b128 v[200:203], v209 offset:59392\n\t" \
    "ds_read_b128 v[204:207], v209 offset:60416\n\t" \
    "v_mfma_f32_16x16x32_bf16 v[32:35], v[176:179], a[128:131], v[32:35]\n\t" \
    "s_waitcnt vmcnt(24)\n\t" \
    "v_cvt_pk_bf16_f32 v117, v117, v117\n\t" \
    "v_lshlrev_b32 v109, 16, v109\n\t" \
    "v_mfma_f32_16x16x32_bf16 v[32:35], v[180:183], a[136:139], v[32:35]\n\t" \
    "s_waitcnt lgkmcnt(4)\n\t" \
    "buffer_load_dwordx4 v211, s[40:43], s48 offen offset:1024 lds\n\t" \
    "v_mfma_f32_16x16x32_bf16 v[32:35], v[184:187], a[144:147], v[32:35]\n\t" \
    "v_mfma_f32_16x16x32_bf16 v[32:35], v[188:191], a[152:155], v[32:35]\n\t" \
    "v_lshlrev_b32 v117, 16, v117\n\t" \
    "v_mul_f32 v109, v109, v109\n\t" \
    "v_mfma_f32_16x16x32_bf16 v[36:39], v[176:179], a[132:135], v[36:39]\n\t" \
    "v_mul_f32 v117, v117, v117\n\t" \
    "v_rcp_f32 v216, v109\n\t" \
    "v_mfma_f32_16x16x32_bf16 v[36:39], v[180:183], a[140:143], v[36:39]\n\t" \
    "ds_read_b128 v[160:163], v209 offset:61440\n\t" \
    "ds_read_b128 v[164:167], v209 offset:62464\n\t" \
    "v_mfma_f32_16x16x32_bf16 v[36:39], v[184:187], a[148:151], v[36:39]\n\t" \
    "buffer_load_dwordx4 v211, s[40:43], s48 offen offset:2048 lds\n\t" \
    "v_mfma_f32_16x16x32_bf16 v[36:39], v[188:191], a[156:159], v[36:39]\n\t" \
    "s_waitcnt lgkmcnt(4)\n\t" \
    "v_mfma_f32_16x16x32_bf16 v[40:43], v[192:195], a[128:131], v[40:43]\n\t" \
    "ds_read_b128 v[168:171], v209 offset:63488\n\t" \
    "ds_read_b128 v[172:175], v209 offset:64512\n\t" \
    "v_mfma_f32_16x16x32_bf16 v[40:43], v[196:199], a[136:139], v[40:43]\n\t" \
    "s_waitcnt lgkmcnt(4)\n\t" \
    "v_rcp_f32 v217, v117\n\t" \
    "v_mfma_f32_16x16x32_bf16 v[40:43], v[200:203], a[144:147], v[40:43]\n\t" \
    "buffer_load_dwordx4 v211, s[40:43], s48 offen offset:3072 lds\n\t" \
    "v_mfma_f32_16x16x32_bf16 v[40:43], v[204:207], a[152:155], v[40:43]\n\t" \
    "s_barrier\n\t" \
    "s_add_u32 m0, %[m0base], 49152\n\t" \
    "v_mfma_f32_16x16x32_bf16 v[44:47], v[192:195], a[132:135], v[44:47]\n\t" \
    "s_add_u32 s48, %[w2off], 208896\n\t" \
    "v_mfma_f32_16x16x32_bf16 v[44:47], v[196:199], a[140:143], v[44:47]\n\t" \
    "ds_read_b128 v[176:179], v210 offset:0\n\t" \
    "ds_read_b128 v[180:183], v210 offset:1024\n\t" \
    "v_mfma_f32_16x16x32_bf16 v[44:47], v[200:203], a[148:151], v[44:47]\n\t" \
    "v_lshlrev_b32 v108, 16, v220\n\t" \
    "v_and_b32 v109, 0xffff0000, v220\n\t" \
    "v_mfma_f32_16x16x32_bf16 v[44:47], v[204:207], a[156:159], v[44:47]\n\t" \
    "s_waitcnt lgkmcnt(4)\n\t" \
    "ds_read_b128 v[184:187], v210 offset:2048\n\t" \
    "ds_read_b128 v[188:191], v210 offset:3072\n\t" \
    "v_mfma_f32_16x16x32_bf16 v[48:51], v[160:163], a[128:131], v[48:51]\n\t" \
    "buffer_load_dwordx4 v211, s[40:43], s48 offen offset:0 lds\n\t" \
    "v_mfma_f32_16x16x32_bf16 v[48:51], v[164:167], a[136:139], v[48:51]\n\t" \
    "s_waitcnt lgkmcnt(4)\n\t" \
    "v_lshlrev_b32 v110, 16, v221\n\t" \
    "v_and_b32 v111, 0xffff0000, v221\n\t" \
    "v_mfma_f32_16x16x32_bf16 v[48:51], v[168:171], a[144:147], v[48:51]\n\t" \
    "v_lshlrev_b32 v112, 16, v222\n\t" \
    "v_and_b32 v113, 0xffff0000, v222\n\t" \
    "v_mfma_f32_16x16x32_bf16 v[48:51], v[172:175], a[152:155], v[48:51]\n\t" \
    "v_lshlrev_b32 v114, 16, v223\n\t" \
    "v_and_b32 v115, 0xffff0000, v223\n\t" \
    "v_mfma_f32_16x16x32_bf16 v[52:55], v[160:163], a[132:135], v[52:55]\n\t" \
    "v_lshlrev_b32 v116, 16, v224\n\t" \
    "v_and_b32 v117, 0xffff0000, v224\n\t" \
    "v_mfma_f32_16x16x32_bf16 v[52:55], v[164:167], a[140:143], v[52:55]\n\t" \
    "buffer_load_dwordx4 v211, s[40:43], s48 offen offset:1024 lds\n\t" \
    "v_mfma_f32_16x16x32_bf16 v[52:55], v[168:171], a[148:151], v[52:55]\n\t" \
    "ds_read_b128 v[192:195], v210 offset:4096\n\t" \
    "ds_read_b128 v[196:199], v210 offset:5120\n\t" \
    "v_mfma_f32_16x16x32_bf16 v[52:55], v[172:175], a[156:159], v[52:55]\n\t" \
    "s_waitcnt lgkmcnt(4)\n\t" \
    "ds_read_b128 v[200:203], v210 offset:6144\n\t" \
    "ds_read_b128 v[204:207], v210 offset:7168\n\t" \
    "v_mfma_f32_16x16x32_bf16 v[56:59], v[176:179], a[128:131], v[56:59]\n\t" \
    "v_lshlrev_b32 v118, 16, v225\n\t" \
    "v_and_b32 v119, 0xffff0000, v225\n\t" \
    "v_mfma_f32_16x16x32_bf16 v[56:59], v[180:183], a[136:139], v[56:59]\n\t" \
    "s_waitcnt lgkmcnt(4)\n\t" \
    "v_lshlrev_b32 v120, 16, v226\n\t" \
    "v_and_b32 v121, 0xffff0000, v226\n\t" \
    "v_mfma_f32_16x16x32_bf16 v[56:59], v[184:187], a[144:147], v[56:59]\n\t" \
    "buffer_load_dwordx4 v211, s[40:43], s48 offen offset:2048 lds\n\t" \
    "v_mfma_f32_16x16x32_bf16 v[56:59], v[188:191], a[152:155], v[56:59]\n\t" \
    "v_lshlrev_b32 v122, 16, v227\n\t" \
    "v_and_b32 v123, 0xffff0000, v227\n\t" \
    "v_mfma_f32_16x16x32_bf16 v[60:63], v[176:179], a[132:135], v[60:63]\n\t" \
    "v_lshlrev_b32 v244, 16, v228\n\t" \
    "v_and_b32 v245, 0xffff0000, v228\n\t" \
    "v_mfma_f32_16x16x32_bf16 v[60:63], v[180:183], a[140:143], v[60:63]\n\t" \
    "ds_read_b128 v[160:163], v210 offset:8192\n\t" \
    "ds_read_b128 v[164:167], v210 offset:9216\n\t" \
    "v_mfma_f32_16x16x32_bf16 v[60:63], v[184:187], a[148:151], v[60:63]\n\t" \
    "v_lshlrev_b32 v246, 16, v229\n\t" \
    "v_and_b32 v247, 0xffff0000, v229\n\t" \
    "v_mfma_f32_16x16x32_bf16 v[60:63], v[188:191], a[156:159], v[60:63]\n\t" \
    "s_waitcnt lgkmcnt(4)\n\t" \
    "buffer_load_dwordx4 v211, s[40:43], s48 offen offset:3072 lds\n\t" \
    "v_mfma_f32_16x16x32_bf16 v[64:67], v[192:195], a[128:131], v[64:67]\n\t" \
    "s_add_u32 m0, %[m0base], 65536\n\t" \
    "ds_read_b128 v[168:171], v210 offset:10240\n\t" \
    "ds_read_b128 v[172:175], v210 offset:11264\n\t" \
    "v_mfma_f32_16x16x32_bf16 v[64:67], v[196:199], a[136:139], v[64:67]\n\t" \
    "s_waitcnt lgkmcnt(4)\n\t" \
    "s_add_u32 s48, %[w2off], 225280\n\t" \
    "v_lshlrev_b32 v248, 16, v230\n\t" \
    "v_mfma_f32_16x16x32_bf16 v[64:67], v[200:203], a[144:147], v[64:67]\n\t" \
    "v_and_b32 v249, 0xffff0000, v230\n\t" \
    "v_lshlrev_b32 v250, 16, v231\n\t" \
    "v_mfma_f32_16x16x32_bf16 v[64:67], v[204:207], a[152:155], v[64:67]\n\t" \
    "v_and_b32 v251, 0xffff0000, v231\n\t" \
    "s_mov_b32 s53, 0x3f80\n\t" \
    "v_mfma_f32_16x16x32_bf16 v[68:71], v[192:195], a[132:135], v[68:71]\n\t" \
    "s_mov_b32 s54, 0x3f800000\n\t" \
    "buffer_load_dwordx4 v211, s[40:43], s48 offen offset:0 lds\n\t" \
    "v_mfma_f32_16x16x32_bf16 v[68:71], v[196:199], a[140:143], v[68:71]\n\t" \
    "ds_read_b128 v[176:179], v210 offset:12288\n\t" \
    "ds_read_b128 v[180:183], v210 offset:13312\n\t" \
    "v_mfma_f32_16x16x32_bf16 v[68:71], v[200:203], a[148:151], v[68:71]\n\t" \
    "v_mfma_f32_16x16x32_bf16 v[68:71], v[204:207], a[156:159], v[68:71]\n\t" \
    "s_waitcnt lgkmcnt(4)\n\t" \
    "ds_read_b128 v[184:187], v210 offset:14336\n\t" \
    "ds_read_b128 v[188:191], v210 offset:15360\n\t" \
    "v_mfma_f32_16x16x32_bf16 v[72:75], v[160:163], a[128:131], v[72:75]\n\t" \
    "s_waitcnt vmcnt(15)\n\t" \
    "buffer_load_dwordx4 v211, s[40:43], s48 offen offset:1024 lds\n\t" \
    "v_mfma_f32_16x16x32_bf16 v[72:75], v[164:167], a[136:139], v[72:75]\n\t" \
    "s_waitcnt lgkmcnt(4)\n\t" \
    "v_readfirstlane_b32 s51, v213\n\t" \
    "v_mfma_f32_16x16x32_bf16 v[72:75], v[168:171], a[144:147], v[72:75]\n\t" \
    "s_bfe_u32 s51, s51, 0x80010\n\t" \
    "v_mfma_f32_16x16x32_bf16 v[72:75], v[172:175], a[152:155], v[72:75]\n\t" \
    "s_cmp_ge_u32 s51, 8\n\t" \
    "s_cbranch_scc0 Lsl15_%=\n\t" \
    "Lok13_%=:\n\t" \
    "v_mfma_f32_16x16x32_bf16 v[76:79], v[160:163], a[132:135], v[76:79]\n\t" \
    "s_barrier\n\t" \
    "s_add_u32 s49, %[pld], 49152\n\t" \
    "v_mfma_f32_16x16x32_bf16 v[76:79], v[164:167], a[140:143], v[76:79]\n\t" \
    "buffer_load_dword v213, off, s[40:43], %[flagin] sc1\n\t" \
    "v_mfma_f32_16x16x32_bf16 v[76:79], v[168:171], a[148:151], v[76:79]\n\t" \
    "ds_read_b128 v[192:195], v210 offset:16384\n\t" \
    "ds_read_b128 v[196:199], v210 offset:17408\n\t" \
    "v_mfma_f32_16x16x32_bf16 v[76:79], v[172:175], a[156:159], v[76:79]\n\t" \
    "s_waitcnt lgkmcnt(4)\n\t" \
    "ds_read_b128 v[200:203], v210 offset:18432\n\t" \
    "ds_read_b128 v[204:207], v210 offset:19456\n\t" \
    "v_mfma_f32_16x16x32_bf16 v[80:83], v[176:179], a[128:131], v[80:83]\n\t" \
    "v_mfma_f32_16x16x32_bf16 v[80:83], v[180:183], a[136:139], v[80:83]\n\t" \
    "s_waitcnt lgkmcnt(4)\n\t" \
    "buffer_load_dwordx4 a[192:195], v211, s[40:43], s49 offen offset:0 sc1\n\t" \
    "v_mfma_f32_16x16x32_bf16 v[80:83], v[184:187], a[144:147], v[80:83]\n\t" \
    "v_mfma_f32_16x16x32_bf16 v[80:83], v[188:191], a[152:155], v[80:83]\n\t" \
    "v_mfma_f32_16x16x32_bf16 v[84:87], v[176:179], a[132:135], v[84:87]\n\t" \
    "v_mfma_f32_16x16x32_bf16 v[84:87], v[180:183], a[140:143], v[84:87]\n\t" \
    "ds_read_b128 v[160:163], v210 offset:20480\n\t" \
    "ds_read_b128 v[164:167], v210 offset:21504\n\t" \
    "v_mfma_f32_16x16x32_bf16 v[84:87], v[184:187], a[148:151], v[84:87]\n\t" \
    "buffer_load_dwordx4 a[196:199], v211, s[40:43], s49 offen offset:1024 sc1\n\t" \
    "v_mfma_f32_16x16x32_bf16 v[84:87], v[188:191], a[156:159], v[84:87]\n\t" \
    "s_waitcnt lgkmcnt(4)\n\t" \
    "v_mfma_f32_16x16x32_bf16 v[32:35], v[192:195], a[160:163], v[32:35]\n\t" \
    "s_waitcnt vmcnt(17)\n\t" \
    "ds_read_b128 v[168:171], v210 offset:22528\n\t" \
    "ds_read_b128 v[172:175], v210 offset:23552\n\t" \
    "v_mfma_f32_16x16x32_bf16 v[32:35], v[196:199], a[168:171], v[32:35]\n\t" \
    "s_waitcnt lgkmcnt(4)\n\t" \
    "v_mfma_f32_16x16x32_bf16 v[32:35], v[200:203], a[176:179], v[32:35]\n\t" \
    "buffer_load_dwordx4 a[200:203], v211, s[40:43], s49 offen offset:2048 sc1\n\t" \
    "v_mfma_f32_16x16x32_bf16 v[32:35], v[204:207], a[184:187], v[32:35]\n\t" \
    "v_mfma_f32_16x16x32_bf16 v[36:39], v[192:195], a[164:167], v[36:39]\n\t" \
    "v_mfma_f32_16x16x32_bf16 v[36:39], v[196:199], a[172:175], v[36:39]\n\t" \
    "ds_read_b128 v[176:179], v210 offset:24576\n\t" \
    "ds_read_b128 v[180:183], v210 offset:25600\n\t" \
    "v_mfma_f32_16x16x32_bf16 v[36:39], v[200:203], a[180:183], v[36:39]\n\t" \
    "buffer_load_dwordx4 a[204:207], v211, s[40:43], s49 offen offset:3072 sc1\n\t" \
    "v_mfma_f32_16x16x32_bf16 v[36:39], v[204:207], a[188:191], v[36:39]\n\t" \
    "s_waitcnt lgkmcnt(4)\n\t" \
    "ds_read_b128 v[184:187], v210 offset:26624\n\t" \
    "ds_read_b128 v[188:191], v210 offset:27648\n\t" \
    "v_mfma_f32_16x16x32_bf16 v[40:43], v[160:163], a[160:163], v[40:43]\n\t" \
    "s_add_u32 s49, %[pld], 53248\n\t" \
    "v_mfma_f32_16x16x32_bf16 v[40:43], v[164:167], a[168:171], v[40:43]\n\t" \
    "s_waitcnt lgkmcnt(4)\n\t" \
    "v_mfma_f32_16x16x32_bf16 v[40:43], v[168:171], a[176:179], v[40:43]\n\t" \
    "buffer_load_dwordx4 a[208:211], v211, s[40:43], s49 offen offset:0 sc1\n\t" \
    "v_mfma_f32_16x16x32_bf16 v[40:43], v[172:175], a[184:187], v[40:43]\n\t" \
    "v_mfma_f32_16x16x32_bf16 v[44:47], v[160:163], a[164:167], v[44:47]\n\t" \
    "v_mfma_f32_16x16x32_bf16 v[44:47], v[164:167], a[172:175], v[44:47]\n\t" \
    "v_mfma_f32_16x16x32_bf16 v[44:47], v[168:171], a[180:183], v[44:47]\n\t" \
    "ds_read_b128 v[192:195], v210 offset:28672\n\t" \
    "ds_read_b128 v[196:199], v210 offset:29696\n\t" \
    "v_mfma_f32_16x16x32_bf16 v[44:47], v[172:175], a[188:191], v[44:47]\n\t" \
    "s_waitcnt lgkmcnt(4)\n\t" \
    "ds_read_b128 v[200:203], v210 offset:30720\n\t" \
    "ds_read_b128 v[204:207], v210 offset:31744\n\t" \
    "v_mfma_f32_16x16x32_bf16 v[48:51], v[176:179], a[160:163], v[48:51]\n\t" \
    "buffer_load_dwordx4 a[212:215], v211, s[40:43], s49 offen offset:1024 sc1\n\t" \
    "v_mfma_f32_16x16x32_bf16 v[48:51], v[180:183], a[168:171], v[48:51]\n\t" \
    "s_waitcnt lgkmcnt(4)\n\t" \
    "v_mfma_f32_16x16x32_bf16 v[48:51], v[184:187], a[176:179], v[48:51]\n\t" \
    "v_mfma_f32_16x16x32_bf16 v[48:51], v[188:191], a[184:187], v[48:51]\n\t" \
    "v_mfma_f32_16x16x32_bf16 v[52:55], v[176:179], a[164:167], v[52:55]\n\t" \
    "s_barrier\n\t" \
    "v_mfma_f32_16x16x32_bf16 v[52:55], v[180:183], a[172:175], v[52:55]\n\t" \
    "ds_read_b128 v[160:163], v208 offset:0\n\t" \
    "ds_read_b128 v[164:167], v208 offset:1024\n\t" \
    "v_mfma_f32_16x16x32_bf16 v[52:55], v[184:187], a[180:183], v[52:55]\n\t" \
    "buffer_load_dwordx4 a[216:219], v211, s[40:43], s49 offen offset:2048 sc1\n\t" \
    "v_mfma_f32_16x16x32_bf16 v[52:55], v[188:191], a[188:191], v[52:55]\n\t" \
    "s_waitcnt lgkmcnt(4)\n\t" \
    "v_mfma_f32_16x16x32_bf16 v[56:59], v[192:195], a[160:163], v[56:59]\n\t" \
    "ds_read_b128 v[168:171], v208 offset:2048\n\t" \
    "ds_read_b128 v[172:175], v208 offset:3072\n\t" \
    "v_mfma_f32_16x16x32_bf16 v[56:59], v[196:199], a[168:171], v[56:59]\n\t" \
    "s_waitcnt lgkmcnt(4)\n\t" \
    "v_mfma_f32_16x16x32_bf16 v[56:59], v[200:203], a[176:179], v[56:59]\n\t" \
    "buffer_load_dwordx4 a[220:223], v211, s[40:43], s49 offen offset:3072 sc1\n\t" \
    "v_mfma_f32_16x16x32_bf16 v[56:59], v[204:207], a[184:187], v[56:59]\n\t" \
    "s_add_u32 s49, %[pld], 57344\n\t" \
    "v_mfma_f32_16x16x32_bf16 v[60:63], v[192:195], a[164:167], v[60:63]\n\t" \
    "v_mfma_f32_16x16x32_bf16 v[60:63], v[196:199], a[172:175], v[60:63]\n\t" \
    "ds_read_b128 v[176:179], v208 offset:4096\n\t" \
    "ds_read_b128 v[180:183], v208 offset:5120\n\t" \
    "v_mfma_f32_16x16x32_bf16 v[60:63], v[200:203], a[180:183], v[60:63]\n\t" \
    "buffer_load_dwordx4 a[224:227], v211, s[40:43], s49 offen offset:0 sc1\n\t" \
    "v_mfma_f32_16x16x32_bf16 v[60:63], v[204:207], a[188:191], v[60:63]\n\t" \
    "s_waitcnt lgkmcnt(4)\n\t" \
    "ds_read_b128 v[184:187], v208 offset:6144\n\t" \
    "ds_read_b128 v[188:191], v208 offset:7168\n\t" \
    "v_mfma_f32_16x16x32_bf16 v[64:67], v[160:163], a[160:163], v[64:67]\n\t" \
    "v_mfma_f32_16x16x32_bf16 v[64:67], v[164:167], a[168:171], v[64:67]\n\t" \
    "s_waitcnt lgkmcnt(4)\n\t" \
    "v_mfma_f32_16x16x32_bf16 v[64:67], v[168:171], a[176:179], v[64:67]\n\t" \
    "buffer_load_dwordx4 a[228:231], v211, s[40:43], s49 offen offset:1024 sc1\n\t" \
    "v_mfma_f32_16x16x32_bf16 v[64:67], v[172:175], a[184:187], v[64:67]\n\t" \
    "v_mfma_f32_16x16x32_bf16 v[68:71], v[160:163], a[164:167], v[68:71]\n\t" \
    "v_mfma_f32_16x16x32_bf16 v[68:71], v[164:167], a[172:175], v[68:71]\n\t" \
    "v_mfma_f32_16x16x32_bf16 v[68:71], v[168:171], a[180:183], v[68:71]\n\t" \
    "ds_read_b128 v[192:195], v208 offset:8192\n\t" \
    "ds_read_b128 v[196:199], v208 offset:9216\n\t" \
    "v_mfma_f32_16x16x32_bf16 v[68:71], v[172:175], a[188:191], v[68:71]\n\t" \
    "s_waitcnt lgkmcnt(4)\n\t" \
    "ds_read_b128 v[200:203], v208 offset:10240\n\t" \
    "ds_read_b128 v[204:207], v208 offset:11264\n\t" \
    "v_mfma_f32_16x16x32_bf16 v[72:75], v[176:179], a[160:163], v[72:75]\n\t" \
    "buffer_load_dwordx4 a[232:235], v211, s[40:43], s49 offen offset:2048 sc1\n\t" \
    "v_mfma_f32_16x16x32_bf16 v[72:75], v[180:183], a[168:171], v[72:75]\n\t" \
    "s_waitcnt lgkmcnt(4)\n\t" \
    "v_mfma_f32_16x16x32_bf16 v[72:75], v[184:187], a[176:179], v[72:75]\n\t" \
    "v_mfma_f32_16x16x32_bf16 v[72:75], v[188:191], a[184:187], v[72:75]\n\t" \
    "v_mfma_f32_16x16x32_bf16 v[76:79], v[176:179], a[164:167], v[76:79]\n\t" \
    "buffer_load_dwordx4 a[236:239], v211, s[40:43], s49 offen offset:3072 sc1\n\t" \
    "v_mfma_f32_16x16x32_bf16 v[76:79], v[180:183], a[172:175], v[76:79]\n\t" \
    "s_add_u32 s49, %[pld], 61440\n\t" \
    "ds_read_b128 v[160:163], v208 offset:12288\n\t" \
    "ds_read_b128 v[164:167], v208 offset:13312\n\t" \
    "v_mfma_f32_16x16x32_bf16 v[76:79], v[184:187], a[180:183], v[76:79]\n\t" \
    "v_mfma_f32_16x16x32_bf16 v[76:79], v[188:191], a[188:191], v[76:79]\n\t" \
    "s_waitcnt lgkmcnt(4)\n\t" \
    "v_mfma_f32_16x16x32_bf16 v[80:83], v[192:195], a[160:163], v[80:83]\n\t" \
    "ds_read_b128 v[168:171], v208 offset:14336\n\t" \
    "ds_read_b128 v[172:175], v208 offset:15360\n\t" \
    "v_mfma_f32_16x16x32_bf16 v[80:83], v[196:199], a[168:171], v[80:83]\n\t" \
    "s_waitcnt lgkmcnt(4)\n\t" \
    "buffer_load_dwordx4 a[240:243], v211, s[40:43], s49 offen offset:0 sc1\n\t" \
    "v_mfma_f32_16x16x32_bf16 v[80:83], v[200:203], a[176:179], v[80:83]\n\t" \
    "v_mfma_f32_16x16x32_bf16 v[80:83], v[204:207], a[184:187], v[80:83]\n\t" \
    "s_waitcnt vmcnt(24)\n\t" \
    "s_barrier\n\t" \
    "v_mfma_f32_16x16x32_bf16 v[84:87], v[192:195], a[164:167], v[84:87]\n\t" \
    "v_mfma_f32_16x16x32_bf16 v[84:87], v[196:199], a[172:175], v[84:87]\n\t" \
    "ds_read_b128 v[176:179], v208 offset:16384\n\t" \
    "ds_read_b128 v[180:183], v208 offset:17408\n\t" \
    "v_mfma_f32_16x16x32_bf16 v[84:87], v[200:203], a[180:183], v[84:87]\n\t" \
    "s_waitcnt vmcnt(9)\n\t" \
    "buffer_load_dwordx4 a[244:247], v211, s[40:43], s49 offen offset:1024 sc1\n\t" \
    "v_mfma_f32_16x16x32_bf16 v[84:87], v[204:207], a[188:191], v[84:87]\n\t" \
    "s_waitcnt lgkmcnt(4)\n\t" \
    "ds_read_b128 v[184:187], v208 offset:18432\n\t" \
    "ds_read_b128 v[188:191], v208 offset:19456\n\t" \
    "v_mfma_f32_16x16x32_bf16 v[32:35], v[160:163], a[192:195], v[32:35]\n\t" \
    "s_waitcnt vmcnt(6)\n\t" \
    "v_mfma_f32_16x16x32_bf16 v[32:35], v[164:167], a[200:203], v[32:35]\n\t" \
    "s_waitcnt lgkmcnt(4)\n\t" \
    "v_mfma_f32_16x16x32_bf16 v[32:35], v[168:171], a[208:211], v[32:35]\n\t" \
    "buffer_load_dwordx4 a[248:251], v211, s[40:43], s49 offen offset:2048 sc1\n\t" \
    "v_mfma_f32_16x16x32_bf16 v[32:35], v[172:175], a[216:219], v[32:35]\n\t" \
    "v_mfma_f32_16x16x32_bf16 v[36:39], v[160:163], a[196:199], v[36:39]\n\t" \
    "v_mfma_f32_16x16x32_bf16 v[36:39], v[164:167], a[204:207], v[36:39]\n\t" \
    "v_mfma_f32_16x16x32_bf16 v[36:39], v[168:171], a[212:215], v[36:39]\n\t" \
    "ds_read_b128 v[192:195], v208 offset:20480\n\t" \
    "ds_read_b128 v[196:199], v208 offset:21504\n\t" \
    "v_mfma_f32_16x16x32_bf16 v[36:39], v[172:175], a[220:223], v[36:39]\n\t" \
    "s_waitcnt lgkmcnt(4)\n\t" \
    "ds_read_b128 v[200:203], v208 offset:22528\n\t" \
    "ds_read_b128 v[204:207], v208 offset:23552\n\t" \
    "v_mfma_f32_16x16x32_bf16 v[40:43], v[176:179], a[192:195], v[40:43]\n\t" \
    "buffer_load_dwordx4 a[252:255], v211, s[40:43], s49 offen offset:3072 sc1\n\t" \
    "v_mfma_f32_16x16x32_bf16 v[40:43], v[180:183], a[200:203], v[40:43]\n\t" \
    "s_waitcnt lgkmcnt(4)\n\t" \
    "v_readfirstlane_b32 s51, v213\n\t" \
    "v_mfma_f32_16x16x32_bf16 v[40:43], v[184:187], a[208:211], v[40:43]\n\t" \
    "s_bfe_u32 s51, s51, 0x80018\n\t" \
    "v_mfma_f32_16x16x32_bf16 v[40:43], v[188:191], a[216:219], v[40:43]\n\t" \
    "s_cmp_ge_u32 s51, 8\n\t" \
    "s_cbranch_scc0 Lsl18_%=\n\t" \
    "Lok16_%=:\n\t" \
    "v_mfma_f32_16x16x32_bf16 v[44:47], v[176:179], a[196:199], v[44:47]\n\t" \
    "s_add_u32 s49, %[pld], 65536\n\t" \
    "buffer_load_dwordx4 v211, s[40:43], s48 offen offset:2048 lds\n\t" \
    "v_mfma_f32_16x16x32_bf16 v[44:47], v[180:183], a[204:207], v[44:47]\n\t" \
    "ds_read_b128 v[160:163], v208 offset:24576\n\t" \
    "ds_read_b128 v[164:167], v208 offset:25600\n\t" \
    "v_mfma_f32_16x16x32_bf16 v[44:47], v[184:187], a[212:215], v[44:47]\n\t" \
    "v_mfma_f32_16x16x32_bf16 v[44:47], v[188:191], a[220:223], v[44:47]\n\t" \
    "s_waitcnt lgkmcnt(4)\n\t" \
    "v_mfma_f32_16x16x32_bf16 v[48:51], v[192:195], a[192:195], v[48:51]\n\t" \
    "ds_read_b128 v[168:171], v208 offset:26624\n\t" \
    "ds_read_b128 v[172:175], v208 offset:27648\n\t" \
    "v_mfma_f32_16x16x32_bf16 v[48:51], v[196:199], a[200:203], v[48:51]\n\t" \
    "s_waitcnt lgkmcnt(4)\n\t" \
    "buffer_load_dwordx4 v211, s[40:43], s48 offen offset:3072 lds\n\t" \
    "v_mfma_f32_16x16x32_bf16 v[48:51], v[200:203], a[208:211], v[48:51]\n\t" \
    "s_add_u32 m0, %[m0base], 81920\n\t" \
    "s_add_u32 s48, %[w2off], 241664\n\t" \
    "v_mfma_f32_16x16x32_bf16 v[48:51], v[204:207], a[216:219], v[48:51]\n\t" \
    "v_mfma_f32_16x16x32_bf16 v[52:55], v[192:195], a[196:199], v[52:55]\n\t" \
    "v_mfma_f32_16x16x32_bf16 v[52:55], v[196:199], a[204:207], v[52:55]\n\t" \
    "ds_read_b128 v[176:179], v208 offset:28672\n\t" \
    "ds_read_b128 v[180:183], v208 offset:29696\n\t" \
    "v_mfma_f32_16x16x32_bf16 v[52:55], v[200:203], a[212:215], v[52:55]\n\t" \
    "buffer_load_dwordx4 v211, s[40:43], s48 offen offset:0 lds\n\t" \
    "v_mfma_f32_16x16x32_bf16 v[52:55], v[204:207], a[220:223], v[52:55]\n\t" \
    "s_waitcnt lgkmcnt(4)\n\t" \
    "ds_read_b128 v[184:187], v208 offset:30720\n\t" \
    "ds_read_b128 v[188:191], v208 offset:31744\n\t" \
    "v_mfma_f32_16x16x32_bf16 v[56:59], v[160:163], a[192:195], v[56:59]\n\t" \
    "v_mfma_f32_16x16x32_bf16 v[56:59], v[164:167], a[200:203], v[56:59]\n\t" \
    "s_waitcnt lgkmcnt(4)\n\t" \
    "v_mfma_f32_16x16x32_bf16 v[56:59], v[168:171], a[208:211], v[56:59]\n\t" \
    "buffer_load_dwordx4 v211, s[40:43], s48 offen offset:1024 lds\n\t" \
    "v_mfma_f32_16x16x32_bf16 v[56:59], v[172:175], a[216:219], v[56:59]\n\t" \
    "v_mfma_f32_16x16x32_bf16 v[60:63], v[160:163], a[196:199], v[60:63]\n\t" \
    "s_barrier\n\t" \
    "v_mfma_f32_16x16x32_bf16 v[60:63], v[164:167], a[204:207], v[60:63]\n\t" \
    "v_mfma_f32_16x16x32_bf16 v[60:63], v[168:171], a[212:215], v[60:63]\n\t" \
    "ds_read_b128 v[192:195], v208 offset:32768\n\t" \
    "ds_read_b128 v[196:199], v208 offset:33792\n\t" \
    "v_mfma_f32_16x16x32_bf16 v[60:63], v[172:175], a[220:223], v[60:63]\n\t" \
    "s_waitcnt lgkmcnt(4)\n\t" \
    "ds_read_b128 v[200:203], v208 offset:34816\n\t" \
    "ds_read_b128 v[204:207], v208 offset:35840\n\t" \
    "v_mfma_f32_16x16x32_bf16 v[64:67], v[176:179], a[192:195], v[64:67]\n\t" \
    "buffer_load_dwordx4 v211, s[40:43], s48 offen offset:2048 lds\n\t" \
    "v_mfma_f32_16x16x32_bf16 v[64:67], v[180:183], a[200:203], v[64:67]\n\t" \
    "s_waitcnt lgkmcnt(4)\n\t" \
    "v_mfma_f32_16x16x32_bf16 v[64:67], v[184:187], a[208:211], v[64:67]\n\t" \
    "v_mfma_f32_16x16x32_bf16 v[64:67], v[188:191], a[216:219], v[64:67]\n\t" \
    "v_mfma_f32_16x16x32_bf16 v[68:71], v[176:179], a[196:199], v[68:71]\n\t" \
    "buffer_load_dwordx4 v211, s[40:43], s48 offen offset:3072 lds\n\t" \
    "v_mfma_f32_16x16x32_bf16 v[68:71], v[180:183], a[204:207], v[68:71]\n\t" \
    "s_add_u32 m0, %[m0base], 98304\n\t" \
    "ds_read_b128 v[160:163], v208 offset:36864\n\t" \
    "ds_read_b128 v[164:167], v208 offset:37888\n\t" \
    "v_mfma_f32_16x16x32_bf16 v[68:71], v[184:187], a[212:215], v[68:71]\n\t" \
    "s_add_u32 s48, %[w2off], 258048\n\t" \
    "v_mfma_f32_16x16x32_bf16 v[68:71], v[188:191], a[220:223], v[68:71]\n\t" \
    "s_waitcnt lgkmcnt(4)\n\t" \
    "v_mfma_f32_16x16x32_bf16 v[72:75], v[192:195], a[192:195], v[72:75]\n\t" \
    "ds_read_b128 v[168:171], v208 offset:38912\n\t" \
    "ds_read_b128 v[172:175], v208 offset:39936\n\t" \
    "v_mfma_f32_16x16x32_bf16 v[72:75], v[196:199], a[200:203], v[72:75]\n\t" \
    "s_waitcnt lgkmcnt(4)\n\t" \
    "buffer_load_dwordx4 v[128:131], v211, s[40:43], s49 offen offset:0 sc1\n\t" \
    "v_mfma_f32_16x16x32_bf16 v[72:75], v[200:203], a[208:211], v[72:75]\n\t" \
    "v_mfma_f32_16x16x32_bf16 v[72:75], v[204:207], a[216:219], v[72:75]\n\t" \
    "v_mfma_f32_16x16x32_bf16 v[76:79], v[192:195], a[196:199], v[76:79]\n\t" \
    "v_mfma_f32_16x16x32_bf16 v[76:79], v[196:199], a[204:207], v[76:79]\n\t" \
    "ds_read_b128 v[176:179], v208 offset:40960\n\t" \
    "ds_read_b128 v[180:183], v208 offset:41984\n\t" \
    "v_mfma_f32_16x16x32_bf16 v[76:79], v[200:203], a[212:215], v[76:79]\n\t" \
    "buffer_load_dwordx4 v[132:135], v211, s[40:43], s49 offen offset:1024 sc1\n\t" \
    "v_mfma_f32_16x16x32_bf16 v[76:79], v[204:207], a[220:223], v[76:79]\n\t" \
    "s_waitcnt lgkmcnt(4)\n\t" \
    "ds_read_b128 v[184:187], v208 offset:43008\n\t" \
    "ds_read_b128 v[188:191], v208 offset:44032\n\t" \
    "v_mfma_f32_16x16x32_bf16 v[80:83], v[160:163], a[192:195], v[80:83]\n\t" \
    "v_mfma_f32_16x16x32_bf16 v[80:83], v[164:167], a[200:203], v[80:83]\n\t" \
    "s_waitcnt lgkmcnt(4)\n\t" \
    "v_mfma_f32_16x16x32_bf16 v[80:83], v[168:171], a[208:211], v[80:83]\n\t" \
    "buffer_load_dwordx4 v[136:139], v211, s[40:43], s49 offen offset:2048 sc1\n\t" \
    "v_mfma_f32_16x16x32_bf16 v[80:83], v[172:175], a[216:219], v[80:83]\n\t" \
    "v_mfma_f32_16x16x32_bf16 v[84:87], v[160:163], a[196:199], v[84:87]\n\t" \
    "v_mfma_f32_16x16x32_bf16 v[84:87], v[164:167], a[204:207], v[84:87]\n\t" \
    "v_mfma_f32_16x16x32_bf16 v[84:87], v[168:171], a[212:215], v[84:87]\n\t" \
    "s_waitcnt vmcnt(13)\n\t" \
    "ds_read_b128 v[192:195], v208 offset:45056\n\t" \
    "ds_read_b128 v[196:199], v208 offset:46080\n\t" \
    "v_mfma_f32_16x16x32_bf16 v[84:87], v[172:175], a[220:223], v[84:87]\n\t" \
    "s_waitcnt lgkmcnt(4)\n\t" \
    "ds_read_b128 v[200:203], v208 offset:47104\n\t" \
    "ds_read_b128 v[204:207], v208 offset:48128\n\t" \
    "v_mfma_f32_16x16x32_bf16 v[32:35], v[176:179], a[224:227], v[32:35]\n\t" \
    "s_waitcnt vmcnt(9)\n\t" \
    "buffer_load_dwordx4 v[140:143], v211, s[40:43], s49 offen offset:3072 sc1\n\t" \
    "v_mfma_f32_16x16x32_bf16 v[32:35], v[180:183], a[232:235], v[32:35]\n\t" \
    "s_waitcnt lgkmcnt(4)\n\t" \
    "s_add_u32 s49, %[pld], 69632\n\t" \
    "v_mfma_f32_16x16x32_bf16 v[32:35], v[184:187], a[240:243], v[32:35]\n\t" \
    "v_mfma_f32_16x16x32_bf16 v[32:35], v[188:191], a[248:251], v[32:35]\n\t" \
    "v_mfma_f32_16x16x32_bf16 v[36:39], v[176:179], a[228:231], v[36:39]\n\t" \
    "s_barrier\n\t" \
    "v_mfma_f32_16x16x32_bf16 v[36:39], v[180:183], a[236:239], v[36:39]\n\t" \
    "ds_read_b128 v[160:163], v208 offset:49152\n\t" \
    "ds_read_b128 v[164:167], v208 offset:50176\n\t" \
    "v_mfma_f32_16x16x32_bf16 v[36:39], v[184:187], a[244:247], v[36:39]\n\t" \
    "buffer_load_dwordx4 v[144:147], v211, s[40:43], s49 offen offset:0 sc1\n\t" \
    "v_mfma_f32_16x16x32_bf16 v[36:39], v[188:191], a[252:255], v[36:39]\n\t" \
    "s_waitcnt lgkmcnt(4)\n\t" \
    "v_mfma_f32_16x16x32_bf16 v[40:43], v[192:195], a[224:227], v[40:43]\n\t" \
    "ds_read_b128 v[168:171], v208 offset:51200\n\t" \
    "ds_read_b128 v[172:175], v208 offset:52224\n\t" \
    "v_mfma_f32_16x16x32_bf16 v[40:43], v[196:199], a[232:235], v[40:43]\n\t" \
    "s_waitcnt lgkmcnt(4)\n\t" \
    "v_mfma_f32_16x16x32_bf16 v[40:43], v[200:203], a[240:243], v[40:43]\n\t" \
    "buffer_load_dwordx4 v[148:151], v211, s[40:43], s49 offen offset:1024 sc1\n\t" \
    "v_mfma_f32_16x16x32_bf16 v[40:43], v[204:207], a[248:251], v[40:43]\n\t" \
    "v_mfma_f32_16x16x32_bf16 v[44:47], v[192:195], a[228:231], v[44:47]\n\t" \
    "v_mfma_f32_16x16x32_bf16 v[44:47], v[196:199], a[236:239], v[44:47]\n\t" \
    "ds_read_b128 v[176:179], v208 offset:53248\n\t" \
    "ds_read_b128 v[180:183], v208 offset:54272\n\t" \
    "v_mfma_f32_16x16x32_bf16 v[44:47], v[200:203], a[244:247], v[44:47]\n\t" \
    "buffer_load_dwordx4 v[152:155], v211, s[40:43], s49 offen offset:2048 sc1\n\t" \
    "v_mfma_f32_16x16x32_bf16 v[44:47], v[204:207], a[252:255], v[44:47]\n\t" \
    "s_waitcnt lgkmcnt(4)\n\t" \
    "ds_read_b128 v[184:187], v208 offset:55296\n\t" \
    "ds_read_b128 v[188:191], v208 offset:56320\n\t" \
    "v_mfma_f32_16x16x32_bf16 v[48:51], v[160:163], a[224:227], v[48:51]\n\t" \
    "v_mfma_f32_16x16x32_bf16 v[48:51], v[164:167], a[232:235], v[48:51]\n\t" \
    "s_waitcnt lgkmcnt(4)\n\t" \
    "v_mfma_f32_16x16x32_bf16 v[48:51], v[168:171], a[240:243], v[48:51]\n\t" \
    "buffer_load_dwordx4 v[156:159], v211, s[40:43], s49 offen offset:3072 sc1\n\t" \
    "v_mfma_f32_16x16x32_bf16 v[48:51], v[172:175], a[248:251], v[48:51]\n\t" \
    "v_mfma_f32_16x16x32_bf16 v[52:55], v[160:163], a[228:231], v[52:55]\n\t" \
    "v_mfma_f32_16x16x32_bf16 v[52:55], v[164:167], a[236:239], v[52:55]\n\t" \
    "v_mfma_f32_16x16x32_bf16 v[52:55], v[168:171], a[244:247], v[52:55]\n\t" \
    "ds_read_b128 v[192:195], v208 offset:57344\n\t" \
    "ds_read_b128 v[196:199], v208 offset:58368\n\t" \
    "v_mfma_f32_16x16x32_bf16 v[52:55], v[172:175], a[252:255], v[52:55]\n\t" \
    "s_waitcnt lgkmcnt(4)\n\t" \
    "ds_read_b128 v[200:203], v208 offset:59392\n\t" \
    "ds_read_b128 v[204:207], v208 offset:60416\n\t" \
    "v_mfma_f32_16x16x32_bf16 v[56:59], v[176:179], a[224:227], v[56:59]\n\t" \
    "buffer_load_dwordx4 v211, s[40:43], s48 offen offset:0 lds\n\t" \
    "v_mfma_f32_16x16x32_bf16 v[56:59], v[180:183], a[232:235], v[56:59]\n\t" \
    "s_waitcnt lgkmcnt(4)\n\t" \
    "v_mfma_f32_16x16x32_bf16 v[56:59], v[184:187], a[240:243], v[56:59]\n\t" \
    "v_mfma_f32_16x16x32_bf16 v[56:59], v[188:191], a[248:251], v[56:59]\n\t" \
    "v_mfma_f32_16x16x32_bf16 v[60:63], v[176:179], a[228:231], v[60:63]\n\t" \
    "buffer_load_dwordx4 v211, s[40:43], s48 offen offset:1024 lds\n\t" \
    "v_mfma_f32_16x16x32_bf16 v[60:63], v[180:183], a[236:239], v[60:63]\n\t" \
    "ds_read_b128 v[160:163], v208 offset:61440\n\t" \
    "ds_read_b128 v[164:167], v208 offset:62464\n\t" \
    "v_mfma_f32_16x16x32_bf16 v[60:63], v[184:187], a[244:247], v[60:63]\n\t" \
    "v_mfma_f32_16x16x32_bf16 v[60:63], v[188:191], a[252:255], v[60:63]\n\t" \
    "s_waitcnt lgkmcnt(4)\n\t" \
    "v_mfma_f32_16x16x32_bf16 v[64:67], v[192:195], a[224:227], v[64:67]\n\t" \
    "ds_read_b128 v[168:171], v208 offset:63488\n\t" \
    "ds_read_b128 v[172:175], v208 offset:64512\n\t" \
    "v_mfma_f32_16x16x32_bf16 v[64:67], v[196:199], a[232:235], v[64:67]\n\t" \
    "s_waitcnt lgkmcnt(4)\n\t" \
    "buffer_load_dwordx4 v211, s[40:43], s48 offen offset:2048 lds\n\t" \
    "v_mfma_f32_16x16x32_bf16 v[64:67], v[200:203], a[240:243], v[64:67]\n\t" \
    "v_mfma_f32_16x16x32_bf16 v[64:67], v[204:207], a[248:251], v[64:67]\n\t" \
    "s_waitcnt vmcnt(15)\n\t" \
    "s_barrier\n\t" \
    "v_mfma_f32_16x16x32_bf16 v[68:71], v[192:195], a[228:231], v[68:71]\n\t" \
    "v_mfma_f32_16x16x32_bf16 v[68:71], v[196:199], a[236:239], v[68:71]\n\t" \
    "ds_read_b128 v[176:179], v209 offset:0\n\t" \
    "ds_read_b128 v[180:183], v209 offset:1024\n\t" \
    "v_mfma_f32_16x16x32_bf16 v[68:71], v[200:203], a[244:247], v[68:71]\n\t" \
    "buffer_load_dwordx4 v211, s[40:43], s48 offen offset:3072 lds\n\t" \
    "v_mfma_f32_16x16x32_bf16 v[68:71], v[204:207], a[252:255], v[68:71]\n\t" \
    "s_waitcnt lgkmcnt(4)\n\t" \
    "ds_read_b128 v[184:187], v209 offset:2048\n\t" \
    "ds_read_b128 v[188:191], v209 offset:3072\n\t" \
    "v_mfma_f32_16x16x32_bf16 v[72:75], v[160:163], a[224:227], v[72:75]\n\t" \
    "s_waitcnt vmcnt(8)\n\t" \
    "v_mfma_f32_16x16x32_bf16 v[72:75], v[164:167], a[232:235], v[72:75]\n\t" \
    "s_waitcnt lgkmcnt(4)\n\t" \
    "v_swap_b32 v130, v132\n\t" \
    "v_swap_b32 v131, v133\n\t" \
    "v_mfma_f32_16x16x32_bf16 v[72:75], v[168:171], a[240:243], v[72:75]\n\t" \
    "s_add_u32 m0, %[m0base], 114688\n\t" \
    "v_swap_b32 v138, v140\n\t" \
    "v_swap_b32 v139, v141\n\t" \
    "v_mfma_f32_16x16x32_bf16 v[72:75], v[172:175], a[248:251], v[72:75]\n\t" \
    "s_add_u32 s48, %[w2off], 274432\n\t" \
    "buffer_load_dwordx4 v211, s[40:43], s48 offen offset:0 lds\n\t" \
    "v_mfma_f32_16x16x32_bf16 v[76:79], v[160:163], a[228:231], v[76:79]\n\t" \
    "v_mfma_f32_16x16x32_bf16 v[76:79], v[164:167], a[236:239], v[76:79]\n\t" \
    "v_mfma_f32_16x16x32_bf16 v[76:79], v[168:171], a[244:247], v[76:79]\n\t" \
    "ds_read_b128 v[192:195], v209 offset:4096\n\t" \
    "ds_read_b128 v[196:199], v209 offset:5120\n\t" \
    "v_mfma_f32_16x16x32_bf16 v[76:79], v[172:175], a[252:255], v[76:79]\n\t" \
    "s_waitcnt lgkmcnt(4)\n\t" \
    "ds_read_b128 v[200:203], v209 offset:6144\n\t" \
    "ds_read_b128 v[204:207], v209 offset:7168\n\t" \
    "v_mfma_f32_16x16x32_bf16 v[80:83], v[176:179], a[224:227], v[80:83]\n\t" \
    "s_waitcnt vmcnt(5)\n\t" \
    "v_swap_b32 v146, v148\n\t" \
    "v_swap_b32 v147, v149\n\t" \
    "v_mfma_f32_16x16x32_bf16 v[80:83], v[180:183], a[232:235], v[80:83]\n\t" \
    "s_waitcnt lgkmcnt(4)\n\t" \
    "v_swap_b32 v154, v156\n\t" \
    "v_swap_b32 v155, v157\n\t" \
    "v_mfma_f32_16x16x32_bf16 v[80:83], v[184:187], a[240:243], v[80:83]\n\t" \
    "buffer_load_dwordx4 v211, s[40:43], s48 offen offset:1024 lds\n\t" \
    "v_mfma_f32_16x16x32_bf16 v[80:83], v[188:191], a[248:251], v[80:83]\n\t" \
    "v_mfma_f32_16x16x32_bf16 v[84:87], v[176:179], a[228:231], v[84:87]\n\t" \
    "v_mfma_f32_16x16x32_bf16 v[84:87], v[180:183], a[236:239], v[84:87]\n\t" \
    "ds_read_b128 v[160:163], v209 offset:8192\n\t" \
    "ds_read_b128 v[164:167], v209 offset:9216\n\t" \
    "v_mfma_f32_16x16x32_bf16 v[84:87], v[184:187], a[244:247], v[84:87]\n\t" \
    "buffer_load_dwordx4 v211, s[40:43], s48 offen offset:2048 lds\n\t" \
    "v_mfma_f32_16x16x32_bf16 v[84:87], v[188:191], a[252:255], v[84:87]\n\t" \
    "s_waitcnt lgkmcnt(4)\n\t" \
    "v_mfma_f32_16x16x32_bf16 v[32:35], v[192:195], v[128:131], v[32:35]\n\t" \
    "ds_read_b128 v[168:171], v209 offset:10240\n\t" \
    "ds_read_b128 v[172:175], v209 offset:11264\n\t" \
    "v_mfma_f32_16x16x32_bf16 v[32:35], v[196:199], v[136:139], v[32:35]\n\t" \
    "s_waitcnt lgkmcnt(4)\n\t" \
    "v_mfma_f32_16x16x32_bf16 v[32:35], v[200:203], v[144:147], v[32:35]\n\t" \
    "buffer_load_dwordx4 v211, s[40:43], s48 offen offset:3072 lds\n\t" \
    "v_mfma_f32_16x16x32_bf16 v[32:35], v[204:207], v[152:155], v[32:35]\n\t" \
    "s_add_u32 m0, %[m0base], 131072\n\t" \
    "s_add_u32 s48, %[w2off], 290816\n\t" \
    "v_mfma_f32_16x16x32_bf16 v[36:39], v[192:195], v[132:135], v[36:39]\n\t" \
    "v_mfma_f32_16x16x32_bf16 v[36:39], v[196:199], v[140:143], v[36:39]\n\t" \
    "ds_read_b128 v[176:179], v209 offset:12288\n\t" \
    "ds_read_b128 v[180:183], v209 offset:13312\n\t" \
    "v_mfma_f32_16x16x32_bf16 v[36:39], v[200:203], v[148:151], v[36:39]\n\t" \
    "buffer_load_dwordx4 v211, s[40:43], s48 offen offset:0 lds\n\t" \
    "v_mfma_f32_16x16x32_bf16 v[36:39], v[204:207], v[156:159], v[36:39]\n\t" \
    "s_waitcnt lgkmcnt(4)\n\t" \
    "ds_read_b128 v[184:187], v209 offset:14336\n\t" \
    "ds_read_b128 v[188:191], v209 offset:15360\n\t" \
    "v_mfma_f32_16x16x32_bf16 v[40:43], v[160:163], v[128:131], v[40:43]\n\t" \
    "v_mfma_f32_16x16x32_bf16 v[40:43], v[164:167], v[136:139], v[40:43]\n\t" \
    "s_waitcnt lgkmcnt(4)\n\t" \
    "v_mfma_f32_16x16x32_bf16 v[40:43], v[168:171], v[144:147], v[40:43]\n\t" \
    "buffer_load_dwordx4 v211, s[40:43], s48 offen offset:1024 lds\n\t" \
    "v_mfma_f32_16x16x32_bf16 v[40:43], v[172:175], v[152:155], v[40:43]\n\t" \
    "v_mfma_f32_16x16x32_bf16 v[44:47], v[160:163], v[132:135], v[44:47]\n\t" \
    "s_barrier\n\t" \
    "v_mfma_f32_16x16x32_bf16 v[44:47], v[164:167], v[140:143], v[44:47]\n\t" \
    "v_mfma_f32_16x16x32_bf16 v[44:47], v[168:171], v[148:151], v[44:47]\n\t" \
    "ds_read_b128 v[192:195], v209 offset:16384\n\t" \
    "ds_read_b128 v[196:199], v209 offset:17408\n\t" \
    "v_mfma_f32_16x16x32_bf16 v[44:47], v[172:175], v[156:159], v[44:47]\n\t" \
    "s_waitcnt lgkmcnt(4)\n\t" \
    "ds_read_b128 v[200:203], v209 offset:18432\n\t" \
    "ds_read_b128 v[204:207], v209 offset:19456\n\t" \
    "v_mfma_f32_16x16x32_bf16 v[48:51], v[176:179], v[128:131], v[48:51]\n\t" \
    "buffer_load_dwordx4 v211, s[40:43], s48 offen offset:2048 lds\n\t" \
    "v_mfma_f32_16x16x32_bf16 v[48:51], v[180:183], v[136:139], v[48:51]\n\t" \
    "s_waitcnt lgkmcnt(4)\n\t" \
    "v_mfma_f32_16x16x32_bf16 v[48:51], v[184:187], v[144:147], v[48:51]\n\t" \
    "v_cvt_pk_bf16_f32 v232, v32, v33\n\t" \
    "v_cvt_pk_bf16_f32 v233, v34, v35\n\t" \
    "v_mfma_f32_16x16x32_bf16 v[48:51], v[188:191], v[152:155], v[48:51]\n\t" \
    "v_cvt_pk_bf16_f32 v234, v40, v41\n\t" \
    "v_cvt_pk_bf16_f32 v235, v42, v43\n\t" \
    "v_mfma_f32_16x16x32_bf16 v[52:55], v[176:179], v[132:135], v[52:55]\n\t" \
    "buffer_store_dwordx4 v[232:235], v211, s[40:43], %[exo0] offen offset:0 sc1\n\t" \
    "v_mfma_f32_16x16x32_bf16 v[52:55], v[180:183], v[140:143], v[52:55]\n\t" \
    "ds_read_b128 v[160:163], v209 offset:20480\n\t" \
    "ds_read_b128 v[164:167], v209 offset:21504\n\t" \
    "v_mfma_f32_16x16x32_bf16 v[52:55], v[184:187], v[148:151], v[52:55]\n\t" \
    "v_cvt_pk_bf16_f32 v236, v36, v37\n\t" \
    "v_cvt_pk_bf16_f32 v237, v38, v39\n\t" \
    "v_mfma_f32_16x16x32_bf16 v[52:55], v[188:191], v[156:159], v[52:55]\n\t" \
    "s_waitcnt lgkmcnt(4)\n\t" \
    "v_cvt_pk_bf16_f32 v238, v44, v45\n\t" \
    "v_cvt_pk_bf16_f32 v239, v46, v47\n\t" \
    "v_mfma_f32_16x16x32_bf16 v[56:59], v[192:195], v[128:131], v[56:59]\n\t" \
    "ds_read_b128 v[168:171], v209 offset:22528\n\t" \
    "ds_read_b128 v[172:175], v209 offset:23552\n\t" \
    "v_mfma_f32_16x16x32_bf16 v[56:59], v[196:199], v[136:139], v[56:59]\n\t" \
    "s_waitcnt lgkmcnt(4)\n\t" \
    "v_mfma_f32_16x16x32_bf16 v[56:59], v[200:203], v[144:147], v[56:59]\n\t" \
    "v_mfma_f32_16x16x32_bf16 v[56:59], v[204:207], v[152:155], v[56:59]\n\t" \
    "buffer_store_dwordx4 v[236:239], v211, s[40:43], %[exo0] offen offset:1024 sc1\n\t" \
    "v_mfma_f32_16x16x32_bf16 v[60:63], v[192:195], v[132:135], v[60:63]\n\t" \
    "v_mfma_f32_16x16x32_bf16 v[60:63], v[196:199], v[140:143], v[60:63]\n\t" \
    "ds_read_b128 v[176:179], v209 offset:24576\n\t" \
    "ds_read_b128 v[180:183], v209 offset:25600\n\t" \
    "v_mfma_f32_16x16x32_bf16 v[60:63], v[200:203], v[148:151], v[60:63]\n\t" \
    "v_mfma_f32_16x16x32_bf16 v[60:63], v[204:207], v[156:159], v[60:63]\n\t" \
    "s_waitcnt lgkmcnt(4)\n\t" \
    "ds_read_b128 v[184:187], v209 offset:26624\n\t" \
    "ds_read_b128 v[188:191], v209 offset:27648\n\t" \
    "v_mfma_f32_16x16x32_bf16 v[64:67], v[160:163], v[128:131], v[64:67]\n\t" \
    "v_mfma_f32_16x16x32_bf16 v[64:67], v[164:167], v[136:139], v[64:67]\n\t" \
    "s_waitcnt lgkmcnt(4)\n\t" \
    "v_mfma_f32_16x16x32_bf16 v[64:67], v[168:171], v[144:147], v[64:67]\n\t" \
    "v_cvt_pk_bf16_f32 v252, v48, v49\n\t" \
    "v_cvt_pk_bf16_f32 v253, v50, v51\n\t" \
    "v_mfma_f32_16x16x32_bf16 v[64:67], v[172:175], v[152:155], v[64:67]\n\t" \
    "v_cvt_pk_bf16_f32 v254, v56, v57\n\t" \
    "v_cvt_pk_bf16_f32 v255, v58, v59\n\t" \
    "v_mfma_f32_16x16x32_bf16 v[68:71], v[160:163], v[132:135], v[68:71]\n\t" \
    "buffer_store_dwordx4 v[252:255], v211, s[40:43], %[exo1] offen offset:0 sc1\n\t" \
    "v_mfma_f32_16x16x32_bf16 v[68:71], v[164:167], v[140:143], v[68:71]\n\t" \
    "v_cvt_pk_bf16_f32 v232, v52, v53\n\t" \
    "v_cvt_pk_bf16_f32 v233, v54, v55\n\t" \
    "v_mfma_f32_16x16x32_bf16 v[68:71], v[168:171], v[148:151], v[68:71]\n\t" \
    "ds_read_b128 v[192:195], v209 offset:28672\n\t" \
    "ds_read_b128 v[196:199], v209 offset:29696\n\t" \
    "v_mfma_f32_16x16x32_bf16 v[68:71], v[172:175], v[156:159], v[68:71]\n\t" \
    "s_waitcnt lgkmcnt(4)\n\t" \
    "ds_read_b128 v[200:203], v209 offset:30720\n\t" \
    "ds_read_b128 v[204:207], v209 offset:31744\n\t" \
    "v_mfma_f32_16x16x32_bf16 v[72:75], v[176:179], v[128:131], v[72:75]\n\t" \
    "v_cvt_pk_bf16_f32 v234, v60, v61\n\t" \
    "v_cvt_pk_bf16_f32 v235, v62, v63\n\t" \
    "v_mfma_f32_16x16x32_bf16 v[72:75], v[180:183], v[136:139], v[72:75]\n\t" \
    "s_waitcnt lgkmcnt(4)\n\t" \
    "v_mfma_f32_16x16x32_bf16 v[72:75], v[184:187], v[144:147], v[72:75]\n\t" \
    "v_mfma_f32_16x16x32_bf16 v[72:75], v[188:191], v[152:155], v[72:75]\n\t" \
    "buffer_store_dwordx4 v[232:235], v211, s[40:43], %[exo1] offen offset:1024 sc1\n\t" \
    "v_mfma_f32_16x16x32_bf16 v[76:79], v[176:179], v[132:135], v[76:79]\n\t" \
    "s_waitcnt vmcnt(11)\n\t" \
    "s_barrier\n\t" \
    "v_mfma_f32_16x16x32_bf16 v[76:79], v[180:183], v[140:143], v[76:79]\n\t" \
    "ds_read_b128 v[160:163], v209 offset:32768\n\t" \
    "ds_read_b128 v[164:167], v209 offset:33792\n\t" \
    "v_mfma_f32_16x16x32_bf16 v[76:79], v[184:187], v[148:151], v[76:79]\n\t" \
    "v_mfma_f32_16x16x32_bf16 v[76:79], v[188:191], v[156:159], v[76:79]\n\t" \
    "s_waitcnt lgkmcnt(4)\n\t" \
    "v_mfma_f32_16x16x32_bf16 v[80:83], v[192:195], v[128:131], v[80:83]\n\t" \
    "ds_read_b128 v[168:171], v209 offset:34816\n\t" \
    "ds_read_b128 v[172:175], v209 offset:35840\n\t" \
    "v_mfma_f32_16x16x32_bf16 v[80:83], v[196:199], v[136:139], v[80:83]\n\t" \
    "s_waitcnt lgkmcnt(4)\n\t" \
    "ds_read_b128 v[176:179], v209 offset:36864\n\t" \
    "ds_read_b128 v[180:183], v209 offset:37888\n\t" \
    "v_mfma_f32_16x16x32_bf16 v[80:83], v[200:203], v[144:147], v[80:83]\n\t" \
    "v_cvt_pk_bf16_f32 v236, v64, v65\n\t" \
    "v_cvt_pk_bf16_f32 v237, v66, v67\n\t" \
    "v_mfma_f32_16x16x32_bf16 v[80:83], v[204:207], v[152:155], v[80:83]\n\t" \
    "v_cvt_pk_bf16_f32 v238, v72, v73\n\t" \
    "v_cvt_pk_bf16_f32 v239, v74, v75\n\t" \
    "v_mfma_f32_16x16x32_bf16 v[84:87], v[192:195], v[132:135], v[84:87]\n\t" \
    "buffer_store_dwordx4 v[236:239], v211, s[40:43], %[exo2] offen offset:0 sc1\n\t" \
    "v_mfma_f32_16x16x32_bf16 v[84:87], v[196:199], v[140:143], v[84:87]\n\t" \
    "v_cvt_pk_bf16_f32 v252, v68, v69\n\t" \
    "v_cvt_pk_bf16_f32 v253, v70, v71\n\t" \
    "v_mfma_f32_16x16x32_bf16 v[84:87], v[200:203], v[148:151], v[84:87]\n\t" \
    "v_cvt_pk_bf16_f32 v254, v76, v77\n\t" \
    "v_cvt_pk_bf16_f32 v255, v78, v79\n\t" \
    "v_mfma_f32_16x16x32_bf16 v[84:87], v[204:207], v[156:159], v[84:87]\n\t" \
    "s_waitcnt lgkmcnt(4)\n\t" \
    "v_mfma_f32_16x16x32_bf16 v[88:91], v[160:163], a[0:3], 0\n\t" \
    "v_mfma_f32_16x16x32_bf16 v[88:91], v[164:167], a[8:11], v[88:91]\n\t" \
    "s_waitcnt lgkmcnt(2)\n\t" \
    "ds_read_b128 v[184:187], v209 offset:38912\n\t" \
    "ds_read_b128 v[188:191], v209 offset:39936\n\t" \
    "v_mfma_f32_16x16x32_bf16 v[88:91], v[168:171], a[16:19], v[88:91]\n\t" \
    "v_mfma_f32_16x16x32_bf16 v[88:91], v[172:175], a[24:27], v[88:91]\n\t" \
    "s_waitcnt lgkmcnt(2)\n\t" \
    "ds_read_b128 v[192:195], v209 offset:40960\n\t" \
    "ds_read_b128 v[196:199], v209 offset:41984\n\t" \
    "v_mfma_f32_16x16x32_bf16 v[88:91], v[176:179], a[32:35], v[88:91]\n\t" \
    "buffer_store_dwordx4 v[252:255], v211, s[40:43], %[exo2] offen offset:1024 sc1\n\t" \
    "v_mfma_f32_16x16x32_bf16 v[88:91], v[180:183], a[40:43], v[88:91]\n\t" \
    "v_cvt_pk_bf16_f32 v232, v80, v81\n\t" \
    "v_cvt_pk_bf16_f32 v233, v82, v83\n\t" \
    "v_mfma_f32_16x16x32_bf16 v[92:95], v[160:163], a[4:7], 0\n\t" \
    "ds_read_b128 v[200:203], v209 offset:43008\n\t" \
    "ds_read_b128 v[204:207], v209 offset:44032\n\t" \
    "v_mfma_f32_16x16x32_bf16 v[92:95], v[164:167], a[12:15], v[92:95]\n\t" \
    "v_cvt_pk_bf16_f32 v234, v84, v85\n\t" \
    "v_cvt_pk_bf16_f32 v235, v86, v87\n\t" \
    "v_mfma_f32_16x16x32_bf16 v[92:95], v[168:171], a[20:23], v[92:95]\n\t" \
    "v_mfma_f32_16x16x32_bf16 v[92:95], v[172:175], a[28:31], v[92:95]\n\t" \
    "v_mfma_f32_16x16x32_bf16 v[92:95], v[176:179], a[36:39], v[92:95]\n\t" \
    "v_mfma_f32_16x16x32_bf16 v[92:95], v[180:183], a[44:47], v[92:95]\n\t" \
    "s_waitcnt lgkmcnt(4)\n\t" \
    "buffer_store_dwordx4 v[232:235], v211, s[40:43], %[exo8] offen offset:0 sc1\n\t" \
    "v_mfma_f32_16x16x32_bf16 v[96:99], v[184:187], a[0:3], 0\n\t" \
    "v_mfma_f32_16x16x32_bf16 v[96:99], v[188:191], a[8:11], v[96:99]\n\t" \
    "s_waitcnt lgkmcnt(2)\n\t" \
    "ds_read_b128 v[160:163], v209 offset:45056\n\t" \
    "ds_read_b128 v[164:167], v209 offset:46080\n\t" \
    "v_mfma_f32_16x16x32_bf16 v[96:99], v[192:195], a[16:19], v[96:99]\n\t" \
    "s_waitcnt vmcnt(10)\n\t" \
    "s_barrier\n\t" \
    "v_mfma_f32_16x16x32_bf16 v[96:99], v[196:199], a[24:27], v[96:99]\n\t" \
    "s_waitcnt lgkmcnt(2)\n\t" \
    "v_mfma_f32_16x16x32_bf16 v[96:99], v[200:203], a[32:35], v[96:99]\n\t" \
    "ds_read_b128 v[168:171], v209 offset:47104\n\t" \
    "ds_read_b128 v[172:175], v209 offset:48128\n\t" \
    "v_mfma_f32_16x16x32_bf16 v[96:99], v[204:207], a[40:43], v[96:99]\n\t" \
    "ds_read_b128 v[176:179], v209 offset:49152\n\t" \
    "ds_read_b128 v[180:183], v209 offset:50176\n\t" \
    "v_mfma_f32_16x16x32_bf16 v[100:103], v[184:187], a[4:7], 0\n\t" \
    "buffer_load_dwordx4 v211, s[40:43], s48 offen offset:3072 lds\n\t" \
    "v_mfma_f32_16x16x32_bf16 v[100:103], v[188:191], a[12:15], v[100:103]\n\t" \
    "s_add_u32 m0, %[m0base], 147456\n\t" \
    "s_add_u32 s48, %[w2off], 307200\n\t" \
    "v_mfma_f32_16x16x32_bf16 v[100:103], v[192:195], a[20:23], v[100:103]\n\t" \
    "v_mfma_f32_16x16x32_bf16 v[100:103], v[196:199], a[28:31], v[100:103]\n\t" \
    "v_mfma_f32_16x16x32_bf16 v[100:103], v[200:203], a[36:39], v[100:103]\n\t" \
    "buffer_load_dwordx4 v211, s[40:43], s48 offen offset:0 lds\n\t" \
    "v_mfma_f32_16x16x32_bf16 v[100:103], v[204:207], a[44:47], v[100:103]\n\t" \
    "s_waitcnt lgkmcnt(4)\n\t" \
    "v_mfma_f32_16x16x32_bf16 v[88:91], v[160:163], a[48:51], v[88:91]\n\t" \
    "v_mfma_f32_16x16x32_bf16 v[88:91], v[164:167], a[56:59], v[88:91]\n\t" \
    "s_waitcnt lgkmcnt(2)\n\t" \
    "ds_read_b128 v[184:187], v209 offset:51200\n\t" \
    "ds_read_b128 v[188:191], v209 offset:52224\n\t" \
    "v_mfma_f32_16x16x32_bf16 v[88:91], v[168:171], a[64:67], v[88:91]\n\t" \
    "buffer_load_dwordx4 v211, s[40:43], s48 offen offset:1024 lds\n\t" \
    "v_mfma_f32_16x16x32_bf16 v[88:91], v[172:175], a[72:75], v[88:91]\n\t" \
    "s_waitcnt lgkmcnt(2)\n\t" \
    "ds_read_b128 v[192:195], v209 offset:53248\n\t" \
    "ds_read_b128 v[196:199], v209 offset:54272\n\t" \
    "v_mfma_f32_16x16x32_bf16 v[88:91], v[176:179], a[80:83], v[88:91]\n\t" \
    "v_mfma_f32_16x16x32_bf16 v[88:91], v[180:183], a[88:91], v[88:91]\n\t" \
    "v_mfma_f32_16x16x32_bf16 v[92:95], v[160:163], a[52:55], v[92:95]\n\t" \
    "ds_read_b128 v[200:203], v209 offset:55296\n\t" \
    "ds_read_b128 v[204:207], v209 offset:56320\n\t" \
    "v_mfma_f32_16x16x32_bf16 v[92:95], v[164:167], a[60:63], v[92:95]\n\t" \
    "buffer_load_dwordx4 v211, s[40:43], s48 offen offset:2048 lds\n\t" \
    "v_mfma_f32_16x16x32_bf16 v[92:95], v[168:171], a[68:71], v[92:95]\n\t" \
    "v_mfma_f32_16x16x32_bf16 v[92:95], v[172:175], a[76:79], v[92:95]\n\t" \
    "v_mfma_f32_16x16x32_bf16 v[92:95], v[176:179], a[84:87], v[92:95]\n\t" \
    "v_mfma_f32_16x16x32_bf16 v[92:95], v[180:183], a[92:95], v[92:95]\n\t" \
    "s_waitcnt lgkmcnt(4)\n\t" \
    "buffer_load_dwordx4 v211, s[40:43], s48 offen offset:3072 lds\n\t" \
    "v_mfma_f32_16x16x32_bf16 v[96:99], v[184:187], a[48:51], v[96:99]\n\t" \
    "s_add_u32 m0, %[m0base], 0\n\t" \
    "s_add_u32 s48, %[w2off], 323584\n\t" \
    "v_mfma_f32_16x16x32_bf16 v[96:99], v[188:191], a[56:59], v[96:99]\n\t" \
    "s_waitcnt lgkmcnt(2)\n\t" \
    "ds_read_b128 v[160:163], v209 offset:57344\n\t" \
    "ds_read_b128 v[164:167], v209 offset:58368\n\t" \
    "v_mfma_f32_16x16x32_bf16 v[96:99], v[192:195], a[64:67], v[96:99]\n\t" \
    "v_mfma_f32_16x16x32_bf16 v[96:99], v[196:199], a[72:75], v[96:99]\n\t" \
    "s_waitcnt lgkmcnt(2)\n\t" \
    "buffer_load_dwordx4 v211, s[40:43], s48 offen offset:0 lds\n\t" \
    "v_mfma_f32_16x16x32_bf16 v[96:99], v[200:203], a[80:83], v[96:99]\n\t" \
    "ds_read_b128 v[168:171], v209 offset:59392\n\t" \
    "ds_read_b128 v[172:175], v209 offset:60416\n\t" \
    "v_mfma_f32_16x16x32_bf16 v[96:99], v[204:207], a[88:91], v[96:99]\n\t" \
    "ds_read_b128 v[176:179], v209 offset:61440\n\t" \
    "ds_read_b128 v[180:183], v209 offset:62464\n\t" \
    "v_mfma_f32_16x16x32_bf16 v[100:103], v[184:187], a[52:55], v[100:103]\n\t" \
    "v_mfma_f32_16x16x32_bf16 v[100:103], v[188:191], a[60:63], v[100:103]\n\t" \
    "buffer_load_dwordx4 v211, s[40:43], s48 offen offset:1024 lds\n\t" \
    "v_mfma_f32_16x16x32_bf16 v[100:103], v[192:195], a[68:71], v[100:103]\n\t" \
    "v_mfma_f32_16x16x32_bf16 v[100:103], v[196:199], a[76:79], v[100:103]\n\t" \
    "v_mfma_f32_16x16x32_bf16 v[100:103], v[200:203], a[84:87], v[100:103]\n\t" \
    "v_mfma_f32_16x16x32_bf16 v[100:103], v[204:207], a[92:95], v[100:103]\n\t" \
    "s_waitcnt lgkmcnt(4)\n\t" \
    "buffer_load_dwordx4 v211, s[40:43], s48 offen offset:2048 lds\n\t" \
    "v_mfma_f32_16x16x32_bf16 v[88:91], v[160:163], a[96:99], v[88:91]\n\t" \
    "v_mfma_f32_16x16x32_bf16 v[88:91], v[164:167], a[104:107], v[88:91]\n\t" \
    "s_waitcnt lgkmcnt(2)\n\t" \
    "ds_read_b128 v[184:187], v209 offset:63488\n\t" \
    "ds_read_b128 v[188:191], v209 offset:64512\n\t" \
    "v_mfma_f32_16x16x32_bf16 v[88:91], v[168:171], a[112:115], v[88:91]\n\t" \
    "s_waitcnt vmcnt(7)\n\t" \
    "s_barrier\n\t" \
    "v_mfma_f32_16x16x32_bf16 v[88:91], v[172:175], a[120:123], v[88:91]\n\t" \
    "s_waitcnt lgkmcnt(2)\n\t" \
    "ds_read_b128 v[192:195], v210 offset:0\n\t" \
    "ds_read_b128 v[196:199], v210 offset:1024\n\t" \
    "v_mfma_f32_16x16x32_bf16 v[88:91], v[176:179], a[128:131], v[88:91]\n\t" \
    "buffer_load_dwordx4 v211, s[40:43], s48 offen offset:3072 lds\n\t" \
    "v_mfma_f32_16x16x32_bf16 v[88:91], v[180:183], a[136:139], v[88:91]\n\t" \
    "v_mfma_f32_16x16x32_bf16 v[92:95], v[160:163], a[100:103], v[92:95]\n\t" \
    "ds_read_b128 v[200:203], v210 offset:2048\n\t" \
    "ds_read_b128 v[204:207], v210 offset:3072\n\t" \
    "v_mfma_f32_16x16x32_bf16 v[92:95], v[164:167], a[108:111], v[92:95]\n\t" \
    "v_mfma_f32_16x16x32_bf16 v[92:95], v[168:171], a[116:119], v[92:95]\n\t" \
    "v_mfma_f32_16x16x32_bf16 v[92:95], v[172:175], a[124:127], v[92:95]\n\t" \
    "v_mfma_f32_16x16x32_bf16 v[92:95], v[176:179], a[132:135], v[92:95]\n\t" \
    "v_mfma_f32_16x16x32_bf16 v[92:95], v[180:183], a[140:143], v[92:95]\n\t" \
    "s_waitcnt lgkmcnt(4)\n\t" \
    "v_mfma_f32_16x16x32_bf16 v[96:99], v[184:187], a[96:99], v[96:99]\n\t" \
    "v_mfma_f32_16x16x32_bf16 v[96:99], v[188:191], a[104:107], v[96:99]\n\t" \
    "s_waitcnt lgkmcnt(2)\n\t" \
    "ds_read_b128 v[160:163], v210 offset:4096\n\t" \
    "ds_read_b128 v[164:167], v210 offset:5120\n\t" \
    "v_mfma_f32_16x16x32_bf16 v[96:99], v[192:195], a[112:115], v[96:99]\n\t" \
    "v_mfma_f32_16x16x32_bf16 v[96:99], v[196:199], a[120:123], v[96:99]\n\t" \
    "s_waitcnt lgkmcnt(2)\n\t" \
    "v_mfma_f32_16x16x32_bf16 v[96:99], v[200:203], a[128:131], v[96:99]\n\t" \
    "ds_read_b128 v[168:171], v210 offset:6144\n\t" \
    "ds_read_b128 v[172:175], v210 offset:7168\n\t" \
    "v_mfma_f32_16x16x32_bf16 v[96:99], v[204:207], a[136:139], v[96:99]\n\t" \
    "ds_read_b128 v[176:179], v210 offset:8192\n\t" \
    "ds_read_b128 v[180:183], v210 offset:9216\n\t" \
    "v_mfma_f32_16x16x32_bf16 v[100:103], v[184:187], a[100:103], v[100:103]\n\t" \
    "v_mfma_f32_16x16x32_bf16 v[100:103], v[188:191], a[108:111], v[100:103]\n\t" \
    "v_mfma_f32_16x16x32_bf16 v[100:103], v[192:195], a[116:119], v[100:103]\n\t" \
    "v_mfma_f32_16x16x32_bf16 v[100:103], v[196:199], a[124:127], v[100:103]\n\t" \
    "v_mfma_f32_16x16x32_bf16 v[100:103], v[200:203], a[132:135], v[100:103]\n\t" \
    "v_mfma_f32_16x16x32_bf16 v[100:103], v[204:207], a[140:143], v[100:103]\n\t" \
    "s_waitcnt lgkmcnt(4)\n\t" \
    "v_mfma_f32_16x16x32_bf16 v[88:91], v[160:163], a[144:147], v[88:91]\n\t" \
    "v_mfma_f32_16x16x32_bf16 v[88:91], v[164:167], a[152:155], v[88:91]\n\t" \
    "s_waitcnt lgkmcnt(2)\n\t" \
    "ds_read_b128 v[184:187], v210 offset:10240\n\t" \
    "ds_read_b128 v[188:191], v210 offset:11264\n\t" \
    "v_mfma_f32_16x16x32_bf16 v[88:91], v[168:171], a[160:163], v[88:91]\n\t" \
    "s_add_u32 s49, %[exin], 0\n\t" \
    "buffer_load_dwordx4 v[0:3], v211, s[40:43], s49 offen offset:0 sc1\n\t" \
    "v_mfma_f32_16x16x32_bf16 v[88:91], v[172:175], a[168:171], v[88:91]\n\t" \
    "s_waitcnt lgkmcnt(2)\n\t" \
    "ds_read_b128 v[192:195], v210 offset:12288\n\t" \
    "ds_read_b128 v[196:199], v210 offset:13312\n\t" \
    "v_mfma_f32_16x16x32_bf16 v[88:91], v[176:179], a[176:179], v[88:91]\n\t" \
    "v_mfma_f32_16x16x32_bf16 v[88:91], v[180:183], a[184:187], v[88:91]\n\t" \
    "v_mfma_f32_16x16x32_bf16 v[92:95], v[160:163], a[148:151], v[92:95]\n\t" \
    "ds_read_b128 v[200:203], v210 offset:14336\n\t" \
    "ds_read_b128 v[204:207], v210 offset:15360\n\t" \
    "v_mfma_f32_16x16x32_bf16 v[92:95], v[164:167], a[156:159], v[92:95]\n\t" \
    "buffer_load_dwordx4 v[4:7], v211, s[40:43], s49 offen offset:1024 sc1\n\t" \
    "v_mfma_f32_16x16x32_bf16 v[92:95], v[168:171], a[164:167], v[92:95]\n\t" \
    "v_mfma_f32_16x16x32_bf16 v[92:95], v[172:175], a[172:175], v[92:95]\n\t" \
    "v_mfma_f32_16x16x32_bf16 v[92:95], v[176:179], a[180:183], v[92:95]\n\t" \
    "v_mfma_f32_16x16x32_bf16 v[92:95], v[180:183], a[188:191], v[92:95]\n\t" \
    "s_waitcnt lgkmcnt(4)\n\t" \
    "buffer_load_dwordx4 v[8:11], v211, s[40:43], s49 offen offset:2048 sc1\n\t" \
    "v_mfma_f32_16x16x32_bf16 v[96:99], v[184:187], a[144:147], v[96:99]\n\t" \
    "s_waitcnt vmcnt(7)\n\t" \
    "s_barrier\n\t" \
    "v_mfma_f32_16x16x32_bf16 v[96:99], v[188:191], a[152:155], v[96:99]\n\t" \
    "s_waitcnt lgkmcnt(2)\n\t" \
    "ds_read_b128 v[160:163], v210 offset:16384\n\t" \
    "ds_read_b128 v[164:167], v210 offset:17408\n\t" \
    "v_mfma_f32_16x16x32_bf16 v[96:99], v[192:195], a[160:163], v[96:99]\n\t" \
    "v_mfma_f32_16x16x32_bf16 v[96:99], v[196:199], a[168:171], v[96:99]\n\t" \
    "s_waitcnt lgkmcnt(2)\n\t" \
    "buffer_load_dwordx4 v[12:15], v211, s[40:43], s49 offen offset:3072 sc1\n\t" \
    "v_mfma_f32_16x16x32_bf16 v[96:99], v[200:203], a[176:179], v[96:99]\n\t" \
    "s_add_u32 s49, %[exin], 4096\n\t" \
    "ds_read_b128 v[168:171], v210 offset:18432\n\t" \
    "ds_read_b128 v[172:175], v210 offset:19456\n\t" \
    "v_mfma_f32_16x16x32_bf16 v[96:99], v[204:207], a[184:187], v[96:99]\n\t" \
    "ds_read_b128 v[176:179], v210 offset:20480\n\t" \
    "ds_read_b128 v[180:183], v210 offset:21504\n\t" \
    "v_mfma_f32_16x16x32_bf16 v[100:103], v[184:187], a[148:151], v[100:103]\n\t" \
    "v_mfma_f32_16x16x32_bf16 v[100:103], v[188:191], a[156:159], v[100:103]\n\t" \
    "buffer_load_dwordx4 v[16:19], v211, s[40:43], s49 offen offset:0 sc1\n\t" \
    "v_mfma_f32_16x16x32_bf16 v[100:103], v[192:195], a[164:167], v[100:103]\n\t" \
    "v_mfma_f32_16x16x32_bf16 v[100:103], v[196:199], a[172:175], v[100:103]\n\t" \
    "v_mfma_f32_16x16x32_bf16 v[100:103], v[200:203], a[180:183], v[100:103]\n\t" \
    "v_mfma_f32_16x16x32_bf16 v[100:103], v[204:207], a[188:191], v[100:103]\n\t" \
    "s_waitcnt lgkmcnt(4)\n\t" \
    "buffer_load_dwordx4 v[20:23], v211, s[40:43], s49 offen offset:1024 sc1\n\t" \
    "v_mfma_f32_16x16x32_bf16 v[88:91], v[160:163], a[192:195], v[88:91]\n\t" \
    "v_mfma_f32_16x16x32_bf16 v[88:91], v[164:167], a[200:203], v[88:91]\n\t" \
    "s_waitcnt lgkmcnt(2)\n\t" \
    "ds_read_b128 v[184:187], v210 offset:22528\n\t" \
    "ds_read_b128 v[188:191], v210 offset:23552\n\t" \
    "v_mfma_f32_16x16x32_bf16 v[88:91], v[168:171], a[208:211], v[88:91]\n\t" \
    "v_mfma_f32_16x16x32_bf16 v[88:91], v[172:175], a[216:219], v[88:91]\n\t" \
    "s_waitcnt lgkmcnt(2)\n\t" \
    "ds_read_b128 v[192:195], v210 offset:24576\n\t" \
    "ds_read_b128 v[196:199], v210 offset:25600\n\t" \
    "v_mfma_f32_16x16x32_bf16 v[88:91], v[176:179], a[224:227], v[88:91]\n\t" \
    "buffer_load_dwordx4 v[24:27], v211, s[40:43], %[exin8] offen offset:0 sc1\n\t" \
    "v_mfma_f32_16x16x32_bf16 v[88:91], v[180:183], a[232:235], v[88:91]\n\t" \
    "v_mfma_f32_16x16x32_bf16 v[92:95], v[160:163], a[196:199], v[92:95]\n\t" \
    "ds_read_b128 v[200:203], v210 offset:26624\n\t" \
    "ds_read_b128 v[204:207], v210 offset:27648\n\t" \
    "v_mfma_f32_16x16x32_bf16 v[92:95], v[164:167], a[204:207], v[92:95]\n\t" \
    "v_mfma_f32_16x16x32_bf16 v[92:95], v[168:171], a[212:215], v[92:95]\n\t" \
    "buffer_load_dwordx4 v[28:31], v211, s[40:43], %[exin8] offen offset:1024 sc1\n\t" \
    "v_mfma_f32_16x16x32_bf16 v[92:95], v[172:175], a[220:223], v[92:95]\n\t" \
    "v_mfma_f32_16x16x32_bf16 v[92:95], v[176:179], a[228:231], v[92:95]\n\t" \
    "v_mfma_f32_16x16x32_bf16 v[92:95], v[180:183], a[236:239], v[92:95]\n\t" \
    "s_waitcnt lgkmcnt(4)\n\t" \
    "v_mfma_f32_16x16x32_bf16 v[96:99], v[184:187], a[192:195], v[96:99]\n\t" \
    "buffer_load_dwordx4 v[104:107], v211, s[40:43], %[exin8] offen offset:2048 sc1\n\t" \
    "v_mfma_f32_16x16x32_bf16 v[96:99], v[188:191], a[200:203], v[96:99]\n\t" \
    "s_waitcnt lgkmcnt(2)\n\t" \
    "ds_read_b128 v[160:163], v210 offset:28672\n\t" \
    "ds_read_b128 v[164:167], v210 offset:29696\n\t" \
    "v_mfma_f32_16x16x32_bf16 v[96:99], v[192:195], a[208:211], v[96:99]\n\t" \
    "s_waitcnt vmcnt(9)\n\t" \
    "s_barrier\n\t" \
    "v_mfma_f32_16x16x32_bf16 v[96:99], v[196:199], a[216:219], v[96:99]\n\t" \
    "s_waitcnt lgkmcnt(2)\n\t" \
    "v_mfma_f32_16x16x32_bf16 v[96:99], v[200:203], a[224:227], v[96:99]\n\t" \
    "ds_read_b128 v[168:171], v210 offset:30720\n\t" \
    "ds_read_b128 v[172:175], v210 offset:31744\n\t" \
    "v_mfma_f32_16x16x32_bf16 v[96:99], v[204:207], a[232:235], v[96:99]\n\t" \
    "ds_read_b128 v[176:179], v208 offset:0\n\t" \
    "ds_read_b128 v[180:183], v208 offset:1024\n\t" \
    "v_mfma_f32_16x16x32_bf16 v[100:103], v[184:187], a[196:199], v[100:103]\n\t" \
    "v_mfma_f32_16x16x32_bf16 v[100:103], v[188:191], a[204:207], v[100:103]\n\t" \
    "v_mfma_f32_16x16x32_bf16 v[100:103], v[192:195], a[212:215], v[100:103]\n\t" \
    "v_mfma_f32_16x16x32_bf16 v[100:103], v[196:199], a[220:223], v[100:103]\n\t" \
    "v_mfma_f32_16x16x32_bf16 v[100:103], v[200:203], a[228:231], v[100:103]\n\t" \
    "v_mfma_f32_16x16x32_bf16 v[100:103], v[204:207], a[236:239], v[100:103]\n\t" \
    "s_waitcnt lgkmcnt(4)\n\t" \
    "v_mfma_f32_16x16x32_bf16 v[88:91], v[160:163], a[240:243], v[88:91]\n\t" \
    "v_mfma_f32_16x16x32_bf16 v[88:91], v[164:167], a[248:251], v[88:91]\n\t" \
    "s_waitcnt lgkmcnt(2)\n\t" \
    "ds_read_b128 v[184:187], v208 offset:2048\n\t" \
    "ds_read_b128 v[188:191], v208 offset:3072\n\t" \
    "v_mfma_f32_16x16x32_bf16 v[88:91], v[168:171], v[128:131], v[88:91]\n\t" \
    "v_mfma_f32_16x16x32_bf16 v[88:91], v[172:175], v[136:139], v[88:91]\n\t" \
    "s_waitcnt lgkmcnt(2)\n\t" \
    "ds_read_b128 v[192:195], v208 offset:4096\n\t" \
    "ds_read_b128 v[196:199], v208 offset:5120\n\t" \
    "v_mfma_f32_16x16x32_bf16 v[88:91], v[176:179], v[144:147], v[88:91]\n\t" \
    "v_mfma_f32_16x16x32_bf16 v[88:91], v[180:183], v[152:155], v[88:91]\n\t" \
    "v_mfma_f32_16x16x32_bf16 v[92:95], v[160:163], a[244:247], v[92:95]\n\t" \
    "ds_read_b128 v[200:203], v208 offset:6144\n\t" \
    "ds_read_b128 v[204:207], v208 offset:7168\n\t" \
    "v_mfma_f32_16x16x32_bf16 v[92:95], v[164:167], a[252:255], v[92:95]\n\t" \
    "v_mfma_f32_16x16x32_bf16 v[92:95], v[168:171], v[132:135], v[92:95]\n\t" \
    "v_mfma_f32_16x16x32_bf16 v[92:95], v[172:175], v[140:143], v[92:95]\n\t" \
    "v_mfma_f32_16x16x32_bf16 v[92:95], v[176:179], v[148:151], v[92:95]\n\t" \
    "v_mfma_f32_16x16x32_bf16 v[92:95], v[180:183], v[156:159], v[92:95]\n\t" \
    "s_waitcnt lgkmcnt(4)\n\t" \
    "v_mfma_f32_16x16x32_bf16 v[96:99], v[184:187], a[240:243], v[96:99]\n\t" \
    "v_mfma_f32_16x16x32_bf16 v[96:99], v[188:191], a[248:251], v[96:99]\n\t" \
    "s_waitcnt lgkmcnt(2)\n\t" \
    "v_mfma_f32_16x16x32_bf16 v[96:99], v[192:195], v[128:131], v[96:99]\n\t" \
    "v_mfma_f32_16x16x32_bf16 v[96:99], v[196:199], v[136:139], v[96:99]\n\t" \
    "s_waitcnt lgkmcnt(0)\n\t" \
    "v_mfma_f32_16x16x32_bf16 v[96:99], v[200:203], v[144:147], v[96:99]\n\t" \
    "v_mfma_f32_16x16x32_bf16 v[96:99], v[204:207], v[152:155], v[96:99]\n\t" \
    "v_mfma_f32_16x16x32_bf16 v[100:103], v[184:187], a[244:247], v[100:103]\n\t" \
    "v_mfma_f32_16x16x32_bf16 v[100:103], v[188:191], a[252:255], v[100:103]\n\t" \
    "v_mfma_f32_16x16x32_bf16 v[100:103], v[192:195], v[132:135], v[100:103]\n\t" \
    "v_mfma_f32_16x16x32_bf16 v[100:103], v[196:199], v[140:143], v[100:103]\n\t" \
    "v_mfma_f32_16x16x32_bf16 v[100:103], v[200:203], v[148:151], v[100:103]\n\t" \
    "v_mfma_f32_16x16x32_bf16 v[100:103], v[204:207], v[156:159], v[100:103]\n\t" \
    "s_waitcnt vmcnt(0)\n\t" \
    "v_max_u32 v240, v0, v1\n\t" \
    "v_max_u32 v241, v2, v3\n\t" \
    "v_max3_u32 v240, v4, v5, v240\n\t" \
    "v_max3_u32 v241, v6, v7, v241\n\t" \
    "v_max3_u32 v240, v8, v9, v240\n\t" \
    "v_max3_u32 v241, v10, v11, v241\n\t" \
    "v_max3_u32 v240, v12, v13, v240\n\t" \
    "v_max3_u32 v241, v14, v15, v241\n\t" \
    "v_max3_u32 v240, v16, v17, v240\n\t" \
    "v_max3_u32 v241, v18, v19, v241\n\t" \
    "v_max3_u32 v240, v20, v21, v240\n\t" \
    "v_max3_u32 v241, v22, v23, v241\n\t" \
    "v_max3_u32 v240, v24, v25, v240\n\t" \
    "v_max3_u32 v241, v26, v27, v241\n\t" \
    "v_max3_u32 v240, v28, v29, v240\n\t" \
    "v_max3_u32 v241, v30, v31, v241\n\t" \
    "v_max3_u32 v240, v104, v105, v240\n\t" \
    "v_max3_u32 v241, v106, v107, v241\n\t" \
    "v_max_u32 v240, v240, v241\n\t" \
    "v_cmp_eq_u32 vcc, -1, v240\n\t" \
    "s_nop 4\n\t" \
    "s_cbranch_vccnz Lexsl21_%=\n\t" \
    "Lexok20_%=:\n\t" \
    "v_dot2_f32_bf16 v88, v0, s53, v88\n\t" \
    "v_dot2_f32_bf16 v89, v0, s54, v89\n\t" \
    "v_dot2_f32_bf16 v90, v1, s53, v90\n\t" \
    "v_dot2_f32_bf16 v91, v1, s54, v91\n\t" \
    "v_dot2_f32_bf16 v96, v2, s53, v96\n\t" \
    "v_dot2_f32_bf16 v97, v2, s54, v97\n\t" \
    "v_dot2_f32_bf16 v98, v3, s53, v98\n\t" \
    "v_dot2_f32_bf16 v99, v3, s54, v99\n\t" \
    "v_dot2_f32_bf16 v88, v8, s53, v88\n\t" \
    "v_dot2_f32_bf16 v89, v8, s54, v89\n\t" \
    "v_dot2_f32_bf16 v90, v9, s53, v90\n\t" \
    "v_dot2_f32_bf16 v91, v9, s54, v91\n\t" \
    "v_dot2_f32_bf16 v96, v10, s53, v96\n\t" \
    "v_dot2_f32_bf16 v97, v10, s54, v97\n\t" \
    "v_dot2_f32_bf16 v98, v11, s53, v98\n\t" \
    "v_dot2_f32_bf16 v99, v11, s54, v99\n\t" \
    "v_dot2_f32_bf16 v88, v16, s53, v88\n\t" \
    "v_dot2_f32_bf16 v89, v16, s54, v89\n\t" \
    "v_dot2_f32_bf16 v90, v17, s53, v90\n\t" \
    "v_dot2_f32_bf16 v91, v17, s54, v91\n\t" \
    "v_dot2_f32_bf16 v96, v18, s53, v96\n\t" \
    "v_dot2_f32_bf16 v97, v18, s54, v97\n\t" \
    "v_dot2_f32_bf16 v98, v19, s53, v98\n\t" \
    "v_dot2_f32_bf16 v99, v19, s54, v99\n\t" \
    "v_fma_f32 v88, v88, v216, v108\n\t" \
    "v_fma_f32 v89, v89, v216, v109\n\t" \
    "v_fma_f32 v90, v90, v216, v110\n\t" \
    "v_fma_f32 v91, v91, v216, v111\n\t" \
    "v_fma_f32 v96, v96, v216, v112\n\t" \
    "v_fma_f32 v97, v97, v216, v113\n\t" \
    "v_fma_f32 v98, v98, v216, v114\n\t" \
    "v_fma_f32 v99, v99, v216, v115\n\t" \
    "v_cvt_pk_bf16_f32 v232, v88, v89\n\t" \
    "v_cvt_pk_bf16_f32 v233, v90, v91\n\t" \
    "v_cvt_pk_bf16_f32 v234, v96, v97\n\t" \
    "v_cvt_pk_bf16_f32 v235, v98, v99\n\t" \
    "s_add_u32 s50, %[oown], 0\n\t" \
    "buffer_store_dwordx4 v[232:235], v219, s[44:47], s50 offen sc1\n\t" \
    "v_dot2_f32_bf16 v92, v4, s53, v92\n\t" \
    "v_dot2_f32_bf16 v93, v4, s54, v93\n\t" \
    "v_dot2_f32_bf16 v94, v5, s53, v94\n\t" \
    "v_dot2_f32_bf16 v95, v5, s54, v95\n\t" \
    "v_dot2_f32_bf16 v100, v6, s53, v100\n\t" \
    "v_dot2_f32_bf16 v101, v6, s54, v101\n\t" \
    "v_dot2_f32_bf16 v102, v7, s53, v102\n\t" \
    "v_dot2_f32_bf16 v103, v7, s54, v103\n\t" \
    "v_dot2_f32_bf16 v92, v12, s53, v92\n\t" \
    "v_dot2_f32_bf16 v93, v12, s54, v93\n\t" \
    "v_dot2_f32_bf16 v94, v13, s53, v94\n\t" \
    "v_dot2_f32_bf16 v95, v13, s54, v95\n\t" \
    "v_dot2_f32_bf16 v100, v14, s53, v100\n\t" \
    "v_dot2_f32_bf16 v101, v14, s54, v101\n\t" \
    "v_dot2_f32_bf16 v102, v15, s53, v102\n\t" \
    "v_dot2_f32_bf16 v103, v15, s54, v103\n\t" \
    "v_dot2_f32_bf16 v92, v20, s53, v92\n\t" \
    "v_dot2_f32_bf16 v93, v20, s54, v93\n\t" \
    "v_dot2_f32_bf16 v94, v21, s53, v94\n\t" \
    "v_dot2_f32_bf16 v95, v21, s54, v95\n\t" \
    "v_dot2_f32_bf16 v100, v22, s53, v100\n\t" \
    "v_dot2_f32_bf16 v101, v22, s54, v101\n\t" \
    "v_dot2_f32_bf16 v102, v23, s53, v102\n\t" \
    "v_dot2_f32_bf16 v103, v23, s54, v103\n\t" \
    "v_fma_f32 v92, v92, v217, v116\n\t" \
    "v_fma_f32 v93, v93, v217, v117\n\t" \
    "v_fma_f32 v94, v94, v217, v118\n\t" \
    "v_fma_f32 v95, v95, v217, v119\n\t" \
    "v_fma_f32 v100, v100, v217, v120\n\t" \
    "v_fma_f32 v101, v101, v217, v121\n\t" \
    "v_fma_f32 v102, v102, v217, v122\n\t" \
    "v_fma_f32 v103, v103, v217, v123\n\t" \
    "v_cvt_pk_bf16_f32 v236, v92, v93\n\t" \
    "v_cvt_pk_bf16_f32 v237, v94, v95\n\t" \
    "v_cvt_pk_bf16_f32 v238, v100, v101\n\t" \
    "v_cvt_pk_bf16_f32 v239, v102, v103\n\t" \
    "s_add_u32 s50, %[oown], 18432\n\t" \
    "buffer_store_dwordx4 v[236:239], v219, s[44:47], s50 offen sc1\n\t" \
    "s_cmp_eq_u32 %[isred], 0\n\t" \
    "s_cbranch_scc1 Lend22_%=\n\t" \
    "v_dot2_f32_bf16 v80, v24, s53, v80\n\t" \
    "v_dot2_f32_bf16 v81, v24, s54, v81\n\t" \
    "v_dot2_f32_bf16 v82, v25, s53, v82\n\t" \
    "v_dot2_f32_bf16 v83, v25, s54, v83\n\t" \
    "v_dot2_f32_bf16 v80, v28, s53, v80\n\t" \
    "v_dot2_f32_bf16 v81, v28, s54, v81\n\t" \
    "v_dot2_f32_bf16 v82, v29, s53, v82\n\t" \
    "v_dot2_f32_bf16 v83, v29, s54, v83\n\t" \
    "v_dot2_f32_bf16 v80, v104, s53, v80\n\t" \
    "v_dot2_f32_bf16 v81, v104, s54, v81\n\t" \
    "v_dot2_f32_bf16 v82, v105, s53, v82\n\t" \
    "v_dot2_f32_bf16 v83, v105, s54, v83\n\t" \
    "v_fma_f32 v80, v80, v216, v244\n\t" \
    "v_fma_f32 v81, v81, v216, v245\n\t" \
    "v_fma_f32 v82, v82, v216, v246\n\t" \
    "v_fma_f32 v83, v83, v216, v247\n\t" \
    "v_cvt_pk_bf16_f32 v240, v80, v81\n\t" \
    "v_cvt_pk_bf16_f32 v241, v82, v83\n\t" \
    "s_add_u32 s50, %[o8], 0\n\t" \
    "buffer_store_dwordx2 v[240:241], v218, s[44:47], s50 offen sc1\n\t" \
    "v_dot2_f32_bf16 v84, v26, s53, v84\n\t" \
    "v_dot2_f32_bf16 v85, v26, s54, v85\n\t" \
    "v_dot2_f32_bf16 v86, v27, s53, v86\n\t" \
    "v_dot2_f32_bf16 v87, v27, s54, v87\n\t" \
    "v_dot2_f32_bf16 v84, v30, s53, v84\n\t" \
    "v_dot2_f32_bf16 v85, v30, s54, v85\n\t" \
    "v_dot2_f32_bf16 v86, v31, s53, v86\n\t" \
    "v_dot2_f32_bf16 v87, v31, s54, v87\n\t" \
    "v_dot2_f32_bf16 v84, v106, s53, v84\n\t" \
    "v_dot2_f32_bf16 v85, v106, s54, v85\n\t" \
    "v_dot2_f32_bf16 v86, v107, s53, v86\n\t" \
    "v_dot2_f32_bf16 v87, v107, s54, v87\n\t" \
    "v_fma_f32 v84, v84, v217, v248\n\t" \
    "v_fma_f32 v85, v85, v217, v249\n\t" \
    "v_fma_f32 v86, v86, v217, v250\n\t" \
    "v_fma_f32 v87, v87, v217, v251\n\t" \
    "v_cvt_pk_bf16_f32 v242, v84, v85\n\t" \
    "v_cvt_pk_bf16_f32 v243, v86, v87\n\t" \
    "s_add_u32 s50, %[o8], 18432\n\t" \
    "buffer_store_dwordx2 v[242:243], v218, s[44:47], s50 offen sc1\n\t" \
    "s_branch Lend22_%=\n\t" \
    "Lsl3_%=:\n\t" \
    "s_mov_b32 s52, 0x4000\n\t" \
    "Lsp2_%=:\n\t" \
    "s_sleep 1\n\t" \
    "buffer_load_dword v213, off, s[40:43], %[flagin] sc1\n\t" \
    "s_waitcnt vmcnt(0)\n\t" \
    "v_readfirstlane_b32 s51, v213\n\t" \
    "s_bfe_u32 s51, s51, 0x80000\n\t" \
    "s_cmp_ge_u32 s51, 8\n\t" \
    "s_cbranch_scc1 Lok1_%=\n\t" \
    "s_sub_u32 s52, s52, 1\n\t" \
    "s_cmp_lg_u32 s52, 0\n\t" \
    "s_cbranch_scc1 Lsp2_%=\n\t" \
    "s_branch Lok1_%=\n\t" \
    "Lsl6_%=:\n\t" \
    "s_mov_b32 s52, 0x4000\n\t" \
    "Lsp5_%=:\n\t" \
    "s_sleep 1\n\t" \
    "buffer_load_dword v213, off, s[40:43], %[flagin] sc1\n\t" \
    "s_waitcnt vmcnt(0)\n\t" \
    "v_readfirstlane_b32 s51, v213\n\t" \
    "s_bfe_u32 s51, s51, 0x80008\n\t" \
    "s_cmp_ge_u32 s51, 8\n\t" \
    "s_cbranch_scc1 Lok4_%=\n\t" \
    "s_sub_u32 s52, s52, 1\n\t" \
    "s_cmp_lg_u32 s52, 0\n\t" \
    "s_cbranch_scc1 Lsp5_%=\n\t" \
    "s_branch Lok4_%=\n\t" \
    "Lsl9_%=:\n\t" \
    "s_mov_b32 s52, 0x4000\n\t" \
    "Lsp8_%=:\n\t" \
    "s_sleep 1\n\t" \
    "buffer_load_dword v213, off, s[40:43], %[cflag] sc1\n\t" \
    "s_waitcnt vmcnt(0)\n\t" \
    "v_readfirstlane_b32 s51, v213\n\t" \
    "s_bfe_u32 s51, s51, 0x80000\n\t" \
    "s_cmp_ge_u32 s51, 16\n\t" \
    "s_cbranch_scc1 Lok7_%=\n\t" \
    "s_sub_u32 s52, s52, 1\n\t" \
    "s_cmp_lg_u32 s52, 0\n\t" \
    "s_cbranch_scc1 Lsp8_%=\n\t" \
    "s_branch Lok7_%=\n\t" \
    "Lstsl12_%=:\n\t" \
    "s_mov_b32 s52, 0x4000\n\t" \
    "Lst10_%=:\n\t" \
    "buffer_load_dwordx4 v[108:111], v243, s[40:43], %[stld] offen offset:0 sc1\n\t" \
    "buffer_load_dwordx4 v[112:115], v243, s[40:43], %[stld] offen offset:16 sc1\n\t" \
    "buffer_load_dwordx4 v[116:119], v243, s[40:43], %[stld] offen offset:512 sc1\n\t" \
    "buffer_load_dwordx4 v[120:123], v243, s[40:43], %[stld] offen offset:528 sc1\n\t" \
    "s_waitcnt vmcnt(0)\n\t" \
    "v_max_u32 v240, v108, v109\n\t" \
    "v_max_u32 v241, v110, v111\n\t" \
    "v_max3_u32 v240, v112, v113, v240\n\t" \
    "v_max3_u32 v241, v114, v115, v241\n\t" \
    "v_max3_u32 v240, v116, v117, v240\n\t" \
    "v_max3_u32 v241, v118, v119, v241\n\t" \
    "v_max3_u32 v240, v120, v121, v240\n\t" \
    "v_max3_u32 v241, v122, v123, v241\n\t" \
    "v_max_u32 v240, v240, v241\n\t" \
    "v_cmp_eq_u32 vcc, -1, v240\n\t" \
    "s_nop 4\n\t" \
    "s_cbranch_vccz Lstok11_%=\n\t" \
    "s_sub_u32 s52, s52, 1\n\t" \
    "s_cmp_lg_u32 s52, 0\n\t" \
    "s_cbranch_scc1 Lst10_%=\n\t" \
    "s_branch Lstok11_%=\n\t" \
    "Lsl15_%=:\n\t" \
    "s_mov_b32 s52, 0x4000\n\t" \
    "Lsp14_%=:\n\t" \
    "s_sleep 1\n\t" \
    "buffer_load_dword v213, off, s[40:43], %[flagin] sc1\n\t" \
    "s_waitcnt vmcnt(0)\n\t" \
    "v_readfirstlane_b32 s51, v213\n\t" \
    "s_bfe_u32 s51, s51, 0x80010\n\t" \
    "s_cmp_ge_u32 s51, 8\n\t" \
    "s_cbranch_scc1 Lok13_%=\n\t" \
    "s_sub_u32 s52, s52, 1\n\t" \
    "s_cmp_lg_u32 s52, 0\n\t" \
    "s_cbranch_scc1 Lsp14_%=\n\t" \
    "s_branch Lok13_%=\n\t" \
    "Lsl18_%=:\n\t" \
    "s_mov_b32 s52, 0x4000\n\t" \
    "Lsp17_%=:\n\t" \
    "s_sleep 1\n\t" \
    "buffer_load_dword v213, off, s[40:43], %[flagin] sc1\n\t" \
    "s_waitcnt vmcnt(0)\n\t" \
    "v_readfirstlane_b32 s51, v213\n\t" \
    "s_bfe_u32 s51, s51, 0x80018\n\t" \
    "s_cmp_ge_u32 s51, 8\n\t" \
    "s_cbranch_scc1 Lok16_%=\n\t" \
    "s_sub_u32 s52, s52, 1\n\t" \
    "s_cmp_lg_u32 s52, 0\n\t" \
    "s_cbranch_scc1 Lsp17_%=\n\t" \
    "s_branch Lok16_%=\n\t" \
    "Lexsl21_%=:\n\t" \
    "s_mov_b32 s52, 0x4000\n\t" \
    "Lex19_%=:\n\t" \
    "s_add_u32 s49, %[exin], 0\n\t" \
    "buffer_load_dwordx4 v[0:3], v211, s[40:43], s49 offen offset:0 sc1\n\t" \
    "buffer_load_dwordx4 v[4:7], v211, s[40:43], s49 offen offset:1024 sc1\n\t" \
    "buffer_load_dwordx4 v[8:11], v211, s[40:43], s49 offen offset:2048 sc1\n\t" \
    "buffer_load_dwordx4 v[12:15], v211, s[40:43], s49 offen offset:3072 sc1\n\t" \
    "s_add_u32 s49, %[exin], 4096\n\t" \
    "buffer_load_dwordx4 v[16:19], v211, s[40:43], s49 offen offset:0 sc1\n\t" \
    "buffer_load_dwordx4 v[20:23], v211, s[40:43], s49 offen offset:1024 sc1\n\t" \
    "buffer_load_dwordx4 v[24:27], v211, s[40:43], %[exin8] offen offset:0 sc1\n\t" \
    "buffer_load_dwordx4 v[28:31], v211, s[40:43], %[exin8] offen offset:1024 sc1\n\t" \
    "buffer_load_dwordx4 v[104:107], v211, s[40:43], %[exin8] offen offset:2048 sc1\n\t" \
    "s_waitcnt vmcnt(0)\n\t" \
    "v_max_u32 v240, v0, v1\n\t" \
    "v_max_u32 v241, v2, v3\n\t" \
    "v_max3_u32 v240, v4, v5, v240\n\t" \
    "v_max3_u32 v241, v6, v7, v241\n\t" \
    "v_max3_u32 v240, v8, v9, v240\n\t" \
    "v_max3_u32 v241, v10, v11, v241\n\t" \
    "v_max3_u32 v240, v12, v13, v240\n\t" \
    "v_max3_u32 v241, v14, v15, v241\n\t" \
    "v_max3_u32 v240, v16, v17, v240\n\t" \
    "v_max3_u32 v241, v18, v19, v241\n\t" \
    "v_max3_u32 v240, v20, v21, v240\n\t" \
    "v_max3_u32 v241, v22, v23, v241\n\t" \
    "v_max3_u32 v240, v24, v25, v240\n\t" \
    "v_max3_u32 v241, v26, v27, v241\n\t" \
    "v_max3_u32 v240, v28, v29, v240\n\t" \
    "v_max3_u32 v241, v30, v31, v241\n\t" \
    "v_max3_u32 v240, v104, v105, v240\n\t" \
    "v_max3_u32 v241, v106, v107, v241\n\t" \
    "v_max_u32 v240, v240, v241\n\t" \
    "v_cmp_eq_u32 vcc, -1, v240\n\t" \
    "s_nop 4\n\t" \
    "s_cbranch_vccz Lexok20_%=\n\t" \
    "s_sub_u32 s52, s52, 1\n\t" \
    "s_cmp_lg_u32 s52, 0\n\t" \
    "s_cbranch_scc1 Lex19_%=\n\t" \
    "s_branch Lexok20_%=\n\t" \
    "Lend22_%=:\n\t"
#define FFN_ASM_CLOBBERS "v0", "v1", "v2", "v3", "v4", "v5", "v6", "v7", "v8", "v9", "v10", "v11", "v12", "v13", "v14", "v15", "v16", "v17", "v18", "v19", "v20", "v21", "v22", "v23", "v24", "v25", "v26", "v27", "v28", "v29", "v30", "v31", "v32", "v33", "v34", "v35", "v36", "v37", "v38", "v39", "v40", "v41", "v42", "v43", "v44", "v45", "v46", "v47", "v48", "v49", "v50", "v51", "v52", "v53", "v54", "v55", "v56", "v57", "v58", "v59", "v60", "v61", "v62", "v63", "v64", "v65", "v66", "v67", "v68", "v69", "v70", "v71", "v72", "v73", "v74", "v75", "v76", "v77", "v78", "v79", "v80", "v81", "v82", "v83", "v84", "v85", "v86", "v87", "v88", "v89", "v90", "v91", "v92", "v93", "v94", "v95", "v96", "v97", "v98", "v99", "v100", "v101", "v102", "v103", "v104", "v105", "v106", "v107", "v108", "v109", "v110", "v111", "v112", "v113", "v114", "v115", "v116", "v117", "v118", "v119", "v120", "v121", "v122", "v123", "v124", "v125", "v126", "v127", "v128", "v129", "v130", "v131", "v132", "v133", "v134", "v135", "v136", "v137", "v138", "v139", "v140", "v141", "v142", "v143", "v144", "v145", "v146", "v147", "v148", "v149", "v150", "v151", "v152", "v153", "v154", "v155", "v156", "v157", "v158", "v159", "v160", "v161", "v162", "v163", "v164", "v165", "v166", "v167", "v168", "v169", "v170", "v171", "v172", "v173", "v174", "v175", "v176", "v177", "v178", "v179", "v180", "v181", "v182", "v183", "v184", "v185", "v186", "v187", "v188", "v189", "v190", "v191", "v192", "v193", "v194", "v195", "v196", "v197", "v198", "v199", "v200", "v201", "v202", "v203", "v204", "v205", "v206", "v207", "v208", "v209", "v210", "v211", "v212", "v213", "v214", "v215", "v216", "v217", "v218", "v219", "v220", "v221", "v222", "v223", "v224", "v225", "v226", "v227", "v228", "v229", "v230", "v231", "v232", "v233", "v234", "v235", "v236", "v237", "v238", "v239", "v240", "v241", "v242", "v243", "v244", "v245", "v246", "v247", "v248", "v249", "v250", "v251", "v252", "v253", "v254", "v255", "a0", "a1", "a2", "a3", "a4", "a5", "a6", "a7", "a8", "a9", "a10", "a11", "a12", "a13", "a14", "a15", "a16", "a17", "a18", "a19", "a20", "a21", "a22", "a23", "a24", "a25", "a26", "a27", "a28", "a29", "a30", "a31", "a32", "a33", "a34", "a35", "a36", "a37", "a38", "a39", "a40", "a41", "a42", "a43", "a44", "a45", "a46", "a47", "a48", "a49", "a50", "a51", "a52", "a53", "a54", "a55", "a56", "a57", "a58", "a59", "a60", "a61", "a62", "a63", "a64", "a65", "a66", "a67", "a68", "a69", "a70", "a71", "a72", "a73", "a74", "a75", "a76", "a77", "a78", "a79", "a80", "a81", "a82", "a83", "a84", "a85", "a86", "a87", "a88", "a89", "a90", "a91", "a92", "a93", "a94", "a95", "a96", "a97", "a98", "a99", "a100", "a101", "a102", "a103", "a104", "a105", "a106", "a107", "a108", "a109", "a110", "a111", "a112", "a113", "a114", "a115", "a116", "a117", "a118", "a119", "a120", "a121", "a122", "a123", "a124", "a125", "a126", "a127", "a128", "a129", "a130", "a131", "a132", "a133", "a134", "a135", "a136", "a137", "a138", "a139", "a140", "a141", "a142", "a143", "a144", "a145", "a146", "a147", "a148", "a149", "a150", "a151", "a152", "a153", "a154", "a155", "a156", "a157", "a158", "a159", "a160", "a161", "a162", "a163", "a164", "a165", "a166", "a167", "a168", "a169", "a170", "a171", "a172", "a173", "a174", "a175", "a176", "a177", "a178", "a179", "a180", "a181", "a182", "a183", "a184", "a185", "a186", "a187", "a188", "a189", "a190", "a191", "a192", "a193", "a194", "a195", "a196", "a197", "a198", "a199", "a200", "a201", "a202", "a203", "a204", "a205", "a206", "a207", "a208", "a209", "a210", "a211", "a212", "a213", "a214", "a215", "a216", "a217", "a218", "a219", "a220", "a221", "a222", "a223", "a224", "a225", "a226", "a227", "a228", "a229", "a230", "a231", "a232", "a233", "a234", "a235", "a236", "a237", "a238", "a239", "a240", "a241", "a242", "a243", "a244", "a245", "a246", "a247", "a248", "a249", "a250", "a251", "a252", "a253", "a254", "a255", "s40", "s41", "s42", "s43", "s44", "s45", "s46", "s47", "s48", "s49", "s50", "s51", "s52", "s53", "s54", "s55", "s56", "s57", "s58", "s59", "s60", "s61", "s62", "s63", "s64", "s65", "s66", "s67", "s68", "s69", "s70", "s71", "s72", "s73", "s74", "s75", "s76", "s77", "s78", "s79", "s80", "s81", "s82", "s83", "s84", "s85", "s86", "s87", "s88", "s89", "s90", "s91", "s92", "s93", "s94", "s95", "vcc", "scc", "memory"
#define W1_ORDER {0,8,9,24,25,26,48,49,50,72,73,74,96,97,98,108,109,110,1,10,11,27,28,29,51,52,53,75,76,77,99,100,101,111,112,113,2,12,13,30,31,32,54,55,56,78,79,80,102,103,104,114,115,116,3,14,15,33,34,35,57,58,59,81,82,83,105,106,107,117,118,119,4,16,17,36,37,38,60,61,62,84,85,86,120,121,122,132,133,134,5,18,19,39,40,41,63,64,65,87,88,89,123,124,125,135,136,137,6,20,21,42,43,44,66,67,68,90,91,92,126,127,128,138,139,140,7,22,23,45,46,47,69,70,71,93,94,95,129,130,131,141,142,143,144,145,146,147,148,149,168,169,170,171,172,173,192,193,194,195,196,197,150,151,152,153,154,155,174,175,176,177,178,179,198,199,200,201,202,203,156,157,158,159,160,161,180,181,182,183,184,185,204,205,206,207,208,209,162,163,164,165,166,167,186,187,188,189,190,191,210,211,212,213,214,215,216,217,218,228,229,230,240,241,242,258,259,260,276,277,278,294,295,296,219,220,221,231,232,233,243,244,245,261,262,263,279,280,281,297,298,299,222,223,224,234,235,236,246,247,248,264,265,266,282,283,284,300,301,302,225,226,227,237,238,239,249,250,251,267,268,269,285,286,287,303,304,305,252,253,254,270,271,272,288,289,290,306,307,308,312,313,314,318,319,320,255,256,257,273,274,275,291,292,293,309,310,311,315,316,317,321,322,323}
#define W2_ORDER {0,4,8,12,16,20,24,252,258,1,5,9,13,17,21,25,253,259,2,6,10,14,18,22,26,254,260,3,7,11,15,19,23,27,255,261,28,32,36,40,44,48,52,256,262,29,33,37,41,45,49,53,257,263,30,34,38,42,46,50,54,264,270,31,35,39,43,47,51,55,265,271,56,60,64,68,72,76,80,266,272,57,61,65,69,73,77,81,267,273,58,62,66,70,74,78,82,268,274,59,63,67,71,75,79,83,269,275,84,88,92,96,100,104,108,276,282,85,89,93,97,101,105,109,277,283,86,90,94,98,102,106,110,278,284,87,91,95,99,103,107,111,279,285,112,116,120,124,128,132,136,280,286,113,117,121,125,129,133,137,281,287,114,118,122,126,130,134,138,288,294,115,119,123,127,131,135,139,289,295,140,144,148,152,156,160,164,290,296,141,145,149,153,157,161,165,291,297,142,146,150,154,158,162,166,292,298,143,147,151,155,159,163,167,293,299,168,172,176,180,184,188,192,300,306,169,173,177,181,185,189,193,301,307,170,174,178,182,186,190,194,302,308,171,175,179,183,187,191,195,303,309,196,200,204,208,212,216,220,304,310,197,201,205,209,213,217,221,305,311,198,202,206,210,214,218,222,312,318,199,203,207,211,215,219,223,313,319,224,228,232,236,240,244,248,314,320,225,229,233,237,241,245,249,315,321,226,230,234,238,242,246,250,316,322,227,231,235,239,243,247,251,317,323}
#define CONV_ASM_BODY \
    "ds_read2_b32 v[128:129], %[ka2] offset0:0 offset1:1\n\t" \
    "ds_read2_b32 v[130:131], %[ka2] offset0:2 offset1:3\n\t" \
    "ds_read_b128 v[64:67], %[ub3] offset:1024\n\t" \
    "ds_read_b128 v[68:71], %[ub3] offset:3072\n\t" \
    "ds_read_b128 v[72:75], %[ub3] offset:5120\n\t" \
    "ds_read_b128 v[76:79], %[ub3] offset:7168\n\t" \
    "ds_read_b128 v[80:83], %[ub3] offset:9216\n\t" \
    "ds_read_b128 v[84:87], %[ub3] offset:11264\n\t" \
    "ds_read_b128 v[88:91], %[ub3] offset:13312\n\t" \
    "ds_read_b128 v[92:95], %[ub3] offset:15360\n\t" \
    "ds_read2_b32 v[132:133], %[ka0] offset0:0 offset1:1\n\t" \
    "ds_read2_b32 v[134:135], %[ka0] offset0:2 offset1:3\n\t" \
    "ds_read2_b32 v[136:137], %[ka0] offset0:128 offset1:129\n\t" \
    "ds_read2_b32 v[138:139], %[ka0] offset0:130 offset1:131\n\t" \
    "s_waitcnt lgkmcnt(11)\n\t" \
    "v_mfma_f32_16x16x32_bf16 %[y0], v[128:131], v[64:67], 0\n\t" \
    "s_waitcnt lgkmcnt(10)\n\t" \
    "ds_read2_b32 v[140:141], %[ka1] offset0:128 offset1:129\n\t" \
    "ds_read2_b32 v[142:143], %[ka1] offset0:130 offset1:131\n\t" \
    "v_mfma_f32_16x16x32_bf16 %[y1], v[128:131], v[68:71], 0\n\t" \
    "s_waitcnt lgkmcnt(11)\n\t" \
    "ds_read_b128 v[152:155], %[uw] offset:0\n\t" \
    "ds_read_b128 v[96:99], %[ub2] offset:1024\n\t" \
    "v_mfma_f32_16x16x32_bf16 %[y2], v[128:131], v[72:75], 0\n\t" \
    "s_waitcnt lgkmcnt(12)\n\t" \
    "v_mfma_f32_16x16x32_bf16 %[y3], v[128:131], v[76:79], 0\n\t" \
    "s_waitcnt lgkmcnt(11)\n\t" \
    "ds_read_b128 v[100:103], %[ub2] offset:3072\n\t" \
    "v_mfma_f32_16x16x32_bf16 %[y4], v[128:131], v[80:83], 0\n\t" \
    "s_waitcnt lgkmcnt(11)\n\t" \
    "v_mfma_f32_16x16x32_bf16 %[y5], v[128:131], v[84:87], 0\n\t" \
    "s_waitcnt lgkmcnt(10)\n\t" \
    "ds_read_b128 v[104:107], %[ub2] offset:5120\n\t" \
    "v_mfma_f32_16x16x32_bf16 %[y6], v[128:131], v[88:91], 0\n\t" \
    "s_waitcnt lgkmcnt(10)\n\t" \
    "v_mfma_f32_16x16x32_bf16 %[y7], v[128:131], v[92:95], 0\n\t" \
    "s_waitcnt lgkmcnt(8)\n\t" \
    "ds_read_b128 v[108:111], %[ub2] offset:7168\n\t" \
    "v_mfma_f32_16x16x32_bf16 %[y4], v[132:135], v[64:67], %[y4]\n\t" \
    "ds_read2_b32 v[144:145], %[ka1] offset0:0 offset1:1\n\t" \
    "ds_read2_b32 v[146:147], %[ka1] offset0:2 offset1:3\n\t" \
    "v_mfma_f32_16x16x32_bf16 %[y5], v[132:135], v[68:71], %[y5]\n\t" \
    "ds_read_b128 v[112:115], %[ub2] offset:9216\n\t" \
    "v_mfma_f32_16x16x32_bf16 %[y6], v[132:135], v[72:75], %[y6]\n\t" \
    "v_mfma_f32_16x16x32_bf16 %[y7], v[132:135], v[76:79], %[y7]\n\t" \
    "ds_read_b128 v[116:119], %[ub2] offset:11264\n\t" \
    "v_mfma_f32_16x16x32_bf16 v[160:163], v[64:67], %[bd7], 0\n\t" \
    "s_waitcnt lgkmcnt(8)\n\t" \
    "v_mfma_f32_16x16x32_bf16 v[156:159], v[152:155], %[bd9], 0\n\t" \
    "ds_read_b128 v[152:155], %[uw] offset:2048\n\t" \
    "ds_read_b128 v[120:123], %[ub2] offset:13312\n\t" \
    "v_mfma_f32_16x16x32_bf16 %[y3], v[136:139], v[64:67], %[y3]\n\t" \
    "ds_read2_b32 v[148:149], %[ka1] offset0:240 offset1:241\n\t" \
    "ds_read2_b32 v[150:151], %[ka1] offset0:242 offset1:243\n\t" \
    "v_mfma_f32_16x16x32_bf16 %[y4], v[136:139], v[68:71], %[y4]\n\t" \
    "ds_read_b128 v[124:127], %[ub2] offset:15360\n\t" \
    "v_mfma_f32_16x16x32_bf16 %[y5], v[136:139], v[72:75], %[y5]\n\t" \
    "v_mfma_f32_16x16x32_bf16 %[y6], v[136:139], v[76:79], %[y6]\n\t" \
    "v_cvt_pk_bf16_f32 v164, v156, v160\n\t" \
    "v_cvt_pk_bf16_f32 v165, v157, v161\n\t" \
    "v_mfma_f32_16x16x32_bf16 %[y7], v[136:139], v[80:83], %[y7]\n\t" \
    "v_cvt_pk_bf16_f32 v166, v158, v162\n\t" \
    "v_cvt_pk_bf16_f32 v167, v159, v163\n\t" \
    "v_mfma_f32_16x16x32_bf16 v[160:163], v[68:71], %[bd7], 0\n\t" \
    "s_waitcnt lgkmcnt(4)\n\t" \
    "v_mfma_f32_16x16x32_bf16 v[156:159], v[152:155], %[bd9], 0\n\t" \
    "ds_read_b128 v[152:155], %[uw] offset:4096\n\t" \
    "v_mfma_f32_16x16x32_bf16 %[y1], v[140:143], v[64:67], %[y1]\n\t" \
    "ds_read2_b32 v[128:129], %[ka0] offset0:112 offset1:113\n\t" \
    "ds_read2_b32 v[130:131], %[ka0] offset0:114 offset1:115\n\t" \
    "v_mfma_f32_16x16x32_bf16 %[y2], v[140:143], v[68:71], %[y2]\n\t" \
    "v_mfma_f32_16x16x32_bf16 %[y3], v[140:143], v[72:75], %[y3]\n\t" \
    "v_mfma_f32_16x16x32_bf16 %[y4], v[140:143], v[76:79], %[y4]\n\t" \
    "s_nop 0\n\t" \
    "v_cvt_pk_bf16_f32 v168, v156, v160\n\t" \
    "v_cvt_pk_bf16_f32 v169, v157, v161\n\t" \
    "v_mfma_f32_16x16x32_bf16 %[y5], v[140:143], v[80:83], %[y5]\n\t" \
    "v_cvt_pk_bf16_f32 v170, v158, v162\n\t" \
    "v_cvt_pk_bf16_f32 v171, v159, v163\n\t" \
    "v_mfma_f32_16x16x32_bf16 %[y6], v[140:143], v[84:87], %[y6]\n\t" \
    "v_mfma_f32_16x16x32_bf16 %[y7], v[140:143], v[88:91], %[y7]\n\t" \
    "v_mfma_f32_16x16x32_bf16 v[160:163], v[72:75], %[bd7], 0\n\t" \
    "s_waitcnt lgkmcnt(2)\n\t" \
    "v_mfma_f32_16x16x32_bf16 v[156:159], v[152:155], %[bd9], 0\n\t" \
    "ds_read_b128 v[152:155], %[uw] offset:6144\n\t" \
    "v_mfma_f32_16x16x32_bf16 %[y2], v[144:147], v[64:67], %[y2]\n\t" \
    "ds_read2_b32 v[132:133], %[ka1] offset0:112 offset1:113\n\t" \
    "ds_read2_b32 v[134:135], %[ka1] offset0:114 offset1:115\n\t" \
    "v_mfma_f32_16x16x32_bf16 %[y3], v[144:147], v[68:71], %[y3]\n\t" \
    "v_mfma_f32_16x16x32_bf16 %[y4], v[144:147], v[72:75], %[y4]\n\t" \
    "v_mfma_f32_16x16x32_bf16 %[y5], v[144:147], v[76:79], %[y5]\n\t" \
    "s_nop 0\n\t" \
    "v_cvt_pk_bf16_f32 v172, v156, v160\n\t" \
    "v_cvt_pk_bf16_f32 v173, v157, v161\n\t" \
    "v_mfma_f32_16x16x32_bf16 %[y6], v[144:147], v[80:83], %[y6]\n\t" \
    "v_cvt_pk_bf16_f32 v174, v158, v162\n\t" \
    "v_cvt_pk_bf16_f32 v175, v159, v163\n\t" \
    "v_mfma_f32_16x16x32_bf16 %[y7], v[144:147], v[84:87], %[y7]\n\t" \
    "v_mfma_f32_16x16x32_bf16 v[160:163], v[76:79], %[bd7], 0\n\t" \
    "s_waitcnt lgkmcnt(2)\n\t" \
    "v_mfma_f32_16x16x32_bf16 v[156:159], v[152:155], %[bd9], 0\n\t" \
    "v_mfma_f32_16x16x32_bf16 %[y0], v[148:151], v[96:99], %[y0]\n\t" \
    "ds_read2_b32 v[136:137], %[ka0] offset0:240 offset1:241\n\t" \
    "ds_read2_b32 v[138:139], %[ka0] offset0:242 offset1:243\n\t" \
    "v_mfma_f32_16x16x32_bf16 %[y1], v[148:151], v[100:103], %[y1]\n\t" \
    "ds_read_b128 v[64:67], %[ub1] offset:1024\n\t" \
    "v_mfma_f32_16x16x32_bf16 %[y2], v[148:151], v[104:107], %[y2]\n\t" \
    "global_load_dwordx2 %[x0], %[xo0], %[xb0]\n\t" \
    "v_mfma_f32_16x16x32_bf16 %[y3], v[148:151], v[108:111], %[y3]\n\t" \
    "v_cvt_pk_bf16_f32 v176, v156, v160\n\t" \
    "v_cvt_pk_bf16_f32 v177, v157, v161\n\t" \
    "v_mfma_f32_16x16x32_bf16 %[y4], v[148:151], v[112:115], %[y4]\n\t" \
    "v_cvt_pk_bf16_f32 v178, v158, v162\n\t" \
    "v_cvt_pk_bf16_f32 v179, v159, v163\n\t" \
    "v_mfma_f32_16x16x32_bf16 %[y5], v[148:151], v[116:119], %[y5]\n\t" \
    "ds_read_b128 v[68:71], %[ub1] offset:3072\n\t" \
    "v_mfma_f32_16x16x32_bf16 %[y6], v[148:151], v[120:123], %[y6]\n\t" \
    "v_mfma_f32_16x16x32_bf16 %[y7], v[148:151], v[124:127], %[y7]\n\t" \
    "ds_read_b128 v[72:75], %[ub1] offset:5120\n\t" \
    "v_mfma_f32_16x16x32_bf16 %[y3], v[128:131], v[96:99], %[y3]\n\t" \
    "ds_read2_b32 v[140:141], %[ka1] offset0:224 offset1:225\n\t" \
    "ds_read2_b32 v[142:143], %[ka1] offset0:226 offset1:227\n\t" \
    "v_mfma_f32_16x16x32_bf16 %[y4], v[128:131], v[100:103], %[y4]\n\t" \
    "ds_read_b128 v[76:79], %[ub1] offset:7168\n\t" \
    "v_mfma_f32_16x16x32_bf16 %[y5], v[128:131], v[104:107], %[y5]\n\t" \
    "v_mfma_f32_16x16x32_bf16 %[y6], v[128:131], v[108:111], %[y6]\n\t" \
    "ds_read_b128 v[80:83], %[ub1] offset:9216\n\t" \
    "v_mfma_f32_16x16x32_bf16 %[y7], v[128:131], v[112:115], %[y7]\n\t" \
    "s_waitcnt lgkmcnt(9)\n\t" \
    "v_mfma_f32_16x16x32_bf16 %[y1], v[132:135], v[96:99], %[y1]\n\t" \
    "ds_read2_b32 v[144:145], %[ka0] offset0:96 offset1:97\n\t" \
    "ds_read2_b32 v[146:147], %[ka0] offset0:98 offset1:99\n\t" \
    "v_mfma_f32_16x16x32_bf16 %[y2], v[132:135], v[100:103], %[y2]\n\t" \
    "global_load_dwordx2 %[x1], %[xo0], %[xb1]\n\t" \
    "v_mfma_f32_16x16x32_bf16 %[y3], v[132:135], v[104:107], %[y3]\n\t" \
    "ds_read_b128 v[84:87], %[ub1] offset:11264\n\t" \
    "v_mfma_f32_16x16x32_bf16 %[y4], v[132:135], v[108:111], %[y4]\n\t" \
    "v_mfma_f32_16x16x32_bf16 %[y5], v[132:135], v[112:115], %[y5]\n\t" \
    "ds_read_b128 v[88:91], %[ub1] offset:13312\n\t" \
    "v_mfma_f32_16x16x32_bf16 %[y6], v[132:135], v[116:119], %[y6]\n\t" \
    "v_mfma_f32_16x16x32_bf16 %[y7], v[132:135], v[120:123], %[y7]\n\t" \
    "s_waitcnt lgkmcnt(11)\n\t" \
    "ds_read_b128 v[92:95], %[ub1] offset:15360\n\t" \
    "v_mfma_f32_16x16x32_bf16 %[y2], v[136:139], v[96:99], %[y2]\n\t" \
    "ds_read2_b32 v[148:149], %[ka1] offset0:96 offset1:97\n\t" \
    "ds_read2_b32 v[150:151], %[ka1] offset0:98 offset1:99\n\t" \
    "v_mfma_f32_16x16x32_bf16 %[y3], v[136:139], v[100:103], %[y3]\n\t" \
    "v_mfma_f32_16x16x32_bf16 %[y4], v[136:139], v[104:107], %[y4]\n\t" \
    "v_mfma_f32_16x16x32_bf16 %[y5], v[136:139], v[108:111], %[y5]\n\t" \
    "v_mfma_f32_16x16x32_bf16 %[y6], v[136:139], v[112:115], %[y6]\n\t" \
    "v_mfma_f32_16x16x32_bf16 %[y7], v[136:139], v[116:119], %[y7]\n\t" \
    "s_waitcnt lgkmcnt(9)\n\t" \
    "v_mfma_f32_16x16x32_bf16 %[y0], v[140:143], v[64:67], %[y0]\n\t" \
    "global_load_dwordx2 %[x2], %[xo0], %[xb2]\n\t" \
    "v_mfma_f32_16x16x32_bf16 %[y1], v[140:143], v[68:71], %[y1]\n\t" \
    "ds_read2_b32 v[128:129], %[ka0] offset0:224 offset1:225\n\t" \
    "ds_read2_b32 v[130:131], %[ka0] offset0:226 offset1:227\n\t" \
    "v_mfma_f32_16x16x32_bf16 %[y2], v[140:143], v[72:75], %[y2]\n\t" \
    "s_waitcnt lgkmcnt(10)\n\t" \
    "ds_read2_b32 v[180:181], %[kg] offset0:48 offset1:49\n\t" \
    "ds_read2_b32 v[182:183], %[kg] offset0:50 offset1:51\n\t" \
    "v_mfma_f32_16x16x32_bf16 %[y3], v[140:143], v[76:79], %[y3]\n\t" \
    "s_waitcnt lgkmcnt(11)\n\t" \
    "ds_read_b128 v[96:99], %[ub0] offset:1024\n\t" \
    "v_mfma_f32_16x16x32_bf16 %[y4], v[140:143], v[80:83], %[y4]\n\t" \
    "s_waitcnt lgkmcnt(9)\n\t" \
    "v_mfma_f32_16x16x32_bf16 %[y5], v[140:143], v[84:87], %[y5]\n\t" \
    "s_waitcnt lgkmcnt(8)\n\t" \
    "ds_read_b128 v[100:103], %[ub0] offset:3072\n\t" \
    "v_mfma_f32_16x16x32_bf16 %[y6], v[140:143], v[88:91], %[y6]\n\t" \
    "s_waitcnt lgkmcnt(8)\n\t" \
    "v_mfma_f32_16x16x32_bf16 %[y7], v[140:143], v[92:95], %[y7]\n\t" \
    "ds_read_b128 v[104:107], %[ub0] offset:5120\n\t" \
    "v_mfma_f32_16x16x32_bf16 %[y3], v[144:147], v[64:67], %[y3]\n\t" \
    "ds_read2_b32 v[132:133], %[ka1] offset0:208 offset1:209\n\t" \
    "ds_read2_b32 v[134:135], %[ka1] offset0:210 offset1:211\n\t" \
    "v_mfma_f32_16x16x32_bf16 %[y4], v[144:147], v[68:71], %[y4]\n\t" \
    "ds_read_b128 v[108:111], %[ub0] offset:7168\n\t" \
    "v_mfma_f32_16x16x32_bf16 %[y5], v[144:147], v[72:75], %[y5]\n\t" \
    "v_mfma_f32_16x16x32_bf16 %[y6], v[144:147], v[76:79], %[y6]\n\t" \
    "ds_read_b128 v[112:115], %[ub0] offset:9216\n\t" \
    "v_mfma_f32_16x16x32_bf16 %[y7], v[144:147], v[80:83], %[y7]\n\t" \
    "s_waitcnt lgkmcnt(7)\n\t" \
    "global_load_dwordx2 %[x3], %[xo0], %[xb3]\n\t" \
    "v_mfma_f32_16x16x32_bf16 %[y4], v[164:167], v[180:183], %[y4]\n\t" \
    "ds_read_b128 v[116:119], %[ub0] offset:11264\n\t" \
    "v_mfma_f32_16x16x32_bf16 %[y5], v[168:171], v[180:183], %[y5]\n\t" \
    "v_mfma_f32_16x16x32_bf16 %[y6], v[172:175], v[180:183], %[y6]\n\t" \
    "ds_read_b128 v[120:123], %[ub0] offset:13312\n\t" \
    "v_mfma_f32_16x16x32_bf16 %[y7], v[176:179], v[180:183], %[y7]\n\t" \
    "v_mfma_f32_16x16x32_bf16 %[y1], v[148:151], v[64:67], %[y1]\n\t" \
    "ds_read2_b32 v[136:137], %[ka0] offset0:80 offset1:81\n\t" \
    "ds_read2_b32 v[138:139], %[ka0] offset0:82 offset1:83\n\t" \
    "v_mfma_f32_16x16x32_bf16 %[y2], v[148:151], v[68:71], %[y2]\n\t" \
    "ds_read_b128 v[124:127], %[ub0] offset:15360\n\t" \
    "v_mfma_f32_16x16x32_bf16 %[y3], v[148:151], v[72:75], %[y3]\n\t" \
    "v_mfma_f32_16x16x32_bf16 %[y4], v[148:151], v[76:79], %[y4]\n\t" \
    "v_mfma_f32_16x16x32_bf16 %[y5], v[148:151], v[80:83], %[y5]\n\t" \
    "v_mfma_f32_16x16x32_bf16 %[y6], v[148:151], v[84:87], %[y6]\n\t" \
    "v_mfma_f32_16x16x32_bf16 %[y7], v[148:151], v[88:91], %[y7]\n\t" \
    "v_mfma_f32_16x16x32_bf16 %[y2], v[128:131], v[64:67], %[y2]\n\t" \
    "ds_read2_b32 v[140:141], %[ka1] offset0:80 offset1:81\n\t" \
    "ds_read2_b32 v[142:143], %[ka1] offset0:82 offset1:83\n\t" \
    "v_mfma_f32_16x16x32_bf16 %[y3], v[128:131], v[68:71], %[y3]\n\t" \
    "v_mfma_f32_16x16x32_bf16 %[y4], v[128:131], v[72:75], %[y4]\n\t" \
    "v_mfma_f32_16x16x32_bf16 %[y5], v[128:131], v[76:79], %[y5]\n\t" \
    "v_mfma_f32_16x16x32_bf16 %[y6], v[128:131], v[80:83], %[y6]\n\t" \
    "v_mfma_f32_16x16x32_bf16 %[y7], v[128:131], v[84:87], %[y7]\n\t" \
    "s_waitcnt lgkmcnt(9)\n\t" \
    "v_mfma_f32_16x16x32_bf16 %[y0], v[132:135], v[96:99], %[y0]\n\t" \
    "ds_read2_b32 v[144:145], %[ka0] offset0:208 offset1:209\n\t" \
    "ds_read2_b32 v[146:147], %[ka0] offset0:210 offset1:211\n\t" \
    "v_mfma_f32_16x16x32_bf16 %[y1], v[132:135], v[100:103], %[y1]\n\t" \
    "ds_read2_b32 v[184:185], %[kg] offset0:32 offset1:33\n\t" \
    "ds_read2_b32 v[186:187], %[kg] offset0:34 offset1:35\n\t" \
    "v_mfma_f32_16x16x32_bf16 %[y2], v[132:135], v[104:107], %[y2]\n\t" \
    "s_waitcnt lgkmcnt(12)\n\t" \
    "ds_read_b128 v[64:67], %[ub3] offset:0\n\t" \
    "v_mfma_f32_16x16x32_bf16 %[y3], v[132:135], v[108:111], %[y3]\n\t" \
    "s_waitcnt lgkmcnt(12)\n\t" \
    "v_mfma_f32_16x16x32_bf16 %[y4], v[132:135], v[112:115], %[y4]\n\t" \
    "s_waitcnt lgkmcnt(11)\n\t" \
    "ds_read_b128 v[68:71], %[ub3] offset:2048\n\t" \
    "v_mfma_f32_16x16x32_bf16 %[y5], v[132:135], v[116:119], %[y5]\n\t" \
    "s_waitcnt lgkmcnt(11)\n\t" \
    "v_mfma_f32_16x16x32_bf16 %[y6], v[132:135], v[120:123], %[y6]\n\t" \
    "s_waitcnt lgkmcnt(8)\n\t" \
    "ds_read_b128 v[72:75], %[ub3] offset:4096\n\t" \
    "v_mfma_f32_16x16x32_bf16 %[y7], v[132:135], v[124:127], %[y7]\n\t" \
    "v_mfma_f32_16x16x32_bf16 %[y3], v[136:139], v[96:99], %[y3]\n\t" \
    "ds_read2_b32 v[148:149], %[ka1] offset0:192 offset1:193\n\t" \
    "ds_read2_b32 v[150:151], %[ka1] offset0:194 offset1:195\n\t" \
    "v_mfma_f32_16x16x32_bf16 %[y4], v[136:139], v[100:103], %[y4]\n\t" \
    "ds_read_b128 v[76:79], %[ub3] offset:6144\n\t" \
    "v_mfma_f32_16x16x32_bf16 %[y5], v[136:139], v[104:107], %[y5]\n\t" \
    "v_mfma_f32_16x16x32_bf16 %[y6], v[136:139], v[108:111], %[y6]\n\t" \
    "ds_read_b128 v[80:83], %[ub3] offset:8192\n\t" \
    "v_mfma_f32_16x16x32_bf16 %[y7], v[136:139], v[112:115], %[y7]\n\t" \
    "s_waitcnt lgkmcnt(7)\n\t" \
    "v_mfma_f32_16x16x32_bf16 %[y5], v[164:167], v[184:187], %[y5]\n\t" \
    "ds_read_b128 v[84:87], %[ub3] offset:10240\n\t" \
    "v_mfma_f32_16x16x32_bf16 %[y6], v[168:171], v[184:187], %[y6]\n\t" \
    "v_mfma_f32_16x16x32_bf16 %[y7], v[172:175], v[184:187], %[y7]\n\t" \
    "ds_read_b128 v[88:91], %[ub3] offset:12288\n\t" \
    "v_mfma_f32_16x16x32_bf16 %[y1], v[140:143], v[96:99], %[y1]\n\t" \
    "ds_read2_b32 v[128:129], %[ka0] offset0:64 offset1:65\n\t" \
    "ds_read2_b32 v[130:131], %[ka0] offset0:66 offset1:67\n\t" \
    "v_mfma_f32_16x16x32_bf16 %[y2], v[140:143], v[100:103], %[y2]\n\t" \
    "ds_read_b128 v[92:95], %[ub3] offset:14336\n\t" \
    "v_mfma_f32_16x16x32_bf16 %[y3], v[140:143], v[104:107], %[y3]\n\t" \
    "v_mfma_f32_16x16x32_bf16 %[y4], v[140:143], v[108:111], %[y4]\n\t" \
    "v_mfma_f32_16x16x32_bf16 %[y5], v[140:143], v[112:115], %[y5]\n\t" \
    "v_mfma_f32_16x16x32_bf16 %[y6], v[140:143], v[116:119], %[y6]\n\t" \
    "v_mfma_f32_16x16x32_bf16 %[y7], v[140:143], v[120:123], %[y7]\n\t" \
    "v_mfma_f32_16x16x32_bf16 %[y2], v[144:147], v[96:99], %[y2]\n\t" \
    "ds_read2_b32 v[132:133], %[ka1] offset0:64 offset1:65\n\t" \
    "ds_read2_b32 v[134:135], %[ka1] offset0:66 offset1:67\n\t" \
    "v_mfma_f32_16x16x32_bf16 %[y3], v[144:147], v[100:103], %[y3]\n\t" \
    "v_mfma_f32_16x16x32_bf16 %[y4], v[144:147], v[104:107], %[y4]\n\t" \
    "v_mfma_f32_16x16x32_bf16 %[y5], v[144:147], v[108:111], %[y5]\n\t" \
    "v_mfma_f32_16x16x32_bf16 %[y6], v[144:147], v[112:115], %[y6]\n\t" \
    "v_mfma_f32_16x16x32_bf16 %[y7], v[144:147], v[116:119], %[y7]\n\t" \
    "s_waitcnt lgkmcnt(9)\n\t" \
    "v_mfma_f32_16x16x32_bf16 %[y0], v[148:151], v[64:67], %[y0]\n\t" \
    "ds_read2_b32 v[136:137], %[ka0] offset0:192 offset1:193\n\t" \
    "ds_read2_b32 v[138:139], %[ka0] offset0:194 offset1:195\n\t" \
    "v_mfma_f32_16x16x32_bf16 %[y1], v[148:151], v[68:71], %[y1]\n\t" \
    "ds_read2_b32 v[180:181], %[kg] offset0:16 offset1:17\n\t" \
    "ds_read2_b32 v[182:183], %[kg] offset0:18 offset1:19\n\t" \
    "v_mfma_f32_16x16x32_bf16 %[y2], v[148:151], v[72:75], %[y2]\n\t" \
    "s_waitcnt lgkmcnt(12)\n\t" \
    "ds_read_b128 v[96:99], %[ub2] offset:0\n\t" \
    "v_mfma_f32_16x16x32_bf16 %[y3], v[148:151], v[76:79], %[y3]\n\t" \
    "s_waitcnt lgkmcnt(12)\n\t" \
    "v_mfma_f32_16x16x32_bf16 %[y4], v[148:151], v[80:83], %[y4]\n\t" \
    "s_waitcnt lgkmcnt(11)\n\t" \
    "ds_read_b128 v[100:103], %[ub2] offset:2048\n\t" \
    "v_mfma_f32_16x16x32_bf16 %[y5], v[148:151], v[84:87], %[y5]\n\t" \
    "s_waitcnt lgkmcnt(11)\n\t" \
    "v_mfma_f32_16x16x32_bf16 %[y6], v[148:151], v[88:91], %[y6]\n\t" \
    "s_waitcnt lgkmcnt(8)\n\t" \
    "ds_read_b128 v[104:107], %[ub2] offset:4096\n\t" \
    "v_mfma_f32_16x16x32_bf16 %[y7], v[148:151], v[92:95], %[y7]\n\t" \
    "v_mfma_f32_16x16x32_bf16 %[y3], v[128:131], v[64:67], %[y3]\n\t" \
    "ds_read2_b32 v[140:141], %[ka1] offset0:176 offset1:177\n\t" \
    "ds_read2_b32 v[142:143], %[ka1] offset0:178 offset1:179\n\t" \
    "v_mfma_f32_16x16x32_bf16 %[y4], v[128:131], v[68:71], %[y4]\n\t" \
    "ds_read_b128 v[108:111], %[ub2] offset:6144\n\t" \
    "v_mfma_f32_16x16x32_bf16 %[y5], v[128:131], v[72:75], %[y5]\n\t" \
    "v_mfma_f32_16x16x32_bf16 %[y6], v[128:131], v[76:79], %[y6]\n\t" \
    "ds_read_b128 v[112:115], %[ub2] offset:8192\n\t" \
    "v_mfma_f32_16x16x32_bf16 %[y7], v[128:131], v[80:83], %[y7]\n\t" \
    "s_waitcnt lgkmcnt(7)\n\t" \
    "v_mfma_f32_16x16x32_bf16 %[y6], v[164:167], v[180:183], %[y6]\n\t" \
    "ds_read_b128 v[116:119], %[ub2] offset:10240\n\t" \
    "v_mfma_f32_16x16x32_bf16 %[y7], v[168:171], v[180:183], %[y7]\n\t" \
    "v_mfma_f32_16x16x32_bf16 %[y1], v[132:135], v[64:67], %[y1]\n\t" \
    "ds_read2_b32 v[144:145], %[ka0] offset0:48 offset1:49\n\t" \
    "ds_read2_b32 v[146:147], %[ka0] offset0:50 offset1:51\n\t" \
    "v_mfma_f32_16x16x32_bf16 %[y2], v[132:135], v[68:71], %[y2]\n\t" \
    "ds_read_b128 v[120:123], %[ub2] offset:12288\n\t" \
    "v_mfma_f32_16x16x32_bf16 %[y3], v[132:135], v[72:75], %[y3]\n\t" \
    "v_mfma_f32_16x16x32_bf16 %[y4], v[132:135], v[76:79], %[y4]\n\t" \
    "ds_read_b128 v[124:127], %[ub2] offset:14336\n\t" \
    "v_mfma_f32_16x16x32_bf16 %[y5], v[132:135], v[80:83], %[y5]\n\t" \
    "v_mfma_f32_16x16x32_bf16 %[y6], v[132:135], v[84:87], %[y6]\n\t" \
    "v_mfma_f32_16x16x32_bf16 %[y7], v[132:135], v[88:91], %[y7]\n\t" \
    "v_mfma_f32_16x16x32_bf16 %[y2], v[136:139], v[64:67], %[y2]\n\t" \
    "ds_read2_b32 v[148:149], %[ka1] offset0:48 offset1:49\n\t" \
    "ds_read2_b32 v[150:151], %[ka1] offset0:50 offset1:51\n\t" \
    "v_mfma_f32_16x16x32_bf16 %[y3], v[136:139], v[68:71], %[y3]\n\t" \
    "v_mfma_f32_16x16x32_bf16 %[y4], v[136:139], v[72:75], %[y4]\n\t" \
    "v_mfma_f32_16x16x32_bf16 %[y5], v[136:139], v[76:79], %[y5]\n\t" \
    "v_mfma_f32_16x16x32_bf16 %[y6], v[136:139], v[80:83], %[y6]\n\t" \
    "v_mfma_f32_16x16x32_bf16 %[y7], v[136:139], v[84:87], %[y7]\n\t" \
    "s_waitcnt lgkmcnt(9)\n\t" \
    "v_mfma_f32_16x16x32_bf16 %[y0], v[140:143], v[96:99], %[y0]\n\t" \
    "ds_read2_b32 v[128:129], %[ka0] offset0:176 offset1:177\n\t" \
    "ds_read2_b32 v[130:131], %[ka0] offset0:178 offset1:179\n\t" \
    "v_mfma_f32_16x16x32_bf16 %[y1], v[140:143], v[100:103], %[y1]\n\t" \
    "ds_read2_b32 v[184:185], %[kg] offset0:0 offset1:1\n\t" \
    "ds_read2_b32 v[186:187], %[kg] offset0:2 offset1:3\n\t" \
    "v_mfma_f32_16x16x32_bf16 %[y2], v[140:143], v[104:107], %[y2]\n\t" \
    "s_waitcnt lgkmcnt(12)\n\t" \
    "ds_read_b128 v[64:67], %[ub1] offset:0\n\t" \
    "v_mfma_f32_16x16x32_bf16 %[y3], v[140:143], v[108:111], %[y3]\n\t" \
    "s_waitcnt lgkmcnt(12)\n\t" \
    "v_mfma_f32_16x16x32_bf16 %[y4], v[140:143], v[112:115], %[y4]\n\t" \
    "s_waitcnt lgkmcnt(11)\n\t" \
    "ds_read_b128 v[68:71], %[ub1] offset:2048\n\t" \
    "v_mfma_f32_16x16x32_bf16 %[y5], v[140:143], v[116:119], %[y5]\n\t" \
    "s_waitcnt lgkmcnt(9)\n\t" \
    "v_mfma_f32_16x16x32_bf16 %[y6], v[140:143], v[120:123], %[y6]\n\t" \
    "s_waitcnt lgkmcnt(8)\n\t" \
    "ds_read_b128 v[72:75], %[ub1] offset:4096\n\t" \
    "v_mfma_f32_16x16x32_bf16 %[y7], v[140:143], v[124:127], %[y7]\n\t" \
    "v_mfma_f32_16x16x32_bf16 %[y3], v[144:147], v[96:99], %[y3]\n\t" \
    "ds_read2_b32 v[132:133], %[ka1] offset0:160 offset1:161\n\t" \
    "ds_read2_b32 v[134:135], %[ka1] offset0:162 offset1:163\n\t" \
    "v_mfma_f32_16x16x32_bf16 %[y4], v[144:147], v[100:103], %[y4]\n\t" \
    "ds_read_b128 v[76:79], %[ub1] offset:6144\n\t" \
    "v_mfma_f32_16x16x32_bf16 %[y5], v[144:147], v[104:107], %[y5]\n\t" \
    "v_mfma_f32_16x16x32_bf16 %[y6], v[144:147], v[108:111], %[y6]\n\t" \
    "ds_read_b128 v[80:83], %[ub1] offset:8192\n\t" \
    "v_mfma_f32_16x16x32_bf16 %[y7], v[144:147], v[112:115], %[y7]\n\t" \
    "s_waitcnt lgkmcnt(7)\n\t" \
    "v_mfma_f32_16x16x32_bf16 %[y7], v[164:167], v[184:187], %[y7]\n\t" \
    "ds_read_b128 v[84:87], %[ub1] offset:10240\n\t" \
    "v_mfma_f32_16x16x32_bf16 %[y1], v[148:151], v[96:99], %[y1]\n\t" \
    "ds_read2_b32 v[136:137], %[ka0] offset0:32 offset1:33\n\t" \
    "ds_read2_b32 v[138:139], %[ka0] offset0:34 offset1:35\n\t" \
    "v_mfma_f32_16x16x32_bf16 %[y2], v[148:151], v[100:103], %[y2]\n\t" \
    "ds_read_b128 v[88:91], %[ub1] offset:12288\n\t" \
    "v_mfma_f32_16x16x32_bf16 %[y3], v[148:151], v[104:107], %[y3]\n\t" \
    "v_mfma_f32_16x16x32_bf16 %[y4], v[148:151], v[108:111], %[y4]\n\t" \
    "ds_read_b128 v[92:95], %[ub1] offset:14336\n\t" \
    "v_mfma_f32_16x16x32_bf16 %[y5], v[148:151], v[112:115], %[y5]\n\t" \
    "v_mfma_f32_16x16x32_bf16 %[y6], v[148:151], v[116:119], %[y6]\n\t" \
    "v_mfma_f32_16x16x32_bf16 %[y7], v[148:151], v[120:123], %[y7]\n\t" \
    "v_mfma_f32_16x16x32_bf16 %[y2], v[128:131], v[96:99], %[y2]\n\t" \
    "ds_read2_b32 v[140:141], %[ka1] offset0:32 offset1:33\n\t" \
    "ds_read2_b32 v[142:143], %[ka1] offset0:34 offset1:35\n\t" \
    "v_mfma_f32_16x16x32_bf16 %[y3], v[128:131], v[100:103], %[y3]\n\t" \
    "v_mfma_f32_16x16x32_bf16 %[y4], v[128:131], v[104:107], %[y4]\n\t" \
    "v_mfma_f32_16x16x32_bf16 %[y5], v[128:131], v[108:111], %[y5]\n\t" \
    "v_mfma_f32_16x16x32_bf16 %[y6], v[128:131], v[112:115], %[y6]\n\t" \
    "v_mfma_f32_16x16x32_bf16 %[y7], v[128:131], v[116:119], %[y7]\n\t" \
    "s_waitcnt lgkmcnt(9)\n\t" \
    "v_mfma_f32_16x16x32_bf16 %[y0], v[132:135], v[64:67], %[y0]\n\t" \
    "ds_read2_b32 v[144:145], %[ka0] offset0:160 offset1:161\n\t" \
    "ds_read2_b32 v[146:147], %[ka0] offset0:162 offset1:163\n\t" \
    "v_mfma_f32_16x16x32_bf16 %[y1], v[132:135], v[68:71], %[y1]\n\t" \
    "ds_read_b128 v[96:99], %[ub0] offset:0\n\t" \
    "v_mfma_f32_16x16x32_bf16 %[y2], v[132:135], v[72:75], %[y2]\n\t" \
    "s_waitcnt lgkmcnt(11)\n\t" \
    "v_mfma_f32_16x16x32_bf16 %[y3], v[132:135], v[76:79], %[y3]\n\t" \
    "s_waitcnt lgkmcnt(10)\n\t" \
    "ds_read_b128 v[100:103], %[ub0] offset:2048\n\t" \
    "v_mfma_f32_16x16x32_bf16 %[y4], v[132:135], v[80:83], %[y4]\n\t" \
    "s_waitcnt lgkmcnt(10)\n\t" \
    "v_mfma_f32_16x16x32_bf16 %[y5], v[132:135], v[84:87], %[y5]\n\t" \
    "s_waitcnt lgkmcnt(7)\n\t" \
    "ds_read_b128 v[104:107], %[ub0] offset:4096\n\t" \
    "v_mfma_f32_16x16x32_bf16 %[y6], v[132:135], v[88:91], %[y6]\n\t" \
    "s_waitcnt lgkmcnt(7)\n\t" \
    "v_mfma_f32_16x16x32_bf16 %[y7], v[132:135], v[92:95], %[y7]\n\t" \
    "ds_read_b128 v[108:111], %[ub0] offset:6144\n\t" \
    "v_mfma_f32_16x16x32_bf16 %[y3], v[136:139], v[64:67], %[y3]\n\t" \
    "ds_read2_b32 v[148:149], %[ka1] offset0:144 offset1:145\n\t" \
    "ds_read2_b32 v[150:151], %[ka1] offset0:146 offset1:147\n\t" \
    "v_mfma_f32_16x16x32_bf16 %[y4], v[136:139], v[68:71], %[y4]\n\t" \
    "ds_read_b128 v[112:115], %[ub0] offset:8192\n\t" \
    "v_mfma_f32_16x16x32_bf16 %[y5], v[136:139], v[72:75], %[y5]\n\t" \
    "v_mfma_f32_16x16x32_bf16 %[y6], v[136:139], v[76:79], %[y6]\n\t" \
    "ds_read_b128 v[116:119], %[ub0] offset:10240\n\t" \
    "v_mfma_f32_16x16x32_bf16 %[y7], v[136:139], v[80:83], %[y7]\n\t" \
    "s_waitcnt lgkmcnt(10)\n\t" \
    "v_mfma_f32_16x16x32_bf16 %[y1], v[140:143], v[64:67], %[y1]\n\t" \
    "ds_read2_b32 v[128:129], %[ka0] offset0:16 offset1:17\n\t" \
    "ds_read2_b32 v[130:131], %[ka0] offset0:18 offset1:19\n\t" \
    "v_mfma_f32_16x16x32_bf16 %[y2], v[140:143], v[68:71], %[y2]\n\t" \
    "ds_read_b128 v[120:123], %[ub0] offset:12288\n\t" \
    "v_mfma_f32_16x16x32_bf16 %[y3], v[140:143], v[72:75], %[y3]\n\t" \
    "v_mfma_f32_16x16x32_bf16 %[y4], v[140:143], v[76:79], %[y4]\n\t" \
    "ds_read_b128 v[124:127], %[ub0] offset:14336\n\t" \
    "v_mfma_f32_16x16x32_bf16 %[y5], v[140:143], v[80:83], %[y5]\n\t" \
    "v_mfma_f32_16x16x32_bf16 %[y6], v[140:143], v[84:87], %[y6]\n\t" \
    "v_mfma_f32_16x16x32_bf16 %[y7], v[140:143], v[88:91], %[y7]\n\t" \
    "s_waitcnt lgkmcnt(12)\n\t" \
    "v_mfma_f32_16x16x32_bf16 %[y2], v[144:147], v[64:67], %[y2]\n\t" \
    "ds_read2_b32 v[132:133], %[ka1] offset0:16 offset1:17\n\t" \
    "ds_read2_b32 v[134:135], %[ka1] offset0:18 offset1:19\n\t" \
    "v_mfma_f32_16x16x32_bf16 %[y3], v[144:147], v[68:71], %[y3]\n\t" \
    "v_mfma_f32_16x16x32_bf16 %[y4], v[144:147], v[72:75], %[y4]\n\t" \
    "v_mfma_f32_16x16x32_bf16 %[y5], v[144:147], v[76:79], %[y5]\n\t" \
    "v_mfma_f32_16x16x32_bf16 %[y6], v[144:147], v[80:83], %[y6]\n\t" \
    "v_mfma_f32_16x16x32_bf16 %[y7], v[144:147], v[84:87], %[y7]\n\t" \
    "s_waitcnt lgkmcnt(8)\n\t" \
    "v_mfma_f32_16x16x32_bf16 %[y0], v[148:151], v[96:99], %[y0]\n\t" \
    "ds_read2_b32 v[136:137], %[ka0] offset0:144 offset1:145\n\t" \
    "ds_read2_b32 v[138:139], %[ka0] offset0:146 offset1:147\n\t" \
    "v_mfma_f32_16x16x32_bf16 %[y1], v[148:151], v[100:103], %[y1]\n\t" \
    "v_mfma_f32_16x16x32_bf16 %[y2], v[148:151], v[104:107], %[y2]\n\t" \
    "v_mfma_f32_16x16x32_bf16 %[y3], v[148:151], v[108:111], %[y3]\n\t" \
    "s_waitcnt lgkmcnt(9)\n\t" \
    "v_mfma_f32_16x16x32_bf16 %[y4], v[148:151], v[112:115], %[y4]\n\t" \
    "s_waitcnt lgkmcnt(8)\n\t" \
    "v_mfma_f32_16x16x32_bf16 %[y5], v[148:151], v[116:119], %[y5]\n\t" \
    "s_waitcnt lgkmcnt(5)\n\t" \
    "v_mfma_f32_16x16x32_bf16 %[y6], v[148:151], v[120:123], %[y6]\n\t" \
    "s_waitcnt lgkmcnt(4)\n\t" \
    "v_mfma_f32_16x16x32_bf16 %[y7], v[148:151], v[124:127], %[y7]\n\t" \
    "v_mfma_f32_16x16x32_bf16 %[y3], v[128:131], v[96:99], %[y3]\n\t" \
    "v_mfma_f32_16x16x32_bf16 %[y4], v[128:131], v[100:103], %[y4]\n\t" \
    "v_mfma_f32_16x16x32_bf16 %[y5], v[128:131], v[104:107], %[y5]\n\t" \
    "v_mfma_f32_16x16x32_bf16 %[y6], v[128:131], v[108:111], %[y6]\n\t" \
    "v_mfma_f32_16x16x32_bf16 %[y7], v[128:131], v[112:115], %[y7]\n\t" \
    "s_waitcnt lgkmcnt(2)\n\t" \
    "v_mfma_f32_16x16x32_bf16 %[y1], v[132:135], v[96:99], %[y1]\n\t" \
    "v_mfma_f32_16x16x32_bf16 %[y2], v[132:135], v[100:103], %[y2]\n\t" \
    "v_mfma_f32_16x16x32_bf16 %[y3], v[132:135], v[104:107], %[y3]\n\t" \
    "v_mfma_f32_16x16x32_bf16 %[y4], v[132:135], v[108:111], %[y4]\n\t" \
    "v_mfma_f32_16x16x32_bf16 %[y5], v[132:135], v[112:115], %[y5]\n\t" \
    "v_mfma_f32_16x16x32_bf16 %[y6], v[132:135], v[116:119], %[y6]\n\t" \
    "v_mfma_f32_16x16x32_bf16 %[y7], v[132:135], v[120:123], %[y7]\n\t" \
    "s_waitcnt lgkmcnt(0)\n\t" \
    "v_mfma_f32_16x16x32_bf16 %[y2], v[136:139], v[96:99], %[y2]\n\t" \
    "v_mfma_f32_16x16x32_bf16 %[y3], v[136:139], v[100:103], %[y3]\n\t" \
    "v_mfma_f32_16x16x32_bf16 %[y4], v[136:139], v[104:107], %[y4]\n\t" \
    "v_mfma_f32_16x16x32_bf16 %[y5], v[136:139], v[108:111], %[y5]\n\t" \
    "v_mfma_f32_16x16x32_bf16 %[y6], v[136:139], v[112:115], %[y6]\n\t" \
    "v_mfma_f32_16x16x32_bf16 %[y7], v[136:139], v[116:119], %[y7]\n\t" \
    "s_waitcnt vmcnt(0)\n\t" \
    "s_nop 7\n\t"
#define CONV_ASM_CLOBBERS "v64", "v65", "v66", "v67", "v68", "v69", "v70", "v71", "v72", "v73", "v74", "v75", "v76", "v77", "v78", "v79", "v80", "v81", "v82", "v83", "v84", "v85", "v86", "v87", "v88", "v89", "v90", "v91", "v92", "v93", "v94", "v95", "v96", "v97", "v98", "v99", "v100", "v101", "v102", "v103", "v104", "v105", "v106", "v107", "v108", "v109", "v110", "v111", "v112", "v113", "v114", "v115", "v116", "v117", "v118", "v119", "v120", "v121", "v122", "v123", "v124", "v125", "v126", "v127", "v128", "v129", "v130", "v131", "v132", "v133", "v134", "v135", "v136", "v137", "v138", "v139", "v140", "v141", "v142", "v143", "v144", "v145", "v146", "v147", "v148", "v149", "v150", "v151", "v152", "v153", "v154", "v155", "v156", "v157", "v158", "v159", "v160", "v161", "v162", "v163", "v164", "v165", "v166", "v167", "v168", "v169", "v170", "v171", "v172", "v173", "v174", "v175", "v176", "v177", "v178", "v179", "v180", "v181", "v182", "v183", "v184", "v185", "v186", "v187", "memory"
#define TAP_POS {12,12,12,12,12,12,12,12,12,12,12,12,12,12,12,12,12,12,12,12,12,12,12,12,12,12,12,2310,2310,2310,2310,2310,2310,2310,2302,2302,2302,2294,2294,2294,2294,2294,2286,2286,2286,2278,2278,2278,2278,2278,2270,2270,2270,2262,2262,2262,2262,2262,2254,2254,2254,2238,2238,2238,2238,2238,2230,2230,2230,2222,2222,2222,2222,2222,2214,2214,2214,2206,2206,2206,2206,2206,2198,2198,2198,2190,2190,2190,2190,2190,2182,2182,2182,2166,2166,2166,2166,2166,2158,2158,2158,2150,2150,2150,2150,2150,2142,2142,2142,2134,2134,2134,2134,2134,2126,2126,2126,2118,2118,2118,2118,2118,2110,2110,2110,2094,2094,2094,2094,2094,2086,2086,2086,2078,2078,2078,2078,2078,2070,2070,2070,2062,2062,2062,2062,2062,2054,2054,2054,2046,2046,2046,2046,2046,2038,2038,2038,2022,2022,2022,2022,2022,2014,2014,2014,2006,2006,2006,2006,2006,1998,1998,1998,1990,1990,1990,1990,1990,1982,1982,1982,1974,1974,1974,1974,1974,1966,1966,1966,1950,1950,1950,1950,1950,1942,1942,1942,1934,1934,1934,1934,1934,1926,1926,1926,1918,1918,1918,1918,1918,1910,1910,1910,1902,1902,1902,1902,1902,1894,1894,1894,1878,1878,1878,1878,1878,1870,1870,1870,1862,1862,1862,1862,1862,1854,1854,1854,1846,1846,1846,1846,1846,1838,1838,1838,1830,1830,1830,1830,1830,1822,1822,1822,1806,1806,1806,1806,1806,1798,1798,1798,1790,1790,1790,1790,1790,1782,1782,1782,1774,1774,1774,1774,1774,1766,1766,1766,1758,1758,1758,1758,1758,1750,1750,1750,1734,1734,1734,1734,1734,1726,1726,1726,1718,1718,1718,1718,1718,1710,1710,1710,1702,1702,1702,1702,1702,1694,1694,1694,1686,1686,1686,1686,1686,1678,1678,1678,1662,1662,1662,1662,1662,1654,1654,1654,1646,1646,1646,1646,1646,1638,1638,1638,1630,1630,1630,1630,1630,1622,1622,1622,1614,1614,1614,1614,1614,1606,1606,1606,1590,1590,1590,1590,1590,1582,1582,1582,1574,1574,1574,1574,1574,1566,1566,1566,1558,1558,1558,1558,1558,1550,1550,1550,1542,1542,1542,1542,1542,1534,1534,1534,1518,1518,1518,1518,1518,1510,1510,1510,1502,1502,1502,1502,1502,1494,1494,1494,1486,1486,1486,1486,1486,1478,1478,1478,1470,1470,1470,1470,1470,1462,1462,1462,1446,1446,1446,1446,1446,1438,1438,1438,1430,1430,1430,1430,1430,1422,1422,1422,1414,1414,1414,1414,1414,1406,1406,1406,1398,1398,1398,1398,1398,1390,1390,1390,1374,1374,1374,1374,1374,1366,1366,1366,1358,1358,1358,1358,1358,1350,1350,1350,1342,1342,1342,1342,1342,1334,1334,1334,1326,1326,1326,1326,1326,1318,1318,1318,1302,1302,1302,1302,1302,1294,1294,1294,1286,1286,1286,1286,1286,1278,1278,1278,1270,1270,1270,1270,1270,1262,1262,1262,1254,1254,1254,1254,1254,1246,1246,1246,1230,1230,1230,1230,1230,1222,1222,1222,1214,1214,1214,1214,1214,1206,1206,1206,1198,1198,1198,1198,1198,1190,1190,1190,1182,1182,1182,1182,1182,1174,1174,1174,1158,1158,1158,1158,1154,1150,1150,1150,1146,1142,1142,1142,1138,1134,1134,1134,1130,1126,1126,1126,1122,1118,1118,1118,1114,1110,1110,1110,1106,1102,1102,1102,1098,1086,1086,1086,1082,1078,1078,1078,1074,1070,1070,1070,1066,1062,1062,1062,1058,1054,1054,1054,1050,1046,1046,1046,1042,1038,1038,1038,1034,1030,1030,1030,1026,1014,1014,1014,1010,1006,1006,1006,1002,998,998,998,994,990,990,990,986,982,982,982,978,974,974,974,970,966,966,966,962,958,958,958,954,942,942,942,938,934,934,934,930,926,926,926,922,918,918,918,914,910,910,910,906,902,902,902,898,894,894,894,890,886,886,886,882,870,870,870,866,862,862,862,858,854,854,854,850,846,846,846,842,838,838,838,834,830,830,830,826,822,822,822,818,814,814,814,810,798,798,798,794,790,790,790,786,782,782,782,778,774,774,774,770,766,766,766,762,758,758,758,754,750,750,750,746,742,742,742,738,726,726,726,722,718,718,718,714,710,710,710,706,702,702,702,698,694,694,694,690,686,686,686,682,678,678,678,674,670,670,670,666,654,654,654,650,646,646,646,642,638,638,638,634,630,630,630,626,622,622,622,618,614,614,614,610,606,606,606,602,598,598,598,594,582,582,580,578,576,574,572,570,568,566,564,562,560,558,556,554,552,550,548,546,544,542,540,538,536,534,532,530,528,526,524,522,520,510,508,506,504,502,500,498,496,494,492,490,488,486,484,482,480,478,476,474,472,470,468,466,464,462,460,458,456,454,452,450,448,438,436,434,432,430,428,426,424,422,420,418,416,414,412,410,408,406,404,402,400,398,396,394,392,390,388,386,384,382,380,378,376,366,364,362,360,358,356,354,352,350,348,346,344,342,340,338,336,334,332,330,328,326,324,322,320,318,316,314,312,310,308,306,304,294,292,290,288,286,284,282,280,278,276,274,272,270,268,266,264,262,260,258,256,254,252,250,248,246,244,242,240,238,236,234,232,222,220,218,216,214,212,210,208,206,204,202,200,198,196,194,192,190,188,186,184,182,180,178,176,174,172,170,168,166,164,162,160,150,148,146,144,142,140,138,136,134,132,130,128,126,124,122,120,118,116,114,112,110,108,106,104,102,100,98,96,94,92,90,88,78,76,74,72,70,68,66,64,62,60,58,56,54,52,50,48,46,44,42,40,38,36,34,32,30,28,26,24,22,20,18,16,14,12,12,12,12,12,12,12,12,12,12,12,12,12,12,12,12,4614,4598,4582,4566,4542,4526,4510,4494,4470,4454,4438,4422,4398,4382,4366,4350,4326,4310,4294,4278,4254,4238,4222,4206,4182,4166,4150,4134,4110,4094,4078,4062,4038,4022,4006,3990,3966,3950,3934,3918,3894,3878,3862,3846,3822,3806,3790,3774,3750,3734,3718,3702,3678,3662,3646,3630,3606,3590,3574,3558,3534,3518,3502,3486,3462,3446,3430,3414,3390,3374,3358,3342,3318,3302,3286,3270,3246,3230,3214,3198,3174,3158,3142,3126,3102,3086,3070,3054,3030,3014,2998,2982,2958,2942,2926,2910,2886,2870,2854,2838,2814,2798,2782,2766,2742,2726,2710,2694,2670,2654,2638,2622,2598,2582,2566,2550,2526,2510,2494,2478,2454,2438,2422,2406,2382,2366,2350,2334,12,12,12,12,12,12,12,12,12,12,12,12,12,12,12,12,12,12,12,12,12,12,12,12,12,12,12,12,12,12,12,12,12,12,12,12,12,12,12,12,12,12,12,12,12,12,12,12,12,12,12,12,12,12,12,12,12,12,12,12,12,12,12,12,12,12,12,12,12,12,12,12,12,12,2310,2310,2310,2310,2310,2310,2310,2302,2302,2302,2294,2294,2294,2294,2294,2286,2286,2286,2278,2278,2278,2278,2278,2270,2270,2270,2262,2262,2262,2262,2262,2254,2254,2254,2238,2238,2238,2238,2238,2230,2230,2230,2222,2222,2222,2222,2222,2214,2214,2214,2206,2206,2206,2206,2206,2198,2198,2198,2190,2190,2190,2190,2190,2182,2182,2182,2166,2166,2166,2166,2166,2158,2158,2158,2150,2150,2150,2150,2150,2142,2142,2142,2134,2134,2134,2134,2134,2126,2126,2126,2118,2118,2118,2118,2118,2110,2110,2110,2094,2094,2094,2094,2094,2086,2086,2086,2078,2078,2078,2078,2078,2070,2070,2070,2062,2062,2062,2062,2062,2054,2054,2054,2046,2046,2046,2046,2046,2038,2038,2038,2022,2022,2022,2022,2022,2014,2014,2014,2006,2006,2006,2006,2006,1998,1998,1998,1990,1990,1990,1990,1990,1982,1982,1982,1974,1974,1974,1974,1974,1966,1966,1966,1950,1950,1950,1950,1950,1942,1942,1942,1934,1934,1934,1934,1934,1926,1926,1926,1918,1918,1918,1918,1918,1910,1910,1910,1902,1902,1902,1902,1902,1894,1894,1894,1878,1878,1878,1878,1878,1870,1870,1870,1862,1862,1862,1862,1862,1854,1854,1854,1846,1846,1846,1846,1846,1838,1838,1838,1830,1830,1830,1830,1830,1822,1822,1822,1806,1806,1806,1806,1806,1798,1798,1798,1790,1790,1790,1790,1790,1782,1782,1782,1774,1774,1774,1774,1774,1766,1766,1766,1758,1758,1758,1758,1758,1750,1750,1750,1734,1734,1734,1734,1734,1726,1726,1726,1718,1718,1718,1718,1718,1710,1710,1710,1702,1702,1702,1702,1702,1694,1694,1694,1686,1686,1686,1686,1686,1678,1678,1678,1662,1662,1662,1662,1662,1654,1654,1654,1646,1646,1646,1646,1646,1638,1638,1638,1630,1630,1630,1630,1630,1622,1622,1622,1614,1614,1614,1614,1614,1606,1606,1606,1590,1590,1590,1590,1590,1582,1582,1582,1574,1574,1574,1574,1574,1566,1566,1566,1558,1558,1558,1558,1558,1550,1550,1550,1542,1542,1542,1542,1542,1534,1534,1534,1518,1518,1518,1518,1518,1510,1510,1510,1502,1502,1502,1502,1502,1494,1494,1494,1486,1486,1486,1486,1486,1478,1478,1478,1470,1470,1470,1470,1470,1462,1462,1462,1446,1446,1446,1446,1446,1438,1438,1438,1430,1430,1430,1430,1430,1422,1422,1422,1414,1414,1414,1414,1414,1406,1406,1406,1398,1398,1398,1398,1398,1390,1390,1390,1374,1374,1374,1374,1374,1366,1366,1366,1358,1358,1358,1358,1358,1350,1350,1350,1342,1342,1342,1342,1342,1334,1334,1334,1326,1326,1326,1326,1326,1318,1318,1318,1302,1302,1302,1302,1302,1294,1294,1294,1286,1286,1286,1286,1286,1278,1278,1278,1270,1270,1270,1270,1270,1262,1262,1262,1254,1254,1254,1254,1254,1246,1246,1246,1230,1230,1230,1230,1230,1222,1222,1222,1214,1214,1214,1214,1214,1206,1206,1206,1198,1198,1198,1198,1198,1190,1190,1190,1182,1182,1182,1182,1182,1174,1174,1174,1158,1158,1158,1158,1154,1150,1150,1150,1146,1142,1142,1142,1138,1134,1134,1134,1130,1126,1126,1126,1122,1118,1118,1118,1114,1110,1110,1110,1106,1102,1102,1102,1098,1086,1086,1086,1082,1078,1078,1078,1074,1070,1070,1070,1066,1062,1062,1062,1058,1054,1054,1054,1050,1046,1046,1046,1042,1038,1038,1038,1034,1030,1030,1030,1026,1014,1014,1014,1010,1006,1006,1006,1002,998,998,998,994,990,990,990,986,982,982,982,978,974,974,974,970,966,966,966,962,958,958,958,954,942,942,942,938,934,934,934,930,926,926,926,922,918,918,918,914,910,910,910,906,902,902,902,898,894,894,894,890,886,886,886,882,870,870,870,866,862,862,862,858,854,854,854,850,846,846,846,842,838,838,838,834,830,830,830,826,822,822,822,818,814,814,814,810,798,798,798,794,790,790,790,786,782,782,782,778,774,774,774,770,766,766,766,762,758,758,758,754,750,750,750,746,742,742,742,738,726,726,726,722,718,718,718,714,710,710,710,706,702,702,702,698,694,694,694,690,686,686,686,682,678,678,678,674,670,670,670,666,654,654,654,650,646,646,646,642,638,638,638,634,630,630,630,626,622,622,622,618,614,614,614,610,606,606,606,602,598,598,598,594,582,582,580,578,576,574,572,570,568,566,564,562,560,558,556,554,552,550,548,546,544,542,540,538,536,534,532,530,528,526,524,522,520,510,508,506,504,502,500,498,496,494,492,490,488,486,484,482,480,478,476,474,472,470,468,466,464,462,460,458,456,454,452,450,448,438,436,434,432,430,428,426,424,422,420,418,416,414,412,410,408,406,404,402,400,398,396,394,392,390,388,386,384,382,380,378,376,366,364,362,360,358,356,354,352,350,348,346,344,342,340,338,336,334,332,330,328,326,324,322,320,318,316,314,312,310,308,306,304,294,292,290,288,286,284,282,280,278,276,274,272,270,268,266,264,262,260,258,256,254,252,250,248,246,244,242,240,238,236,234,232,222,220,218,216,214,212,210,208,206,204,202,200,198,196,194,192,190,188,186,184,182,180,178,176,174,172,170,168,166,164,162,160,150,148,146,144,142,140,138,136,134,132,130,128,126,124,122,120,118,116,114,112,110,108,106,104,102,100,98,96,94,92,90,88,78,76,74,72,70,68,66,64,62,60,58,56,54,52,50,48,46,44,42,40,38,36,34,32,30,28,26,24,22,20,18,16,14,12,12,12,12,12,12,12,12,12,12,12,12,12,12,12,12,12,12,12,12,12,12,12,12,12,12,12,12,12,12,12,12,12,12,12,12,12,12,12,12,12,12,12,12,12,12,12,12,12,12,12,12,12,12,12,12,12,12,12,12,12,12,12,12,12,12,12,12,12,12,12,12,12,12,12,12,12,12,12,12,12,12,12,12,12,12,12,12,12,12,12,12,12,12,12,12,12,12,12,12,12,12,12,12,12,12,12,12,12,12,12,12,12,12,12,12,12,12,12,12,12,12,12,12,12,12,12,12,12,12,12,12,12,12,12,12,12,12,12,12,12,12,12,12,12,12,12,12,12,12,12,12,12,12,12,12,12,12,12,12,12,12,12,12,12,12,12,12,12,12,12,12,12,12,12,12,12,12,12,12,12,12,12,12,12,12,12,12,12,12,12,12,12,12,12,12,12,12,12,12,12,12,12,12,12,12,12,12,12,12,12,12,12,12,12,12,12,12,12,12,12,12,12,12,12,12,12,12,12,12,12,12,12,12,12,12,12,12,12,12,12,12,12,12,12,12,12,12,12,12,12,12,12,12,12,12,12}

DI u32 xslot(u32 octet) { const u32 g = octet / 9; return WS_X + g * XS + (octet - 9 * g) * 32768; }
DI float bf2f(u32 v) { return __uint_as_float(v << 16); }
DI float bfhi(u32 v) { return __uint_as_float(v & 0xffff0000u); }
DI u32 pk_bf16(float a, float b) { f32x2 t = {a, b}; return __builtin_bit_cast(u32, __builtin_convertvector(t, bf16x2)); }
DI float rb(float f) { return bf2f(pk_bf16(f, f) & 0xffffu); }
DI void lds_barrier() { asm volatile("s_waitcnt lgkmcnt(0)\n\ts_barrier" ::: "memory"); }
DI float fadd(float a, float b) { float r; asm("v_add_f32 %0, %1, %2" : "=v"(r) : "v"(a), "v"(b)); return r; }
DI float fsub(float a, float b) { float r; asm("v_sub_f32 %0, %1, %2" : "=v"(r) : "v"(a), "v"(b)); return r; }
DI float fmul(float a, float b) { float r; asm("v_mul_f32 %0, %1, %2" : "=v"(r) : "v"(a), "v"(b)); return r; }
DI u32 rfl(u32 x) { return __builtin_amdgcn_readfirstlane(x); }
DI u32 pk_f16(float a, float b) { f32x2 t = {a, b}; return __builtin_bit_cast(u32, __builtin_convertvector(t, f16x2)); }
DI float fexp(float x) { return __builtin_amdgcn_exp2f(x * 1.44269504088896f); }
DI float dot2(u32 a, u32 b, float c) { return __builtin_amdgcn_fdot2_f32_bf16(__builtin_bit_cast(bf16x2, a), __builtin_bit_cast(bf16x2, b), c, false); }
DI float xor8(float x) { return __uint_as_float(__builtin_amdgcn_update_dpp(0u, __float_as_uint(x), 0x128, 0xf, 0xf, true)); }
DI float sum16(float x) { const auto r = __builtin_amdgcn_permlane16_swap(__float_as_uint(x), __float_as_uint(x), false, false); return __uint_as_float(r[0]) + __uint_as_float(r[1]); }
DI float sum32(float x) { const auto r = __builtin_amdgcn_permlane32_swap(__float_as_uint(x), __float_as_uint(x), false, false); return __uint_as_float(r[0]) + __uint_as_float(r[1]); }
DI __amdgpu_buffer_rsrc_t rsrc(const void* p, u32 bytes) { return __builtin_amdgcn_make_buffer_rsrc((void*)p, 0, bytes, 0x00020000); }
DI void st16(__amdgpu_buffer_rsrc_t rs, u32 off, u32x4 v) { __builtin_amdgcn_raw_buffer_store_b128(v, rs, off, 0, 16); }
DI u32x2 tr_read(const char* p) { return __builtin_bit_cast(u32x2, __builtin_amdgcn_ds_read_tr16_b64_v4i16((__attribute__((address_space(3))) s16x4*)p)); }
DI f32x4 mfma_bf16(u32x4 a, u32x4 b, f32x4 c) { return __builtin_amdgcn_mfma_f32_16x16x32_bf16(__builtin_bit_cast(bf16x8, a), __builtin_bit_cast(bf16x8, b), c, 0, 0, 0); }
DI void dma16(__amdgpu_buffer_rsrc_t rs, u32 voff, u32 lds_addr) {
  u32 keep;
  asm volatile("s_mov_b32 %0, m0\n\ts_mov_b32 m0, %3\n\ts_nop 0\n\tbuffer_load_dwordx4 %1, %2, 0 offen lds\n\ts_mov_b32 m0, %0"
               : "=&s"(keep) : "v"(voff), "s"(rs), "s"(lds_addr) : "memory");
}
DI void touch_lines(const char* p, u32 bytes, u32 line, char* lds_dump) {
  u32 keep;
  asm volatile("s_mov_b32 %0, m0\n\ts_mov_b32 m0, %2\n\ts_nop 0\n\tglobal_load_lds_dword %1, off\n\ts_mov_b32 m0, %0"
               : "=&s"(keep) : "v"(p + min(line * 128, bytes - 4)), "s"(rfl((u32)(size_t)lds_dump)));
}
#define CODE_ADDR(dst, sym) do { int rel_; asm volatile("s_getpc_b64 %0\n\ts_mov_b32 %1, " sym "@rel32@lo+4" : "=s"(dst), "=s"(rel_)); dst += rel_; } while (0)

__device__ const u32 w1_order[324] = W1_ORDER;
__device__ const u32 w2_order[324] = W2_ORDER;
__device__ __attribute__((aligned(16))) const u16 tap_pos[2560] = TAP_POS;

DI void ln1_block(const u16* __restrict__ x, u32 blk, char* ws, char* lds, u32 tid) {
  const u32 lane = tid & 63, w = tid >> 6, t = lane & 7, c8 = lane >> 3;
  const u32 c0 = 18 * w + c8, oob = c8 < 2 ? 0u : OOB;
  __amdgpu_buffer_rsrc_t rx = rsrc(x, L * D * 2);
  u32x4 xv[3];
  #pragma unroll
  for (int i = 0; i < 3; ++i) xv[i] = __builtin_amdgcn_raw_buffer_load_b128(rx, (8 * blk + t) * (D * 2) + (c0 + 8 * i) * 16 + (i == 2 ? oob : 0u), 0, 0);
  float sm = 0.f, sq = 0.f;
  #pragma unroll
  for (int i = 0; i < 3; ++i)
    #pragma unroll
    for (int j = 0; j < 4; ++j) { sm = dot2(xv[i][j], 0x3f803f80u, sm); sq = dot2(xv[i][j], xv[i][j], sq); }
  sm += xor8(sm); sq += xor8(sq);
  sm = sum32(sum16(sm)); sq = sum32(sum16(sq));
  f32x2* red = (f32x2*)(lds + 10240);
  if (lane < 8) red[w * 8 + lane] = f32x2{sm, sq};
  lds_barrier();
  const f32x2 r0 = red[t], r1 = red[8 + t], r2 = red[16 + t], r3 = red[24 + t];
  const float mean = ((r0[0] + r1[0]) + (r2[0] + r3[0])) * (1.f / D);
  const float var = fmaxf(((r0[1] + r1[1]) + (r2[1] + r3[1])) * (1.f / D) - mean * mean, 0.f);
  float rinv = __builtin_amdgcn_rcpf(rb(__builtin_amdgcn_sqrtf(rb(rb(var) + 1e-5f))));
  asm volatile("s_nop 1" : "+v"(rinv));
  const float mb = rb(mean);
  #pragma unroll
  for (int i = 0; i < 3; ++i) {
    u32x4 u;
    #pragma unroll
    for (int j = 0; j < 4; ++j) {
      const u32 dd = pk_bf16(fsub(bf2f(xv[i][j]), mb), fsub(bfhi(xv[i][j]), mb));
      u[j] = pk_bf16(fmul(bf2f(dd), rinv), fmul(bfhi(dd), rinv));
    }
    *(u32x4*)(lds + (i == 2 && c8 >= 2 ? 72 + c8 : c0 + 8 * i) * 128 + t * 16) = u;
  }
  __amdgpu_buffer_rsrc_t rs = rsrc(ws, WS_END);
  #pragma unroll
  for (int i = 0; i < 3; ++i) st16(rs, xslot(c0 + 8 * i) + XO_OFF + (8 * blk + t) * 16 + (i == 2 ? oob : 0u), xv[i]);
  lds_barrier();
  const u32 g = lane >> 4, a = (lane >> 2) & 3, p = lane & 3;
  #pragma unroll
  for (int k = 0; k < 3; ++k) {
    const u32 st = min(w + 4 * k, 8u);
    const u32 src = (8 * st + 2 * g + (p >> 1)) * 128 + a * 16 + (p & 1) * 8;
    const u32x2 lo = tr_read(lds + src);
    const u32x2 hi = tr_read(lds + src + 64);
    st16(rs, xslot(8 * st + (lane >> 3)) + blk * 128 + (lane & 7) * 16 + (w + 4 * k > 8 ? OOB : 0u), u32x4{lo[0], lo[1], hi[0], hi[1]});
  }
}

DI f32x2 cmul(f32x2 a, f32x2 b) {
  f32x2 t, r;
  asm("v_pk_mul_f32 %0, %1, %2 op_sel:[0,0] op_sel_hi:[1,0]" : "=v"(t) : "v"(a), "v"(b));
  asm("v_pk_fma_f32 %0, %1, %2, %3 op_sel:[1,1,0] op_sel_hi:[0,1,1] neg_lo:[1,0,0]" : "=v"(r) : "v"(a), "v"(b), "v"(t));
  return r;
}
#define CM_MUL " op_sel:[0,0] op_sel_hi:[1,0]\n\t"
#define CM_FMA " op_sel:[1,1,0] op_sel_hi:[0,1,1] neg_lo:[1,0,0]\n\t"
DI void cmul2(f32x2& a, f32x2& b, f32x2 s) {
  f32x2 t, u;
  asm("v_pk_mul_f32 %2, %0, %4" CM_MUL "v_pk_mul_f32 %3, %1, %4" CM_MUL "v_pk_fma_f32 %0, %0, %4, %2" CM_FMA "v_pk_fma_f32 %1, %1, %4, %3" CM_FMA
      : "+v"(a), "+v"(b), "=&v"(t), "=&v"(u) : "v"(s));
}
DI u32x4 cmul16x4(u32x4 a, u32x4 b) {
  u32 r0, r1, r2, r3;
  asm("v_pk_mul_f16 %0, %4, %8" CM_MUL "v_pk_mul_f16 %1, %5, %9" CM_MUL "v_pk_mul_f16 %2, %6, %10" CM_MUL "v_pk_mul_f16 %3, %7, %11" CM_MUL
      "v_pk_fma_f16 %0, %4, %8, %0" CM_FMA "v_pk_fma_f16 %1, %5, %9, %1" CM_FMA "v_pk_fma_f16 %2, %6, %10, %2" CM_FMA "v_pk_fma_f16 %3, %7, %11, %3" CM_FMA
      : "=&v"(r0), "=&v"(r1), "=&v"(r2), "=&v"(r3) : "v"(a[0]), "v"(a[1]), "v"(a[2]), "v"(a[3]), "v"(b[0]), "v"(b[1]), "v"(b[2]), "v"(b[3]));
  return u32x4{r0, r1, r2, r3};
}
DI f32x2 cpow(float a2, float br, float p) {
  const float e = __builtin_amdgcn_exp2f(a2 * p), f = __builtin_amdgcn_fractf(br * p);
  return f32x2{__builtin_amdgcn_cosf(f), __builtin_amdgcn_sinf(f)} * e;
}
DI void kgen_block(const u16* __restrict__ lar, const u16* __restrict__ aim, const u16* __restrict__ cre, const u16* __restrict__ cim,
                   const u16* __restrict__ ldt, u32 d, char* ws, char* lds, u32 tid) {
  const u32 lane = tid & 63, w = rfl(tid >> 6), hf = lane >> 5;
  constexpr u32 TS = 144;
  char* tab = lds + 4864 + w * 8192;
  float* k0p = (float*)(lds + 4736);
  const u32 n = d * NM + 32 * w + (lane & 31);
  const u32 i_dt = ldt[d], i_ar = lar[n], i_ai = aim[n], i_cr = cre[n], i_ci = cim[n];
  asm volatile("" ::: "memory");
  const u32x4 pos = *(const u32x4*)(tap_pos + 8 * tid);
  const u32x4 pos2 = *(const u32x4*)(tap_pos + 2048 + 8 * lane);
  const float dt = rb(fexp(bf2f(i_dt)));
  const float a2 = (-rb(fexp(bf2f(i_ar))) * dt) * 1.44269504088896f, br = (bf2f(i_ai) * dt) * (hf ? -0.159154943091895f : 0.159154943091895f);
  const float cr = bf2f(i_cr) * dt, ci = bf2f(i_ci) * dt;
  float mx = fmaxf(fabsf(cr), fabsf(ci)), k0 = cr;
  #define RED(n) "v_max_f32_dpp %0, %0, %0 row_ror:" #n " row_mask:0xf bank_mask:0xf\n\tv_add_f32_dpp %1, %1, %1 row_ror:" #n " row_mask:0xf bank_mask:0xf\n\ts_nop 0\n\t"
  asm("s_nop 1\n\t" RED(1) RED(2) RED(4) RED(8) "s_nop 0" : "+v"(mx), "+v"(k0));
  #undef RED
  const auto sm = __builtin_amdgcn_permlane16_swap(__float_as_uint(mx), __float_as_uint(mx), false, false);
  const auto sk = __builtin_amdgcn_permlane16_swap(__float_as_uint(k0), __float_as_uint(k0), false, false);
  const u32 ex = min(max(max(sm[0], sm[1]) >> 23, 1u), 253u);
  const float sc = __uint_as_float((254u - ex) << 23), isc = __uint_as_float((ex + 1) << 23);
  if (lane == 0) k0p[w] = __uint_as_float(sk[0]) + __uint_as_float(sk[1]);
  const f32x2 step = cpow(a2, br, hf ? 32.f : 1.f), zc = cpow(a2, br, hf ? 512.f : 16.f);
  const f32x2 v0 = hf ? f32x2{cr * sc, -ci * sc} : step;
  f32x2 ve = v0, vo = cmul(v0, step);
  const f32x2 st2 = cmul(step, step);
  char* tw = tab + hf * (16 * TS) + (lane & 31) * 4;
  *(u32*)(tab + 32 * TS + lane * 4) = pk_f16(zc[0], zc[1]);
  #pragma unroll
  for (int r = 0; r < 16; r += 2) {
    *(u32*)(tw + r * TS) = pk_f16(ve[0], ve[1]);
    *(u32*)(tw + (r + 1) * TS) = pk_f16(vo[0], vo[1]);
    if (r < 14) cmul2(ve, vo, st2);
  }
  __builtin_amdgcn_wave_barrier();
  const u32 r = lane & 15, q = lane >> 4;
  f32x4 acc[2][4];
  #pragma unroll
  for (int ks = 0; ks < 2; ++ks) {
    u32x4 A[2], B[4];
    A[0] = *(const u32x4*)(tab + r * TS + ks * 64 + q * 16);
    B[0] = *(const u32x4*)(tab + (16 + r) * TS + ks * 64 + q * 16);
    const u32x4 z16 = *(const u32x4*)(tab + 32 * TS + ks * 64 + q * 16), z512 = *(const u32x4*)(tab + 32 * TS + 128 + ks * 64 + q * 16);
    B[1] = cmul16x4(B[0], z512); A[1] = cmul16x4(A[0], z16); B[2] = cmul16x4(B[1], z512); B[3] = cmul16x4(B[2], z512);
    asm volatile("s_nop 1" : "+v"(A[0]), "+v"(A[1]), "+v"(B[0]), "+v"(B[1]), "+v"(B[2]), "+v"(B[3]));
    #pragma unroll
    for (int ct = 0; ct < 2; ++ct)
      #pragma unroll
      for (int rt = 0; rt < 4; ++rt)
        acc[ct][rt] = __builtin_amdgcn_mfma_f32_16x16x32_f16(__builtin_bit_cast(f16x8, A[ct]), __builtin_bit_cast(f16x8, B[rt]), ks ? acc[ct][rt] : f32x4{0.f, 0.f, 0.f, 0.f}, 0, 0, 0);
  }
  #pragma unroll
  for (int rt = 0; rt < 4; ++rt)
    #pragma unroll
    for (int ct = 0; ct < 2; ++ct) *(f32x4*)(tab + (rt * 2 + ct) * 1024 + lane * 16) = acc[ct][rt] * isc;
  lds_barrier();
  #pragma unroll
  for (int ct = 0; ct < 2; ++ct) {
    const char* src = lds + 4864 + (w * 2 + ct) * 1024 + lane * 16;
    const f32x4 val = (*(const f32x4*)src + *(const f32x4*)(src + 8192)) + (*(const f32x4*)(src + 16384) + *(const f32x4*)(src + 24576));
    *(u32x2*)(lds + 16 + 72 * (16 * w + r) + 32 * ct + 8 * q) = u32x2{pk_bf16(val[0], val[1]), pk_bf16(val[2], val[3])};
  }
  if (tid == 0) *(u32*)(lds + 12) = pk_bf16(0.f, 2.f * ((k0p[0] + k0p[1]) + (k0p[2] + k0p[3])));
  lds_barrier();
  __amdgpu_buffer_rsrc_t rs = rsrc(ws + WS_X + (d / 72) * XS + KT_OFF + (d % 72) * KTS, KTS);
  auto gather = [&](u32x4 ix) {
    u32x4 o;
    #pragma unroll
    for (int j = 0; j < 4; ++j) o[j] = *(const u16*)(lds + (ix[j] & 0xffffu)) | (u32)*(const u16*)(lds + (ix[j] >> 16)) << 16;
    return o;
  };
  st16(rs, tid * 16, gather(pos));
  if (w == 0) st16(rs, 4096 + lane * 16, gather(pos2));
}

DI void pretile_w1(const u16* __restrict__ W, u32 mat, u32 t0, u32 nt, char* ws, char* lds, u32 tid) {
  const u32 lane = tid & 63, w = rfl(tid >> 6);
  __amdgpu_buffer_rsrc_t rw = rsrc(W, F * D * 2);
  const u32 gt = t0 + min(w, nt - 1), slice = gt / 9, tile = gt - slice * 9, lb = rfl((u32)(size_t)lds);
  u32 ord;
  asm volatile("global_load_dword %0, %1, %2" : "=v"(ord) : "v"(min(lane, 17u) * 4), "s"(w1_order + (tile * 2 + mat) * 18) : "memory");
  #pragma unroll
  for (int i = 0; i < 14; ++i) {
    const u32 pc = min(8 * i + w, 107u), e = pc * 64 + lane, r = e / 12, j = e - r * 12;
    dma16(rw, r * (F * 2) + t0 * 32 + j * 16 + (j < 2 * nt ? 0u : OOB), lb + pc * 1024);
  }
  const u32 q = lane >> 4, a = (lane >> 2) & 3, p = lane & 3, dst = lane * 16 + WS_W1 + slice * WSL;
  const char* rd = lds + (8 * q + a) * 192 + min(w, 5u) * 32 + p * 8;
  const u32x4 ones = {0x3f803f80u, 0x3f803f80u, 0x3f803f80u, 0x3f803f80u};
  u32x4 f[18];
  f32x4 cs = {0.f, 0.f, 0.f, 0.f};
  #pragma unroll
  for (int s = 0; s < 18; ++s) {
    if (s == 0) asm volatile("s_waitcnt vmcnt(9)\n\ts_barrier" : "+v"(ord) :: "memory");
    if (s == 6) asm volatile("s_waitcnt vmcnt(5)\n\ts_barrier" ::: "memory");
    if (s == 12) asm volatile("s_waitcnt vmcnt(0)\n\ts_barrier" ::: "memory");
    const u32x2 lo = tr_read(rd + s * 6144), hi = tr_read(rd + s * 6144 + 768);
    f[s] = u32x4{lo[0], lo[1], hi[0], hi[1]};
    cs = mfma_bf16(ones, f[s], cs);
  }
  if (w >= nt) return;
  const float nm0 = cs[0] * (-1.f / D);
  float nm = nm0;
  f32x4 err = {0.f, 0.f, 0.f, 0.f};
  __amdgpu_buffer_rsrc_t rs = rsrc(ws, WS_END);
  #pragma unroll
  for (int s = 0; s < 18; ++s) {
    float u0, t1, t2, t3, t4, t5, t6, t7;
    u32 o0, o1, o2, o3;
    asm("v_dot2_f32_bf16 %4, %12, %16, %18\n\tv_dot2_f32_bf16 %5, %12, %17, %18\n\tv_dot2_f32_bf16 %6, %13, %16, %18\n\tv_dot2_f32_bf16 %7, %13, %17, %18\n\t"
        "v_dot2_f32_bf16 %8, %14, %16, %18\n\tv_dot2_f32_bf16 %9, %14, %17, %18\n\tv_dot2_f32_bf16 %10, %15, %16, %18\n\tv_dot2_f32_bf16 %11, %15, %17, %18\n\t"
        "v_cvt_pk_bf16_f32 %0, %4, %5\n\tv_cvt_pk_bf16_f32 %1, %6, %7\n\tv_cvt_pk_bf16_f32 %2, %8, %9\n\tv_cvt_pk_bf16_f32 %3, %10, %11"
        : "=&v"(o0), "=&v"(o1), "=&v"(o2), "=&v"(o3), "=&v"(u0), "=&v"(t1), "=&v"(t2), "=&v"(t3), "=&v"(t4), "=&v"(t5), "=&v"(t6), "=&v"(t7)
        : "v"(f[s][0]), "v"(f[s][1]), "v"(f[s][2]), "v"(f[s][3]), "s"(0x00003f80u), "s"(0x3f800000u), "v"(nm));
    u32x4 o = {o0, o1, o2, o3};
    asm volatile("s_nop 1" : "+v"(o));
    if (s < 17) {
      err = mfma_bf16(ones ^ 0x80008000u, f[s], err);
      err = mfma_bf16(ones, o, err);
      nm = nm0 * (float)(s + 2) - err[0] * (1.f / 32);
    }
    st16(rs, dst + __builtin_amdgcn_readlane(ord, s) * 1024, o);
  }
}
DI u32 w2_row(u32 task, u32 r) {
  const u32 c = task / 36, sg = task - c * 36, hi = sg >> 5, grp = sg >> 3;
  const u32 sl = hi ? 2 * (sg - 32) : grp >= 2 ? sg & 7 : sg >> 1, off = grp >= 2 ? 32 * grp : 32 * (sg & 1);
  u32 base = 144 * (8 * c + sl) + off, gap = 128 * hi;
  asm volatile("" : "+v"(base), "+v"(gap));
  return base + r + (r >> 4) * gap;
}
DI void pretile_w2_wave(const u16* __restrict__ W, u32 task, u32 w, char* ws, char* lds, u32 lane) {
  const u32 c = task / 36, jp = ((lane >> 1) - c - 1) & 3;
  u32 ord;
  asm volatile("global_load_dword %0, %1, %2" : "=v"(ord) : "v"((lane >= 8 ? 6 : 2 * jp + (jp == 3) + (lane & 1)) * 4), "s"(w2_order + (task - c * 36) * 9) : "memory");
  const u32 r0 = w2_row(task, 0), gap = w2_row(task, 16) - r0 - 16, lb = rfl((u32)(size_t)lds);
  __amdgpu_buffer_rsrc_t rw = rsrc(W, F * D * 2);
  #pragma unroll
  for (int i = 0; i < 9; ++i) {
    const u32 e = i * 64 + lane, r = e / 18, j = e - r * 18;
    dma16(rw, (r0 + r + (r >> 4) * gap) * (D * 2) + w * 288 + j * 16, lb + i * 1024);
  }
  asm volatile("s_waitcnt vmcnt(0)" : "+v"(ord) :: "memory");
  const u32 q = lane >> 4, a = (lane >> 2) & 3, p = lane & 3, dst = lane * 16 + WS_W2 + (w * 4 + c) * WSL;
  __amdgpu_buffer_rsrc_t rs = rsrc(ws, WS_END);
  #pragma unroll
  for (int dt = 0; dt < 9; ++dt) {
    const char* rd = lds + (4 * q + a) * 288 + (dt < 8 ? 64 * (dt >> 1) + 16 * p + 8 * (dt & 1) : 256 + 8 * p);
    const u32x2 lo = tr_read(rd), hi = tr_read(rd + 16 * 288);
    st16(rs, dst + __builtin_amdgcn_readlane(ord, dt) * 1024, u32x4{lo[0], lo[1], hi[0], hi[1]});
  }
}

DI void conv_block(const u16* __restrict__ dskip, const u16* __restrict__ wd, u32 quad, char* ws, char* lds, u32 tid) {
  const u32 lane = tid & 63, w = rfl(tid >> 6), ch = w & 3, lb = rfl((u32)(size_t)lds);
  constexpr u32 LK = 5120, LU = 4 * LK, LY = LU + 18432, RSY = 4160;
  __amdgpu_buffer_rsrc_t rs = rsrc(ws, WS_END);
  const u32 xs = xslot(quad >> 1), ut = xs + (lane & 15) * 128 + (4 * (quad & 1) + (lane >> 4)) * 16;
  #pragma unroll
  for (int k = 0; k < 2; ++k) dma16(rs, ut + (8 * k + w) * 2048, lb + LU + 2048 + (8 * k + w) * 1024);
  const u32 k0 = w < 4 ? 0 : 2048, kp = WS_X + (quad / 18) * XS + KT_OFF + (4 * (quad % 18) + ch) * KTS + k0 + lane * 16;
  #pragma unroll
  for (int k = 0; k < 2; ++k) dma16(rs, kp + k * 1024, lb + ch * LK + k0 + k * 1024);
  const char* xb = ws + xs + XO_OFF + (quad & 1) * 8 + w * 1024;
  u32x2 x0, x1, x2, x3;
  if (w >= 4) {
    dma16(rs, kp + 2048, lb + ch * LK + 4096);
    asm volatile("global_load_dwordx2 %0, %4, %5\n\tglobal_load_dwordx2 %1, %4, %6\n\tglobal_load_dwordx2 %2, %4, %7\n\tglobal_load_dwordx2 %3, %4, %8"
      : "=&v"(x0), "=&v"(x1), "=&v"(x2), "=&v"(x3) : "v"(lane * 16), "s"(xb), "s"(xb + 8192), "s"(xb + 16384), "s"(xb + 24576) : "memory");
  }
  const u32x2 dk = *(const __attribute__((address_space(4))) u32x2*)(dskip + 4 * quad);
  if (tid < 128) *(u32x4*)(lds + LU + tid * 16) = u32x4{0u, 0u, 0u, 0u};
  const u32 cc = lane & 15, q = lane >> 4;
  u32 ka = lb + ch * LK + ((cc & 1) ? 2 * (15 - cc + 8 * q) : 2496 + 2 * (14 - cc + 8 * q));
  u32 kg = lb + ch * LK + 2144 + 60 - 4 * cc + 16 * q;
  const u32 nw = 2 * cc + q + 29;
  u32 uw = lb + LU + (nw >> 4) * 1024 + ch * 256 + (nw & 15) * 16;
  u32x4 bd7, bd9;
  #pragma unroll
  for (int j = 0; j < 4; ++j) {
    const u32 k = 8 * q + 2 * j - cc;
    bd7[j] = (k - 5u < 7u ? 0x3f80u : 0u) | (k - 4u < 7u ? 0x3f800000u : 0u);
    bd9[j] = (k - 4u < 9u ? 0x3f80u : 0u) | (k - 3u < 9u ? 0x3f800000u : 0u);
  }
  u32 ub[4];
  #pragma unroll
  for (int j = 0; j < 4; ++j) { const u32 n = 2 * cc + q + 4 * j + 2; ub[j] = lb + LU + (n >> 4) * 1024 + ch * 256 + (n & 15) * 16; }
  asm volatile("" : "+v"(ka), "+v"(kg), "+v"(uw), "+v"(bd7), "+v"(bd9), "+v"(ub[0]), "+v"(ub[1]), "+v"(ub[2]), "+v"(ub[3]) : "s"(wd));
  if (w < 4) asm volatile("s_waitcnt vmcnt(0) lgkmcnt(0)\n\ts_barrier" ::: "memory");
  else asm volatile("s_waitcnt vmcnt(4) lgkmcnt(0)\n\ts_barrier" ::: "memory");
  if (w < 4) {
    __builtin_amdgcn_s_setprio(2);
    f32x4 y0, y1, y2, y3, y4, y5, y6, y7;
    asm volatile(CONV_ASM_BODY
      : [y0] "=&v"(y0), [y1] "=&v"(y1), [y2] "=&v"(y2), [y3] "=&v"(y3), [y4] "=&v"(y4), [y5] "=&v"(y5), [y6] "=&v"(y6), [y7] "=&v"(y7),
        [x0] "=&v"(x0), [x1] "=&v"(x1), [x2] "=&v"(x2), [x3] "=&v"(x3)
      : [ka0] "v"(ka), [ka1] "v"(ka + 1024), [ka2] "v"(ka + 2048), [kg] "v"(kg), [uw] "v"(uw), [bd7] "v"(bd7), [bd9] "v"(bd9), [ub0] "v"(ub[0]), [ub1] "v"(ub[1]), [ub2] "v"(ub[2]), [ub3] "v"(ub[3]),
        [xo0] "v"(lane * 16), [xb0] "s"(xb), [xb1] "s"(xb + 8192), [xb2] "s"(xb + 16384), [xb3] "s"(xb + 24576)
      : CONV_ASM_CLOBBERS);
    const f32x4 y[8] = {y0, y1, y2, y3, y4, y5, y6, y7};
    #pragma unroll
    for (int i = 0; i < 8; ++i) *(u32x2*)(lds + LY + w * RSY + 512 * i + 32 * cc + 8 * q) = u32x2{pk_bf16(y[i][0], y[i][1]), pk_bf16(y[i][2], y[i][3])};
  } else {
    pretile_w2_wave(wd, quad, ch, ws, lds + 73728 + ch * 9216, lane);
    asm volatile("" : "+v"(x0), "+v"(x1), "+v"(x2), "+v"(x3));
    if (w == 4 && quad < 128) {
      const u32 fill = lane < 32 ? ~0u : 0u;
      st16(rs, lane < 32 ? WS_STAT + (quad * 32 + lane) * 16 : lane < 44 ? WS_FLAG + (quad * 12 + lane - 32) * 16 : OOB, u32x4{fill, fill, fill, fill});
    }
  }
  const u32x2 xx[4] = {x0, x1, x2, x3};
  u32x2 sk[4];
  #pragma unroll
  for (int i = 0; i < 4; ++i) {
    const u32 n = 8 * (8 * i + w) + 2 * q + ((lane >> 1) & 1);
    const u32x2 uu = tr_read(lds + LU + 2048 + (n >> 4) * 1024 + ((lane >> 2) & 3) * 256 + (n & 15) * 16 + (lane & 1) * 8);
    #pragma unroll
    for (int j = 0; j < 2; ++j) sk[i][j] = pk_bf16(fmul(bf2f(uu[j]), bf2f(dk[j])), fmul(bfhi(uu[j]), bfhi(dk[j])));
  }
  lds_barrier();
  #pragma unroll
  for (int i = 0; i < 4; ++i) {
    const u32 t0 = 64 * (8 * i + w);
    const u32x2 yy = tr_read(lds + LY + ((lane >> 2) & 3) * RSY + 2 * (t0 + 16 * q + 4 * (lane & 3)));
    u32x2 h;
    #pragma unroll
    for (int j = 0; j < 2; ++j) {
      const u32 a = pk_bf16(fadd(bf2f(yy[j]), bf2f(sk[i][j])), fadd(bfhi(yy[j]), bfhi(sk[i][j])));
      h[j] = pk_bf16(fadd(bf2f(xx[i][j]), bf2f(a)), fadd(bfhi(xx[i][j]), bfhi(a)));
    }
    __builtin_amdgcn_raw_buffer_store_b64(h, rs, WS_H + (quad >> 1) * 32768 + (t0 + lane) * 16 + (quad & 1) * 8, 0, 0);
  }
}

extern "C" __global__ __launch_bounds__(256, 1) void ffn_kernel(char* ws, u16* out) {
  __shared__ __attribute__((aligned(1024))) char lds[163840];
  const u32 b = blockIdx.x, xcd = b & 7, i = b >> 3, hh = xcd & 1, c = xcd >> 1;
  const u32 w = rfl(threadIdx.x >> 6);
  const u32 panel = 4 * hh + (i & 3), jj = i >> 2, slice = 8 * c + jj;
  const u32 mt = 2 * panel + (jj & 1), nt = i >> 3;
  const u32 R1 = 4 * panel + w, R2 = 4 * mt + w;
  const u32 ldsbase = rfl((u32)(size_t)lds);
  const u32 p1 = WS_P + (2 * R1 * 4 + c) * 73728;
  const u32 w1 = WS_W1 + slice * WSL + w * 4096, w2 = WS_W2 + (nt * 4 + c) * WSL + w * 4096;
  const u32 ex = WS_X + hh * XS + i * EXB;
  const u32 isred = ((w ^ c) - 1u) >> 31;
  asm volatile(FFN_ASM_BODY "ffn_code_end:"
    :
    : [ws] "s"(ws), [out] "s"(out), [ldsbase] "s"(ldsbase), [m0base] "s"(ldsbase + w * 4096),
      [w1off] "s"(w1), [w2off] "s"(w2), [wmix] "s"(w == 0 ? w1 + 20 * 16384 : w2 - 4096),
      [xoff] "s"(WS_H + R1 * 1024),
      [pstA] "s"(p1 + jj * 4096), [pstB] "s"(p1 + jj * 4096 + 2048),
      [pst1] "s"(p1 + (16 + jj) * 2048), [pst2] "s"(p1 + (24 + jj) * 2048), [pst3] "s"(p1 + 32 * 2048 + jj * 1024),
      [pld] "s"(WS_P + (R2 * 4 + c) * 73728),
      [flagout] "s"(WS_FLAG + (R1 * 4 + c) * 128), [flagin] "s"(WS_FLAG + ((R2 >> 1) * 4 + c) * 128),
      [stin] "s"(WS_H + 18 * w * 32768 + (256 * panel + 8 * slice) * 16), [stout] "s"(WS_STAT + (256 * panel + 8 * slice) * 32 + w * 8),
      [stld] "s"(WS_STAT + R2 * 1024),
      [hown] "s"(WS_H + (18 * nt + 4 * c) * 32768 + R2 * 512), [h8] "s"(WS_H + (18 * nt + 16) * 32768 + R2 * 512),
      [exo0] "s"(ex + ((c + 1) & 3) * EXC + (w * 3 + 0) * 2048), [exo1] "s"(ex + ((c + 2) & 3) * EXC + (w * 3 + 1) * 2048),
      [exo2] "s"(ex + ((c + 3) & 3) * EXC + (w * 3 + 2) * 2048), [exo8] "s"(ex + w * EXC + 24576 + ((w - 1 - c) & 3) * 1024),
      [exin] "s"(ex + c * EXC + w * 6144), [exin8] "s"(ex + c * EXC + 24576),
      [clr] "s"(ex + c * EXC + w * 7168), [cflag] "s"(WS_FLAG2 + (hh * 32 + i) * 128),
      [oown] "s"(R2 * 32 * 1152 + 288 * nt + 64 * c), [o8] "s"(R2 * 32 * 1152 + 288 * nt + 256), [isred] "s"(isred)
    : FFN_ASM_CLOBBERS);
}

extern "C" __global__ __launch_bounds__(256, 4) void k1_kernel(const u16* __restrict__ x, const u16* __restrict__ lar, const u16* __restrict__ aim,
    const u16* __restrict__ cre, const u16* __restrict__ cim, const u16* __restrict__ ldt,
    const u16* __restrict__ wg, const u16* __restrict__ wu, const u16* __restrict__ wd, char* ws) {
  __shared__ __attribute__((aligned(16))) char lds[37888 + 256];
  char* dump = lds + 37888;
  const u32 b = blockIdx.x, tid = threadIdx.x;
  {
    u32 off, km;
    unsigned long long pc;
    asm volatile("s_cmp_lt_u32 %5, 16\n\ts_cbranch_scc0 Ls_%=\n\ts_getpc_b64 %1\n\tLe_%=:\n\tv_lshlrev_b32 %0, 7, %3\n\tv_min_u32 %0, k2_code_end-Le_%=-4, %0\n\t"
                 "s_mov_b32 %2, m0\n\ts_mov_b32 m0, %4\n\ts_nop 0\n\tglobal_load_lds_dword %0, %1\n\ts_mov_b32 m0, %2\n\tLs_%=:"
                 : "=&v"(off), "=&s"(pc), "=&s"(km) : "v"(tid), "s"(rfl((u32)(size_t)dump)), "s"(b - 384) : "scc");
  }
  const u32 rnd = b >> 8, p = b & 255;
  if (__builtin_expect(rnd == 0 ? p < 64 : rnd < 3, 1)) {
    kgen_block(lar, aim, cre, cim, ldt, rnd == 0 ? p : b - 192, ws, lds, tid);
  } else if (rnd == 0 || (p >> 6) == 1) {
    __builtin_amdgcn_s_setprio(2);
    ln1_block(x, rnd == 0 ? p - 64 : p + 128, ws, lds, tid);
  } else {
    const u32 xcd = b & 7, e0 = ((p < 64 ? p : p - 64) >> 3) * 256 + tid;
    #pragma unroll
    for (int i = 0; i < 2; ++i) {
      const u32 e = e0 + 6144 * i, r2 = e / 9, ln = e - r2 * 9;
      if (rfl(e) < 18 * 576) touch_lines((const char*)(r2 & 1 ? wu : wg) + (size_t)(r2 >> 1) * (F * 2) + (9 * xcd + ln) * 128, 128, 0, dump);
    }
    const u32 jj = e0 / 288, ln = e0 - jj * 288;
    if (rfl(e0) < 18 * 288) {
      touch_lines((const char*)wd + w2_row(18 * xcd + jj, ln / 9) * (D * 2) + (ln % 9) * 128, 128, 0, dump);
    } else if (rfl(e0) >= 22 * 256) {
      const char *c0, *c1;
      CODE_ADDR(c0, "ffn_kernel"); CODE_ADDR(c1, "ffn_code_end");
      touch_lines(c0, (u32)(c1 - c0), e0 - 22 * 256, dump);
    } else if (rfl(e0) >= 21 * 256) {
      touch_lines(rfl(tid) < 128 ? (const char*)w1_order : (const char*)w2_order, sizeof(w1_order), tid & 63, dump);
    }
  }
}

extern "C" __global__ __launch_bounds__(512, 2) void k2_kernel(const u16* __restrict__ dskip, const u16* __restrict__ wg, const u16* __restrict__ wu,
    const u16* __restrict__ wd, char* ws) {
  __shared__ __attribute__((aligned(1024))) char lds[110592 + 256];
  const u32 b = blockIdx.x, tid = threadIdx.x, xcd = b & 7, slot = b >> 3;
  if (slot < 18) {
    conv_block(dskip, wd, 18 * xcd + slot, ws, lds, tid);
  } else {
    const u32 i = slot - 18, mat = i / 7, k = i - 7 * mat;
    pretile_w1(mat ? wu : wg, mat, 36 * xcd + 5 * k, k < 6 ? 5 : 6, ws, lds, tid);
    if (i < 4 && rfl(tid) >= 384) {
      const char *c0, *c1;
      CODE_ADDR(c0, "ffn_kernel"); CODE_ADDR(c1, "ffn_code_end");
      touch_lines(c0, (u32)(c1 - c0), tid - 384 + i * 128, lds + 110592);
    }
  }
  asm volatile("k2_code_end:");
}

extern "C" void kernel_launch(void* const* d_in, const int* in_sizes, int n_in, void* d_out, int out_size, void* d_ws, size_t ws_size,
                              hipStream_t stream) {
  const u16* const* in = (const u16* const*)d_in;
  char* ws = (char*)d_ws;
  k1_kernel<<<1024, 256, 0, stream>>>(in[0], in[1], in[2], in[3], in[4], in[5], in[8], in[9], in[10], ws);
  k2_kernel<<<256, 512, 0, stream>>>(in[6], in[8], in[9], in[10], ws);
  ffn_kernel<<<256, 256, 0, stream>>>(ws, (u16*)d_out);
}
```
